```python
import torch

def _softmax(x, axis=-1):
    return torch.softmax(x, dim=axis)

def _gelu(x):
    return torch.nn.functional.gelu(x, approximate='tanh')

def _silu(x):
    return torch.nn.functional.silu(x)

def _sigmoid(x):
    return torch.sigmoid(x)

def _layernorm(x, axis=-1, eps=1e-05):
    m = x.mean(dim=axis, keepdim=True)
    v = x.var(dim=axis, keepdim=True, unbiased=False)
    return (x - m) / torch.sqrt(v + eps)

def _scatter_add(tgt, idx, src, axis=0):
    return tgt.clone().index_add_(axis, idx, src)

def _segment_sum(src, seg, n):
    out = torch.zeros((n,) + src.shape[1:], dtype=src.dtype)
    return out.index_add_(0, seg, src)

def _pad(x, pw):
    flat = tuple((v for pair in reversed(pw) for v in pair))
    return torch.nn.functional.pad(x, flat)
kernel_name = 'adaLN_cond__sliding_block_cached__swiglu_ffn__prompt'

def setup_inputs():

    _w = lambda a: (lambda t: t.to(torch.bfloat16) if t.is_floating_point() else t)(torch.as_tensor(a))

    def _A_setup():
        B, Sq, D = (1, 2048, 640)
        Hq, Hkv, Sk, Dh = (32, 32, 2048, 128)
        assert Hq % Hkv == 0, (Hq, Hkv)
        return {'x': _w((lambda *s: torch.randn(*s, dtype=torch.bfloat16) * 0.05)(B, Sq, D)), 'k_cache': _w((lambda *s: torch.randn(*s, dtype=torch.bfloat16) * 3.2)(B, Hkv, Sk, Dh)), 'v_cache': _w((lambda *s: torch.randn(*s, dtype=torch.bfloat16) * 0.4)(B, Hkv, Sk, Dh)), 'wq': _w((lambda *s: torch.randn(*s, dtype=torch.bfloat16) * 3.2)(D, Hq * Dh) * D ** (-0.5)), 'wo': _w((lambda *s: torch.randn(*s, dtype=torch.bfloat16) * 0.4)(Hq * Dh, D) * (Hq * Dh) ** (-0.5))}
    _A_in = _A_setup()

    def _B_setup():
        B, S, D, F = (1, 2048, 640, 1728)
        return {'x': _w((lambda *s: torch.randn(*s, dtype=torch.bfloat16) * 0.05)(B, S, D)), 'w_gate': _w((lambda *s: torch.randn(*s, dtype=torch.bfloat16) * 0.05)(D, F)), 'w_up': _w((lambda *s: torch.randn(*s, dtype=torch.bfloat16) * 0.05)(D, F)), 'w_down': _w((lambda *s: torch.randn(*s, dtype=torch.bfloat16) * 0.05)(F, D))}
    _B_in = _B_setup()
    return {'A_x': _A_in['x'], 'A_k_cache': _A_in['k_cache'], 'A_v_cache': _A_in['v_cache'], 'A_wq': _A_in['wq'], 'A_wo': _A_in['wo'], 'B_x': _B_in['x'], 'B_w_gate': _B_in['w_gate'], 'B_w_up': _B_in['w_up'], 'B_w_down': _B_in['w_down'], 'c': _w((lambda *s: torch.randn(*s, dtype=torch.bfloat16) * 0.1)(1, 640) * 8.0), 'w_mod': _w((lambda *s: torch.randn(*s, dtype=torch.bfloat16) * 0.1)(640, 6 * 640) * 640 ** (-0.5) * 8.0)}

def reference(A_x, A_k_cache, A_v_cache, A_wq, A_wo, B_x, B_w_gate, B_w_up, B_w_down, c, w_mod):

    def _A_ref(x, k_cache, v_cache, wq, wo):
        B, Sq, D = x.shape
        Hq, Dh = (32, 128)
        Hkv, Sk = (k_cache.shape[1], k_cache.shape[2])
        W = min(4096, max(256, Sk // 4))
        rep = Hq // Hkv
        q = (lambda x, *d: x.permute(*d))((x @ wq).reshape(B, Sq, Hq, Dh), 0, 2, 1, 3)
        k = (lambda x, n, axis: torch.repeat_interleave(x, n, dim=axis))(k_cache[:, :, Sk - W:, :], rep, axis=1)
        v = (lambda x, n, axis: torch.repeat_interleave(x, n, dim=axis))(v_cache[:, :, Sk - W:, :], rep, axis=1)
        scores = torch.einsum('bhqd,bhkd->bhqk', q, k) * Dh ** (-0.5)
        p = _softmax(scores, axis=-1)
        o = torch.einsum('bhqk,bhkd->bhqd', p, v)
        return (lambda x, *d: x.permute(*d))(o, 0, 2, 1, 3).reshape(B, Sq, Hq * Dh) @ wo

    def _B_ref(x, w_gate, w_up, w_down):
        g = _silu(x @ w_gate)
        u = x @ w_up
        return g * u @ w_down
    x = A_x
    mod = (_silu(c) @ w_mod).reshape(c.shape[0], 6, 1, -1)
    sa, ca, ga, sb, cb, gb = (mod[:, 0].clone(), mod[:, 1].clone(), mod[:, 2].clone(), mod[:, 3].clone(), mod[:, 4].clone(), mod[:, 5].clone())
    h = _layernorm(x, axis=-1) * (1 + ca) + sa
    x = x + ga * _A_ref(h, A_k_cache, A_v_cache, A_wq, A_wo)
    h = _layernorm(x, axis=-1) * (1 + cb) + sb
    x = x + gb * _B_ref(h, B_w_gate, B_w_up, B_w_down)
    return x
```

```cpp
#include <hip/hip_runtime.h>
#include <hip/hip_bf16.h>
typedef unsigned short u16;
__device__ __forceinline__ float bf2f(u16 v){ return __uint_as_float(((unsigned)v)<<16); }
__device__ __forceinline__ u16 f2bf(float f){ __hip_bfloat16 b = __float2bfloat16(f); return *(u16*)&b; }
constexpr int S=2048,D=640,H=32,DH=128,SK=2048,W=512,F=1728;

__global__ void k_mod(const u16* c,const u16* wm,float* mod){
  int n=blockIdx.x*256+threadIdx.x; if(n>=6*D) return;
  float acc=0; for(int k=0;k<D;k++){ float x=bf2f(c[k]); float s=bf2f(f2bf(x/(1.f+__expf(-x)))); acc+=s*bf2f(wm[k*6*D+n]); }
  mod[n]=bf2f(f2bf(acc));
}
__global__ void k_ln(const u16* x,const float* shift,const float* scale,u16* h){
  __shared__ float red[2][4];
  int r=blockIdx.x; int t=threadIdx.x; float v[3]; float s=0;
  for(int i=0;i<3;i++){int c=t+i*256; v[i]= c<D? bf2f(x[r*D+c]):0.f; s+=v[i];}
  for(int o=32;o;o>>=1) s+=__shfl_xor(s,o);
  if((t&63)==0) red[0][t>>6]=s; __syncthreads();
  float m=(red[0][0]+red[0][1]+red[0][2]+red[0][3])/D; float q=0;
  for(int i=0;i<3;i++){int c=t+i*256; if(c<D){float d=v[i]-m; q+=d*d;}}
  for(int o=32;o;o>>=1) q+=__shfl_xor(q,o);
  if((t&63)==0) red[1][t>>6]=q; __syncthreads();
  float rs=rsqrtf((red[1][0]+red[1][1]+red[1][2]+red[1][3])/D+1e-5f);
  for(int i=0;i<3;i++){int c=t+i*256; if(c<D) h[r*D+c]=f2bf((v[i]-m)*rs*bf2f(f2bf(1.f+scale[c]))+shift[c]);}
}
template<int MODE> __global__ void k_gemm(const u16* A,const u16* B,const u16* B2,u16* C,int M,int N,int K,const u16* res,const float* gate){
  __shared__ float As[16][17],Bs[16][17],Bs2[16][17];
  int tx=threadIdx.x&15,ty=threadIdx.x>>4; int row=blockIdx.y*16+ty,col=blockIdx.x*16+tx; float acc=0,acc2=0;
  for(int k0=0;k0<K;k0+=16){
    As[ty][tx]=bf2f(A[row*K+k0+tx]); Bs[ty][tx]=bf2f(B[(k0+ty)*N+col]); if(MODE==2) Bs2[ty][tx]=bf2f(B2[(k0+ty)*N+col]);
    __syncthreads();
    for(int k=0;k<16;k++){acc+=As[ty][k]*Bs[k][tx]; if(MODE==2) acc2+=As[ty][k]*Bs2[k][tx];}
    __syncthreads();
  }
  if(MODE==0) C[row*N+col]=f2bf(acc);
  if(MODE==1) C[row*N+col]=f2bf(bf2f(res[row*N+col])+gate[col]*acc);
  if(MODE==2) C[row*N+col]=f2bf(acc/(1.f+__expf(-acc))*acc2);
}
__global__ void k_attn(const u16* q,const u16* k,const u16* v,u16* o){
  __shared__ float qs[DH],p[W],red[2];
  int r=blockIdx.x,h=blockIdx.y,t=threadIdx.x;
  qs[t]=bf2f(q[r*H*DH+h*DH+t]); __syncthreads();
  const u16* kb=k+((size_t)h*SK+(SK-W))*DH; const u16* vb=v+((size_t)h*SK+(SK-W))*DH;
  float mx=-1e30f;
  for(int j=t;j<W;j+=128){float s=0; for(int d=0;d<DH;d++) s+=qs[d]*bf2f(kb[j*DH+d]); s*=0.08838834764831845f; p[j]=s; mx=fmaxf(mx,s);}
  for(int o_=32;o_;o_>>=1) mx=fmaxf(mx,__shfl_xor(mx,o_));
  if((t&63)==0) red[t>>6]=mx; __syncthreads(); mx=fmaxf(red[0],red[1]); __syncthreads();
  float sm=0; for(int j=t;j<W;j+=128){float e=__expf(p[j]-mx); p[j]=e; sm+=e;}
  for(int o_=32;o_;o_>>=1) sm+=__shfl_xor(sm,o_);
  if((t&63)==0) red[t>>6]=sm; __syncthreads(); sm=red[0]+red[1];
  float acc=0; for(int j=0;j<W;j++) acc+=bf2f(f2bf(p[j]))*bf2f(vb[j*DH+t]);
  o[r*H*DH+h*DH+t]=f2bf(acc/sm);
}
extern "C" void kernel_launch(void* const* d_in,const int* in_sizes,int n_in,void* d_out,int out_size,void* d_ws,size_t ws_size,hipStream_t stream){
  const u16* x=(const u16*)d_in[0]; const u16* kc=(const u16*)d_in[1]; const u16* vc=(const u16*)d_in[2];
  const u16* wq=(const u16*)d_in[3]; const u16* wo=(const u16*)d_in[4];
  const u16* wg=(const u16*)d_in[6]; const u16* wu=(const u16*)d_in[7]; const u16* wd=(const u16*)d_in[8];
  const u16* c=(const u16*)d_in[9]; const u16* wm=(const u16*)d_in[10];
  char* ws=(char*)d_ws; float* mod=(float*)ws; u16* h=(u16*)(ws+65536); u16* q=h+S*D; u16* o=q+S*H*DH; u16* x1=o+S*H*DH; u16* h2=x1+S*D; u16* act=h2+S*D;
  k_mod<<<15,256,0,stream>>>(c,wm,mod);
  k_ln<<<S,256,0,stream>>>(x,mod,mod+D,h);
  k_gemm<0><<<dim3(H*DH/16,S/16),256,0,stream>>>(h,wq,nullptr,q,S,H*DH,D,nullptr,nullptr);
  k_attn<<<dim3(S,H),128,0,stream>>>(q,kc,vc,o);
  k_gemm<1><<<dim3(D/16,S/16),256,0,stream>>>(o,wo,nullptr,x1,S,D,H*DH,x,mod+2*D);
  k_ln<<<S,256,0,stream>>>(x1,mod+3*D,mod+4*D,h2);
  k_gemm<2><<<dim3(F/16,S/16),256,0,stream>>>(h2,wg,wu,act,S,F,D,nullptr,nullptr);
  k_gemm<1><<<dim3(D/16,S/16),256,0,stream>>>(act,wd,nullptr,(u16*)d_out,S,D,F,x1,mod+5*D);
}
```

```cpp
#include <hip/hip_runtime.h>
#include <stdint.h>
#include <type_traits>
typedef unsigned short u16;
typedef unsigned int u32;
typedef unsigned long long u64;
typedef float f32x4 __attribute__((ext_vector_type(4)));
typedef float f32x2 __attribute__((ext_vector_type(2)));
typedef __bf16 bf16x8 __attribute__((ext_vector_type(8)));
typedef __bf16 bf16x2 __attribute__((ext_vector_type(2)));
typedef u32 u32x4 __attribute__((ext_vector_type(4)));
typedef u32 u32x2 __attribute__((ext_vector_type(2)));
#define DI __device__ __forceinline__
#define GAS __attribute__((address_space(1)))
#define LAS __attribute__((address_space(3)))
#define RLX_AGENT __ATOMIC_RELAXED, __HIP_MEMORY_SCOPE_AGENT

constexpr int SEQ = 2048, DM = 640, NH = 32, DH = 128, SK = 2048, WIN = 512, FF = 1728;
constexpr float QSCALE = 0.12751743082459868f;
constexpr int WS_EPOCH = 0, WS_TMO = 256;
constexpr int WS_FR = 20480;
constexpr int WS_GR = 49152;
constexpr int WS_MODF = WS_GR + 65536;
constexpr int WS_ST = WS_GR + 131072;
constexpr int WS_H = WS_ST + 131072;
constexpr int WS_Y = WS_H + SEQ * DM * 2;
constexpr int WS_OF = WS_Y + SEQ * DM * 2;
constexpr int WS_ACT = WS_OF + SEQ * NH * DH * 2;
constexpr int WS_WOT = WS_ACT + SEQ * FF * 2;
constexpr int WS_WDT = WS_WOT + NH * DH * DM * 2;
constexpr int WS_PP = WS_WDT + FF * DM * 2;
constexpr int WS_MU = WS_PP + SEQ * DM * 4;
constexpr int WS_UV = WS_MU + 32768;
constexpr int WS_FH = WS_UV + 32768;
constexpr int WS_END = WS_UV + 8 * 40960;
constexpr int L_END = 163840, L_TS = 114688, L_SC = 119808, L_GV = 122880, L_XR = 90112;

DI float bf_lo(u32 w) { return __uint_as_float(w << 16); }
DI float bf_hi(u32 w) { return __uint_as_float(w & 0xffff0000u); }
DI u32 pk_bf16(float lo, float hi) { f32x2 v = {lo, hi}; bf16x2 b = __builtin_convertvector(v, bf16x2); return __builtin_bit_cast(u32, b); }
DI float rbf(float x) { return bf_lo(pk_bf16(x, 0.f)); }
DI float silu_f(float x) { return x * __builtin_amdgcn_rcpf(1.f + __builtin_amdgcn_exp2f(-1.4426950408889634f * x)); }
DI f32x4 mfma16(u32x4 a, u32x4 b, f32x4 c) { return __builtin_amdgcn_mfma_f32_16x16x32_bf16(__builtin_bit_cast(bf16x8, a), __builtin_bit_cast(bf16x8, b), c, 0, 0, 0); }
DI int lane_id() { int l; asm volatile("v_mbcnt_lo_u32_b32 %0, -1, 0\n\tv_mbcnt_hi_u32_b32 %0, -1, %0" : "=v"(l)); return l; }
DI float sum8(float v) { return v + __uint_as_float(__builtin_amdgcn_update_dpp(0u, __float_as_uint(v), 0x128, 0xf, 0xf, false)); }
DI float sum16(float v) { auto r = __builtin_amdgcn_permlane16_swap(__float_as_uint(v), __float_as_uint(v), false, false); return __uint_as_float(r[0]) + __uint_as_float(r[1]); }
DI float sum32(float v) { auto r = __builtin_amdgcn_permlane32_swap(__float_as_uint(v), __float_as_uint(v), false, false); return __uint_as_float(r[0]) + __uint_as_float(r[1]); }
DI float max16(float v) { auto r = __builtin_amdgcn_permlane16_swap(__float_as_uint(v), __float_as_uint(v), false, false); return fmaxf(__uint_as_float(r[0]), __uint_as_float(r[1])); }
DI float max32(float v) { auto r = __builtin_amdgcn_permlane32_swap(__float_as_uint(v), __float_as_uint(v), false, false); return fmaxf(__uint_as_float(r[0]), __uint_as_float(r[1])); }
DI u32x4 pack8(f32x4 a, f32x4 b) { return u32x4{pk_bf16(a[0], a[1]), pk_bf16(a[2], a[3]), pk_bf16(b[0], b[1]), pk_bf16(b[2], b[3])}; }
DI void drain() { asm volatile("s_waitcnt vmcnt(0)" ::: "memory"); }
DI void lds_barrier() { asm volatile("s_waitcnt lgkmcnt(0)\n\ts_barrier" ::: "memory"); }
typedef __amdgpu_buffer_rsrc_t rsrc_t;
DI void st16(rsrc_t r, int off, u32x4 v) { __builtin_amdgcn_raw_buffer_store_b128(v, r, off, 0, 16); }
DI void st4(rsrc_t r, int off, u32 v) { __builtin_amdgcn_raw_buffer_store_b32(v, r, off, 0, 16); }
DI u32x4 ld16(rsrc_t r, int off) { return __builtin_amdgcn_raw_buffer_load_b128(r, off, 0, 16); }
constexpr u32 SPINCAP = 30000;
DI void touch(const void* p) { asm volatile("s_mov_b32 m0, 162816\n\ts_nop 0\n\tglobal_load_lds_dword %0, off" :: "v"(p) : "memory"); }
DI void glds16(const void* p, int dst) { asm volatile("s_mov_b32 m0, %1\n\ts_nop 0\n\tglobal_load_lds_dwordx4 %0, off nt" :: "v"(p), "s"(dst) : "memory"); }
DI u32x4 wgather(const u16* W, int ld, int k0, int g, int n) {
  u32x4 r;
#pragma unroll
  for (int j = 0; j < 4; ++j) { const int k = k0 + 16 * (j >> 1) + 4 * g + 2 * (j & 1); r[j] = (u32)W[(size_t)k * ld + n] | ((u32)W[(size_t)(k + 1) * ld + n] << 16); }
  return r;
}
DI void attn_safe(const u16* __restrict__ Kh, const u16* __restrict__ Vh, rsrc_t rw, int of, int lane) {
  const int r = lane & 15, g = lane >> 4, pg = ((g & 1) << 1) | (g >> 1);
#pragma unroll 1
  for (int qt = 0; qt < 4; ++qt) {
    const int ofq = of + qt * 1024 + lane * 16;
    u32x4 qf[4];
#pragma unroll
    for (int ds = 0; ds < 4; ++ds) qf[ds] = ld16(rw, ofq + ds * 4096);
    float m = -INFINITY, l = 0.f; f32x4 o[8] = {};
#pragma unroll 1
    for (int u = 0; u < 16; ++u) {
      f32x4 s[2] = {};
#pragma unroll
      for (int kt = 0; kt < 2; ++kt)
#pragma unroll
        for (int ds = 0; ds < 4; ++ds) s[kt] = mfma16(*(const u32x4*)(Kh + (32 * u + 16 * kt + r) * DH + 32 * ds + 8 * pg), qf[ds], s[kt]);
      float mx = fmaxf(fmaxf(fmaxf(s[0][0], s[0][1]), fmaxf(s[0][2], s[0][3])), fmaxf(fmaxf(s[1][0], s[1][1]), fmaxf(s[1][2], s[1][3])));
      mx = max32(max16(mx));
      const float mn = fmaxf(m, mx), al = __builtin_amdgcn_exp2f(m - mn); m = mn;
      float ps = 0.f;
#pragma unroll
      for (int kt = 0; kt < 2; ++kt)
#pragma unroll
        for (int i = 0; i < 4; ++i) { s[kt][i] = __builtin_amdgcn_exp2f(s[kt][i] - mn); ps += s[kt][i]; }
      l = l * al + ps;
      const u32x4 p = pack8(s[0], s[1]);
#pragma unroll
      for (int dt = 0; dt < 8; ++dt) o[dt] = mfma16(wgather(Vh, DH, 32 * u, g, 16 * dt + r), p, o[dt] * al);
    }
    l = sum32(sum16(l));
    const float il = 1.f / l;
#pragma unroll
    for (int i = 0; i < 4; ++i) st16(rw, ofq + i * 4096, pack8(o[2 * i] * il, o[2 * i + 1] * il));
  }
}
#define EARLY_ASM \
  "v_mbcnt_lo_u32_b32 v203, -1, 0\n\t" \
  "v_mbcnt_hi_u32_b32 v203, -1, v203\n\t" \
  "s_mul_i32 s36, %[head], 256\n\t" \
  "s_mov_b64 s[72:73], %[wq]\n\t" \
  "s_add_u32 s36, s36, s72\n\t" \
  "s_addc_u32 s37, s73, 0\n\t" \
  "s_sub_u32 s36, s36, 4096\n\t" \
  "s_subb_u32 s37, s37, 0\n\t" \
  "s_and_b32 s37, s37, 0xffff\n\t" \
  "s_mov_b32 s38, 0x501000\n\t" \
  "s_mov_b32 s39, 0x20000\n\t" \
  "v_bfe_u32 v232, v203, 2, 3\n\t" \
  "s_mul_i32 s84, %[w], 16\n\t" \
  "v_add_u32 v232, s84, v232\n\t" \
  "v_mul_u32_u24 v234, 8192, v232\n\t" \
  "v_and_b32 v232, 3, v203\n\t" \
  "v_lshl_add_u32 v234, v232, 4, v234\n\t" \
  "v_lshrrev_b32 v232, 5, v203\n\t" \
  "v_lshl_add_u32 v234, v232, 6, v234\n\t" \
  "v_xor_b32 v232, 32, v234\n\t" \
  "v_add_u32 v204, 0x1000, v234\n\t" \
  "v_add_u32 v205, 0xc80, v234\n\t" \
  "v_add_u32 v206, 0x10800, v232\n\t" \
  "v_add_u32 v207, 0x10480, v232\n\t" \
  "s_lshl_b32 s79, %[w], 12\n\t" \
  "s_add_u32 m0, s79, 0\n\t" \
  "s_mov_b32 s74, 0\n\t" \
  "buffer_load_dwordx4 v204, s[36:39], s74 offen offset:0 lds\n\t" \
  "buffer_load_dwordx4 v205, s[36:39], s74 offen offset:1024 lds\n\t" \
  "buffer_load_dwordx4 v206, s[36:39], s74 offen offset:2048 lds\n\t" \
  "buffer_load_dwordx4 v207, s[36:39], s74 offen offset:3072 lds\n\t" \
  "s_add_u32 m0, s79, 4096\n\t" \
  "s_mov_b32 s74, 131072\n\t" \
  "buffer_load_dwordx4 v204, s[36:39], s74 offen offset:0 lds\n\t" \
  "buffer_load_dwordx4 v205, s[36:39], s74 offen offset:1024 lds\n\t" \
  "buffer_load_dwordx4 v206, s[36:39], s74 offen offset:2048 lds\n\t" \
  "buffer_load_dwordx4 v207, s[36:39], s74 offen offset:3072 lds\n\t"
#define EARLY_CLOB "v200", "v201", "v202", "v203", "v204", "v205", "v206", "v207", "v208", "v209", "v210", "v211", "v212", "v213", "v214", "v215", "v216", "v217", "v218", "v219", "v220", "v221", "v222", "v223", "v224", "v225", "v226", "v227", "v228", "v229", "v230", "v231", "v232", "v233", "v234", "v235", "v236", "v237", "v238", "v239", "v240", "v241", "v242", "v243", "v244", "v245", "v246", "v247", "vcc", "scc", "memory", "s36", "s37", "s38", "s39", "s72", "s73", "s74", "s79", "s84"
#define MAIN_ASM \
  "v_mbcnt_lo_u32_b32 v203, -1, 0\n\t" \
  "v_mbcnt_hi_u32_b32 v203, -1, v203\n\t" \
  "v_lshlrev_b32 v202, 4, v203\n\t" \
  "s_mov_b32 s70, 256\n\t" \
  "s_mov_b64 s[72:73], %[ws]\n\t" \
  "s_add_u32 s70, s70, s72\n\t" \
  "s_addc_u32 s71, s73, 0\n\t" \
  "s_cmp_lg_u32 %[entry], 0\n\t" \
  "s_cbranch_scc1 .Lbcold%=\n\t" \
  "s_mul_i32 s40, %[head], 256\n\t" \
  "s_mov_b64 s[72:73], %[wq]\n\t" \
  "s_add_u32 s40, s40, s72\n\t" \
  "s_addc_u32 s41, s73, 0\n\t" \
  "s_sub_u32 s40, s40, 4096\n\t" \
  "s_subb_u32 s41, s41, 0\n\t" \
  "s_and_b32 s41, s41, 0xffff\n\t" \
  "s_mov_b32 s42, 0x501000\n\t" \
  "s_mov_b32 s43, 0x20000\n\t" \
  "v_bfe_u32 v232, v203, 2, 3\n\t" \
  "s_mul_i32 s84, %[w], 16\n\t" \
  "v_add_u32 v232, s84, v232\n\t" \
  "v_mul_u32_u24 v234, 8192, v232\n\t" \
  "v_and_b32 v232, 3, v203\n\t" \
  "v_lshl_add_u32 v234, v232, 4, v234\n\t" \
  "v_lshrrev_b32 v232, 5, v203\n\t" \
  "v_lshl_add_u32 v234, v232, 6, v234\n\t" \
  "v_xor_b32 v232, 32, v234\n\t" \
  "v_add_u32 v208, 0x1000, v234\n\t" \
  "v_add_u32 v209, 0xc80, v234\n\t" \
  "v_add_u32 v210, 0x10800, v232\n\t" \
  "v_add_u32 v211, 0x10480, v232\n\t" \
  "s_lshl_b32 s80, %[w], 12\n\t" \
  "s_add_u32 s79, s80, 32768\n\t" \
  "s_add_u32 m0, s80, 16384\n\t" \
  "s_mov_b32 s75, 524288\n\t" \
  "buffer_load_dwordx4 v208, s[40:43], s75 offen offset:0 lds\n\t" \
  "buffer_load_dwordx4 v209, s[40:43], s75 offen offset:1024 lds\n\t" \
  "buffer_load_dwordx4 v210, s[40:43], s75 offen offset:2048 lds\n\t" \
  "buffer_load_dwordx4 v211, s[40:43], s75 offen offset:3072 lds\n\t" \
  "s_add_u32 m0, s80, 32768\n\t" \
  "s_mov_b32 s75, 1048576\n\t" \
  "buffer_load_dwordx4 v208, s[40:43], s75 offen offset:0 lds\n\t" \
  "buffer_load_dwordx4 v209, s[40:43], s75 offen offset:1024 lds\n\t" \
  "buffer_load_dwordx4 v210, s[40:43], s75 offen offset:2048 lds\n\t" \
  "buffer_load_dwordx4 v211, s[40:43], s75 offen offset:3072 lds\n\t" \
  "s_add_u32 m0, s80, 49152\n\t" \
  "s_mov_b32 s75, 1572864\n\t" \
  "buffer_load_dwordx4 v208, s[40:43], s75 offen offset:0 lds\n\t" \
  "buffer_load_dwordx4 v209, s[40:43], s75 offen offset:1024 lds\n\t" \
  "buffer_load_dwordx4 v210, s[40:43], s75 offen offset:2048 lds\n\t" \
  "buffer_load_dwordx4 v211, s[40:43], s75 offen offset:3072 lds\n\t" \
  "s_add_u32 m0, s80, 65536\n\t" \
  "s_mov_b32 s75, 2097152\n\t" \
  "buffer_load_dwordx4 v208, s[40:43], s75 offen offset:0 lds\n\t" \
  "buffer_load_dwordx4 v209, s[40:43], s75 offen offset:1024 lds\n\t" \
  "buffer_load_dwordx4 v210, s[40:43], s75 offen offset:2048 lds\n\t" \
  "buffer_load_dwordx4 v211, s[40:43], s75 offen offset:3072 lds\n\t" \
  "v_and_b32 v232, 3, v203\n\t" \
  "v_lshlrev_b32 v200, 3, v232\n\t" \
  "v_bfe_u32 v232, v203, 2, 2\n\t" \
  "v_lshl_or_b32 v200, v232, 6, v200\n\t" \
  "v_bfe_u32 v232, v203, 4, 1\n\t" \
  "v_lshl_or_b32 v200, v232, 5, v200\n\t" \
  "v_lshrrev_b32 v232, 4, v203\n\t" \
  "v_lshl_or_b32 v200, v232, 11, v200\n\t" \
  "v_xor_b32 v201, 32, v200\n\t" \
  "s_lshl_b32 s91, %[qblk], 2\n\t" \
  "s_add_u32 s91, s91, %[w]\n\t" \
  "s_mul_i32 s44, s91, 81920\n\t" \
  "s_add_u32 s44, s44, 311296\n\t" \
  "s_mov_b64 s[72:73], %[ws]\n\t" \
  "s_add_u32 s44, s44, s72\n\t" \
  "s_addc_u32 s45, s73, 0\n\t" \
  "s_and_b32 s45, s45, 0xffff\n\t" \
  "s_mov_b32 s46, 0x14000\n\t" \
  "s_mov_b32 s47, 0x20000\n\t" \
  "s_mul_i32 s48, s91, 524288\n\t" \
  "s_mul_i32 s72, %[head], 16384\n\t" \
  "s_add_u32 s48, s48, s72\n\t" \
  "s_add_u32 s48, s48, 5554176\n\t" \
  "s_mov_b64 s[72:73], %[ws]\n\t" \
  "s_add_u32 s48, s48, s72\n\t" \
  "s_addc_u32 s49, s73, 0\n\t" \
  "s_and_b32 s49, s49, 0xffff\n\t" \
  "s_mov_b32 s50, 0x4000\n\t" \
  "s_mov_b32 s51, 0x20000\n\t" \
  "v_and_b32 v232, 3, v203\n\t" \
  "v_lshlrev_b32 v215, 3, v232\n\t" \
  "v_bfe_u32 v232, v203, 2, 3\n\t" \
  "v_lshl_or_b32 v215, v232, 6, v215\n\t" \
  "v_bfe_u32 v232, v203, 4, 1\n\t" \
  "v_lshl_or_b32 v215, v232, 5, v215\n\t" \
  "v_lshrrev_b32 v232, 5, v203\n\t" \
  "v_lshl_or_b32 v215, v232, 11, v215\n\t" \
  "v_add_u32 v215, 0xc000, v215\n\t" \
  "s_mul_i32 s36, %[head], 524288\n\t" \
  "s_add_u32 s36, s36, 393216\n\t" \
  "s_mov_b64 s[72:73], %[kc]\n\t" \
  "s_add_u32 s36, s36, s72\n\t" \
  "s_addc_u32 s37, s73, 0\n\t" \
  "s_and_b32 s37, s37, 0xffff\n\t" \
  "s_mov_b32 s38, 0x20000\n\t" \
  "s_mov_b32 s39, 0x20000\n\t" \
  "s_mov_b32 s74, 0\n\t" \
  "s_mov_b32 s77, 0x4000\n\t" \
  "v_lshrrev_b32 v232, 4, v203\n\t" \
  "v_and_b32 v233, 15, v203\n\t" \
  "v_xor_b32 v233, v232, v233\n\t" \
  "v_lshlrev_b32 v232, 8, v232\n\t" \
  "v_add_u32 v232, s80, v232\n\t" \
  "v_lshl_add_u32 v204, v233, 4, v232\n\t" \
  "v_xor_b32 v205, 64, v204\n\t" \
  "v_xor_b32 v206, 128, v204\n\t" \
  "v_xor_b32 v207, 192, v204\n\t" \
  "s_mov_b64 s[52:53], %[wo]\n\t" \
  "s_and_b32 s53, s53, 0xffff\n\t" \
  "s_mov_b32 s54, 0x500000\n\t" \
  "s_mov_b32 s55, 0x20000\n\t" \
  "s_mov_b32 s56, 29409280\n\t" \
  "s_mov_b64 s[72:73], %[ws]\n\t" \
  "s_add_u32 s56, s56, s72\n\t" \
  "s_addc_u32 s57, s73, 0\n\t" \
  "s_and_b32 s57, s57, 0xffff\n\t" \
  "s_mov_b32 s58, 0x71c000\n\t" \
  "s_mov_b32 s59, 0x20000\n\t" \
  "v_bfe_u32 v232, v203, 2, 3\n\t" \
  "s_mul_i32 s84, %[w], 8\n\t" \
  "v_add_u32 v232, s84, v232\n\t" \
  "v_mul_u32_u24 v213, 1280, v232\n\t" \
  "v_lshrrev_b32 v232, 3, v203\n\t" \
  "v_and_b32 v232, 2, v232\n\t" \
  "v_and_b32 v233, 3, v203\n\t" \
  "v_xor_b32 v232, v232, v233\n\t" \
  "v_lshl_add_u32 v213, v232, 4, v213\n\t" \
  "v_lshrrev_b32 v233, 5, v203\n\t" \
  "v_lshl_add_u32 v213, v233, 6, v213\n\t" \
  "s_lshl_b32 s90, %[w], 10\n\t" \
  "s_add_u32 s90, s90, 0x20000\n\t" \
  "v_and_b32 v232, 3, v203\n\t" \
  "v_lshlrev_b32 v214, 3, v232\n\t" \
  "v_bfe_u32 v232, v203, 2, 3\n\t" \
  "v_lshl_or_b32 v214, v232, 6, v214\n\t" \
  "v_bfe_u32 v232, v203, 4, 1\n\t" \
  "v_lshl_or_b32 v214, v232, 5, v214\n\t" \
  "v_lshrrev_b32 v232, 5, v203\n\t" \
  "v_lshl_or_b32 v214, v232, 10, v214\n\t" \
  "s_and_b32 s84, %[w], 1\n\t" \
  "s_lshl_b32 s84, s84, 5\n\t" \
  "v_xor_b32 v214, s84, v214\n\t" \
  "s_lshr_b32 s84, %[w], 1\n\t" \
  "s_lshl_b32 s84, s84, 9\n\t" \
  "s_add_u32 s84, s84, 0x20000\n\t" \
  "v_add_u32 v214, s84, v214\n\t" \
  "s_lshr_b32 s85, %[bid], 1\n\t" \
  "s_and_b32 s86, %[bid], 1\n\t" \
  "s_mul_i32 s92, s85, 40960\n\t" \
  "s_mul_i32 s87, s86, 640\n\t" \
  "s_add_u32 s92, s92, s87\n\t" \
  "s_lshl_b32 s86, s86, 9\n\t" \
  "s_add_u32 s85, s85, s86\n\t" \
  "s_mul_i32 s87, s85, 5120\n\t" \
  "v_bfe_u32 v232, v203, 4, 1\n\t" \
  "v_lshrrev_b32 v233, 5, v203\n\t" \
  "v_lshl_or_b32 v232, v232, 1, v233\n\t" \
  "v_and_b32 v233, 15, v203\n\t" \
  "v_xor_b32 v232, v232, v233\n\t" \
  "v_lshlrev_b32 v233, 8, v233\n\t" \
  "v_lshl_add_u32 v232, v232, 4, v233\n\t" \
  "v_add_u32 v220, 0x8000, v232\n\t" \
  "v_xor_b32 v221, 64, v220\n\t" \
  "v_xor_b32 v222, 128, v220\n\t" \
  "v_xor_b32 v223, 192, v220\n\t" \
  "v_mov_b32 v224, 0x3f803f80\n\t" \
  "v_mov_b32 v225, 0x3f803f80\n\t" \
  "v_mov_b32 v226, 0x3f803f80\n\t" \
  "v_mov_b32 v227, 0x3f803f80\n\t" \
  "s_mul_i32 s68, s91, 1024\n\t" \
  "s_add_u32 s68, s68, 42172416\n\t" \
  "s_mov_b64 s[72:73], %[ws]\n\t" \
  "s_add_u32 s68, s68, s72\n\t" \
  "s_addc_u32 s69, s73, 0\n\t" \
  "v_and_b32 v232, 7, v203\n\t" \
  "v_mul_u32_u24 v232, 128, v232\n\t" \
  "s_mov_b32 s84, 0\n\t" \
  ".Lph%=:\n\t" \
  "global_load_dword v233, v232, s[68:69] sc1\n\t" \
  "s_waitcnt vmcnt(0)\n\t" \
  "v_cmp_ne_u32 vcc, %[tag], v233\n\t" \
  "s_cbranch_vccz .Lphd%=\n\t" \
  "s_sleep 1\n\t" \
  "s_add_u32 s84, s84, 1\n\t" \
  "s_cmp_lt_u32 s84, 30000\n\t" \
  "s_cbranch_scc1 .Lph%=\n\t" \
  "v_mov_b32 v233, 0\n\t" \
  "v_mov_b32 v234, 1\n\t" \
  "global_store_dword v233, v234, s[70:71] sc1\n\t" \
  "s_waitcnt vmcnt(0)\n\t" \
  ".Lphd%=:\n\t" \
  "s_mov_b32 s76, 0\n\t" \
  "buffer_load_dwordx4 v[136:139], v202, s[44:47], s76 offen offset:0 sc1\n\t" \
  "buffer_load_dwordx4 v[140:143], v202, s[44:47], s76 offen offset:1024 sc1\n\t" \
  "buffer_load_dwordx4 v[144:147], v202, s[44:47], s76 offen offset:2048 sc1\n\t" \
  "buffer_load_dwordx4 v[148:151], v202, s[44:47], s76 offen offset:3072 sc1\n\t" \
  "s_mov_b32 s76, 4096\n\t" \
  "buffer_load_dwordx4 v[152:155], v202, s[44:47], s76 offen offset:0 sc1\n\t" \
  "buffer_load_dwordx4 v[156:159], v202, s[44:47], s76 offen offset:1024 sc1\n\t" \
  "buffer_load_dwordx4 v[160:163], v202, s[44:47], s76 offen offset:2048 sc1\n\t" \
  "buffer_load_dwordx4 v[164:167], v202, s[44:47], s76 offen offset:3072 sc1\n\t" \
  "s_mov_b32 s76, 8192\n\t" \
  "buffer_load_dwordx4 v[168:171], v202, s[44:47], s76 offen offset:0 sc1\n\t" \
  "buffer_load_dwordx4 v[172:175], v202, s[44:47], s76 offen offset:1024 sc1\n\t" \
  "buffer_load_dwordx4 v[176:179], v202, s[44:47], s76 offen offset:2048 sc1\n\t" \
  "buffer_load_dwordx4 v[180:183], v202, s[44:47], s76 offen offset:3072 sc1\n\t" \
  "s_mov_b32 s76, 12288\n\t" \
  "buffer_load_dwordx4 v[184:187], v202, s[44:47], s76 offen offset:0 sc1\n\t" \
  "buffer_load_dwordx4 v[188:191], v202, s[44:47], s76 offen offset:1024 sc1\n\t" \
  "buffer_load_dwordx4 v[192:195], v202, s[44:47], s76 offen offset:2048 sc1\n\t" \
  "buffer_load_dwordx4 v[196:199], v202, s[44:47], s76 offen offset:3072 sc1\n\t" \
  "s_barrier\n\t" \
  "ds_read_b64_tr_b16 a[192:193], v200 offset:0\n\t" \
  "ds_read_b64_tr_b16 a[194:195], v200 offset:256\n\t" \
  "ds_read_b64_tr_b16 a[196:197], v201 offset:0\n\t" \
  "ds_read_b64_tr_b16 a[198:199], v201 offset:256\n\t" \
  "ds_read_b64_tr_b16 a[200:201], v200 offset:512\n\t" \
  "ds_read_b64_tr_b16 a[202:203], v200 offset:768\n\t" \
  "ds_read_b64_tr_b16 a[204:205], v201 offset:512\n\t" \
  "ds_read_b64_tr_b16 a[206:207], v201 offset:768\n\t" \
  "ds_read_b64_tr_b16 a[208:209], v200 offset:1024\n\t" \
  "ds_read_b64_tr_b16 a[210:211], v200 offset:1280\n\t" \
  "ds_read_b64_tr_b16 a[212:213], v201 offset:1024\n\t" \
  "ds_read_b64_tr_b16 a[214:215], v201 offset:1280\n\t" \
  "ds_read_b64_tr_b16 a[216:217], v200 offset:1536\n\t" \
  "ds_read_b64_tr_b16 a[218:219], v200 offset:1792\n\t" \
  "ds_read_b64_tr_b16 a[220:221], v201 offset:1536\n\t" \
  "ds_read_b64_tr_b16 a[222:223], v201 offset:1792\n\t" \
  "s_waitcnt vmcnt(15) lgkmcnt(14)\n\t" \
  "v_mfma_f32_16x16x32_bf16 v[8:11], a[192:195], v[136:139], 0\n\t" \
  "s_mov_b32 s76, 16384\n\t" \
  "buffer_load_dwordx4 a[128:131], v202, s[44:47], s76 offen offset:0 sc1\n\t" \
  "s_waitcnt lgkmcnt(12)\n\t" \
  "v_mfma_f32_16x16x32_bf16 v[12:15], a[196:199], v[136:139], 0\n\t" \
  "ds_read_b64_tr_b16 a[224:225], v200 offset:8192\n\t" \
  "s_waitcnt lgkmcnt(11)\n\t" \
  "v_mfma_f32_16x16x32_bf16 v[16:19], a[200:203], v[136:139], 0\n\t" \
  "ds_read_b64_tr_b16 a[226:227], v200 offset:8448\n\t" \
  "s_waitcnt lgkmcnt(10)\n\t" \
  "v_mfma_f32_16x16x32_bf16 v[20:23], a[204:207], v[136:139], 0\n\t" \
  "ds_read_b64_tr_b16 a[228:229], v201 offset:8192\n\t" \
  "s_waitcnt lgkmcnt(9)\n\t" \
  "v_mfma_f32_16x16x32_bf16 v[24:27], a[208:211], v[136:139], 0\n\t" \
  "buffer_load_dwordx4 a[132:135], v202, s[44:47], s76 offen offset:1024 sc1\n\t" \
  "s_waitcnt lgkmcnt(7)\n\t" \
  "v_mfma_f32_16x16x32_bf16 v[28:31], a[212:215], v[136:139], 0\n\t" \
  "ds_read_b64_tr_b16 a[230:231], v201 offset:8448\n\t" \
  "s_waitcnt lgkmcnt(6)\n\t" \
  "v_mfma_f32_16x16x32_bf16 v[32:35], a[216:219], v[136:139], 0\n\t" \
  "ds_read_b64_tr_b16 a[232:233], v200 offset:8704\n\t" \
  "s_waitcnt lgkmcnt(5)\n\t" \
  "v_mfma_f32_16x16x32_bf16 v[36:39], a[220:223], v[136:139], 0\n\t" \
  "ds_read_b64_tr_b16 a[234:235], v200 offset:8960\n\t" \
  "s_waitcnt vmcnt(16)\n\t" \
  "v_mfma_f32_16x16x32_bf16 v[40:43], a[192:195], v[140:143], 0\n\t" \
  "buffer_load_dwordx4 a[136:139], v202, s[44:47], s76 offen offset:2048 sc1\n\t" \
  "v_mfma_f32_16x16x32_bf16 v[44:47], a[196:199], v[140:143], 0\n\t" \
  "ds_read_b64_tr_b16 a[236:237], v201 offset:8704\n\t" \
  "v_mfma_f32_16x16x32_bf16 v[48:51], a[200:203], v[140:143], 0\n\t" \
  "ds_read_b64_tr_b16 a[238:239], v201 offset:8960\n\t" \
  "v_mfma_f32_16x16x32_bf16 v[52:55], a[204:207], v[140:143], 0\n\t" \
  "ds_read_b64_tr_b16 a[240:241], v200 offset:9216\n\t" \
  "v_mfma_f32_16x16x32_bf16 v[56:59], a[208:211], v[140:143], 0\n\t" \
  "buffer_load_dwordx4 a[140:143], v202, s[44:47], s76 offen offset:3072 sc1\n\t" \
  "v_mfma_f32_16x16x32_bf16 v[60:63], a[212:215], v[140:143], 0\n\t" \
  "ds_read_b64_tr_b16 a[242:243], v200 offset:9472\n\t" \
  "v_mfma_f32_16x16x32_bf16 v[64:67], a[216:219], v[140:143], 0\n\t" \
  "ds_read_b64_tr_b16 a[244:245], v201 offset:9216\n\t" \
  "v_mfma_f32_16x16x32_bf16 v[68:71], a[220:223], v[140:143], 0\n\t" \
  "s_mov_b32 s76, 20480\n\t" \
  "ds_read_b64_tr_b16 a[246:247], v201 offset:9472\n\t" \
  "s_waitcnt vmcnt(17)\n\t" \
  "v_mfma_f32_16x16x32_bf16 v[72:75], a[192:195], v[144:147], 0\n\t" \
  "buffer_load_dwordx4 a[144:147], v202, s[44:47], s76 offen offset:0 sc1\n\t" \
  "v_mfma_f32_16x16x32_bf16 v[76:79], a[196:199], v[144:147], 0\n\t" \
  "ds_read_b64_tr_b16 a[248:249], v200 offset:9728\n\t" \
  "v_mfma_f32_16x16x32_bf16 v[80:83], a[200:203], v[144:147], 0\n\t" \
  "ds_read_b64_tr_b16 a[250:251], v200 offset:9984\n\t" \
  "v_mfma_f32_16x16x32_bf16 v[84:87], a[204:207], v[144:147], 0\n\t" \
  "ds_read_b64_tr_b16 a[252:253], v201 offset:9728\n\t" \
  "v_mfma_f32_16x16x32_bf16 v[88:91], a[208:211], v[144:147], 0\n\t" \
  "buffer_load_dwordx4 a[148:151], v202, s[44:47], s76 offen offset:1024 sc1\n\t" \
  "v_mfma_f32_16x16x32_bf16 v[92:95], a[212:215], v[144:147], 0\n\t" \
  "ds_read_b64_tr_b16 a[254:255], v201 offset:9984\n\t" \
  "v_mfma_f32_16x16x32_bf16 v[96:99], a[216:219], v[144:147], 0\n\t" \
  "buffer_load_dwordx4 a[152:155], v202, s[44:47], s76 offen offset:2048 sc1\n\t" \
  "v_mfma_f32_16x16x32_bf16 v[100:103], a[220:223], v[144:147], 0\n\t" \
  "s_waitcnt vmcnt(19)\n\t" \
  "v_mfma_f32_16x16x32_bf16 v[104:107], a[192:195], v[148:151], 0\n\t" \
  "buffer_load_dwordx4 a[156:159], v202, s[44:47], s76 offen offset:3072 sc1\n\t" \
  "v_mfma_f32_16x16x32_bf16 v[108:111], a[196:199], v[148:151], 0\n\t" \
  "v_mfma_f32_16x16x32_bf16 v[112:115], a[200:203], v[148:151], 0\n\t" \
  "v_mfma_f32_16x16x32_bf16 v[116:119], a[204:207], v[148:151], 0\n\t" \
  "v_mfma_f32_16x16x32_bf16 v[120:123], a[208:211], v[148:151], 0\n\t" \
  "v_mfma_f32_16x16x32_bf16 v[124:127], a[212:215], v[148:151], 0\n\t" \
  "v_mfma_f32_16x16x32_bf16 v[128:131], a[216:219], v[148:151], 0\n\t" \
  "v_mfma_f32_16x16x32_bf16 v[132:135], a[220:223], v[148:151], 0\n\t" \
  "s_waitcnt vmcnt(19) lgkmcnt(3)\n\t" \
  "v_mfma_f32_16x16x32_bf16 v[8:11], a[224:227], v[152:155], v[8:11]\n\t" \
  "s_mov_b32 s76, 24576\n\t" \
  "buffer_load_dwordx4 a[160:163], v202, s[44:47], s76 offen offset:0 sc1\n\t" \
  "v_mfma_f32_16x16x32_bf16 v[12:15], a[228:231], v[152:155], v[12:15]\n\t" \
  "ds_read_b64_tr_b16 a[192:193], v200 offset:16384\n\t" \
  "v_mfma_f32_16x16x32_bf16 v[16:19], a[232:235], v[152:155], v[16:19]\n\t" \
  "ds_read_b64_tr_b16 a[194:195], v200 offset:16640\n\t" \
  "v_mfma_f32_16x16x32_bf16 v[20:23], a[236:239], v[152:155], v[20:23]\n\t" \
  "s_add_u32 m0, s80, 81920\n\t" \
  "s_mov_b32 s75, 2621440\n\t" \
  "ds_read_b64_tr_b16 a[196:197], v201 offset:16384\n\t" \
  "v_mfma_f32_16x16x32_bf16 v[24:27], a[240:243], v[152:155], v[24:27]\n\t" \
  "buffer_load_dwordx4 v208, s[40:43], s75 offen offset:0 lds\n\t" \
  "v_mfma_f32_16x16x32_bf16 v[28:31], a[244:247], v[152:155], v[28:31]\n\t" \
  "ds_read_b64_tr_b16 a[198:199], v201 offset:16640\n\t" \
  "s_waitcnt lgkmcnt(4)\n\t" \
  "v_mfma_f32_16x16x32_bf16 v[32:35], a[248:251], v[152:155], v[32:35]\n\t" \
  "ds_read_b64_tr_b16 a[200:201], v200 offset:16896\n\t" \
  "v_mfma_f32_16x16x32_bf16 v[36:39], a[252:255], v[152:155], v[36:39]\n\t" \
  "ds_read_b64_tr_b16 a[202:203], v200 offset:17152\n\t" \
  "s_waitcnt vmcnt(20)\n\t" \
  "v_mfma_f32_16x16x32_bf16 v[40:43], a[224:227], v[156:159], v[40:43]\n\t" \
  "buffer_load_dwordx4 a[164:167], v202, s[44:47], s76 offen offset:1024 sc1\n\t" \
  "v_mfma_f32_16x16x32_bf16 v[44:47], a[228:231], v[156:159], v[44:47]\n\t" \
  "ds_read_b64_tr_b16 a[204:205], v201 offset:16896\n\t" \
  "v_mfma_f32_16x16x32_bf16 v[48:51], a[232:235], v[156:159], v[48:51]\n\t" \
  "ds_read_b64_tr_b16 a[206:207], v201 offset:17152\n\t" \
  "v_mfma_f32_16x16x32_bf16 v[52:55], a[236:239], v[156:159], v[52:55]\n\t" \
  "ds_read_b64_tr_b16 a[208:209], v200 offset:17408\n\t" \
  "v_mfma_f32_16x16x32_bf16 v[56:59], a[240:243], v[156:159], v[56:59]\n\t" \
  "buffer_load_dwordx4 v209, s[40:43], s75 offen offset:1024 lds\n\t" \
  "v_mfma_f32_16x16x32_bf16 v[60:63], a[244:247], v[156:159], v[60:63]\n\t" \
  "ds_read_b64_tr_b16 a[210:211], v200 offset:17664\n\t" \
  "v_mfma_f32_16x16x32_bf16 v[64:67], a[248:251], v[156:159], v[64:67]\n\t" \
  "ds_read_b64_tr_b16 a[212:213], v201 offset:17408\n\t" \
  "v_mfma_f32_16x16x32_bf16 v[68:71], a[252:255], v[156:159], v[68:71]\n\t" \
  "ds_read_b64_tr_b16 a[214:215], v201 offset:17664\n\t" \
  "s_waitcnt vmcnt(21)\n\t" \
  "v_mfma_f32_16x16x32_bf16 v[72:75], a[224:227], v[160:163], v[72:75]\n\t" \
  "buffer_load_dwordx4 a[168:171], v202, s[44:47], s76 offen offset:2048 sc1\n\t" \
  "v_mfma_f32_16x16x32_bf16 v[76:79], a[228:231], v[160:163], v[76:79]\n\t" \
  "ds_read_b64_tr_b16 a[216:217], v200 offset:17920\n\t" \
  "v_mfma_f32_16x16x32_bf16 v[80:83], a[232:235], v[160:163], v[80:83]\n\t" \
  "ds_read_b64_tr_b16 a[218:219], v200 offset:18176\n\t" \
  "v_mfma_f32_16x16x32_bf16 v[84:87], a[236:239], v[160:163], v[84:87]\n\t" \
  "ds_read_b64_tr_b16 a[220:221], v201 offset:17920\n\t" \
  "v_mfma_f32_16x16x32_bf16 v[88:91], a[240:243], v[160:163], v[88:91]\n\t" \
  "buffer_load_dwordx4 v210, s[40:43], s75 offen offset:2048 lds\n\t" \
  "v_mfma_f32_16x16x32_bf16 v[92:95], a[244:247], v[160:163], v[92:95]\n\t" \
  "ds_read_b64_tr_b16 a[222:223], v201 offset:18176\n\t" \
  "v_mfma_f32_16x16x32_bf16 v[96:99], a[248:251], v[160:163], v[96:99]\n\t" \
  "buffer_load_dwordx4 a[172:175], v202, s[44:47], s76 offen offset:3072 sc1\n\t" \
  "v_mfma_f32_16x16x32_bf16 v[100:103], a[252:255], v[160:163], v[100:103]\n\t" \
  "s_waitcnt vmcnt(23)\n\t" \
  "v_mfma_f32_16x16x32_bf16 v[104:107], a[224:227], v[164:167], v[104:107]\n\t" \
  "buffer_load_dwordx4 v211, s[40:43], s75 offen offset:3072 lds\n\t" \
  "v_mfma_f32_16x16x32_bf16 v[108:111], a[228:231], v[164:167], v[108:111]\n\t" \
  "v_mfma_f32_16x16x32_bf16 v[112:115], a[232:235], v[164:167], v[112:115]\n\t" \
  "v_mfma_f32_16x16x32_bf16 v[116:119], a[236:239], v[164:167], v[116:119]\n\t" \
  "v_mfma_f32_16x16x32_bf16 v[120:123], a[240:243], v[164:167], v[120:123]\n\t" \
  "v_mfma_f32_16x16x32_bf16 v[124:127], a[244:247], v[164:167], v[124:127]\n\t" \
  "v_mfma_f32_16x16x32_bf16 v[128:131], a[248:251], v[164:167], v[128:131]\n\t" \
  "v_mfma_f32_16x16x32_bf16 v[132:135], a[252:255], v[164:167], v[132:135]\n\t" \
  "s_waitcnt vmcnt(23) lgkmcnt(3)\n\t" \
  "v_mfma_f32_16x16x32_bf16 v[8:11], a[192:195], v[168:171], v[8:11]\n\t" \
  "s_mov_b32 s76, 28672\n\t" \
  "buffer_load_dwordx4 a[176:179], v202, s[44:47], s76 offen offset:0 sc1\n\t" \
  "v_mfma_f32_16x16x32_bf16 v[12:15], a[196:199], v[168:171], v[12:15]\n\t" \
  "ds_read_b64_tr_b16 a[224:225], v200 offset:24576\n\t" \
  "v_mfma_f32_16x16x32_bf16 v[16:19], a[200:203], v[168:171], v[16:19]\n\t" \
  "ds_read_b64_tr_b16 a[226:227], v200 offset:24832\n\t" \
  "v_mfma_f32_16x16x32_bf16 v[20:23], a[204:207], v[168:171], v[20:23]\n\t" \
  "ds_read_b64_tr_b16 a[228:229], v201 offset:24576\n\t" \
  "v_mfma_f32_16x16x32_bf16 v[24:27], a[208:211], v[168:171], v[24:27]\n\t" \
  "buffer_load_dwordx4 a[180:183], v202, s[44:47], s76 offen offset:1024 sc1\n\t" \
  "v_mfma_f32_16x16x32_bf16 v[28:31], a[212:215], v[168:171], v[28:31]\n\t" \
  "ds_read_b64_tr_b16 a[230:231], v201 offset:24832\n\t" \
  "s_waitcnt lgkmcnt(4)\n\t" \
  "v_mfma_f32_16x16x32_bf16 v[32:35], a[216:219], v[168:171], v[32:35]\n\t" \
  "ds_read_b64_tr_b16 a[232:233], v200 offset:25088\n\t" \
  "v_mfma_f32_16x16x32_bf16 v[36:39], a[220:223], v[168:171], v[36:39]\n\t" \
  "ds_read_b64_tr_b16 a[234:235], v200 offset:25344\n\t" \
  "s_waitcnt vmcnt(24)\n\t" \
  "v_mfma_f32_16x16x32_bf16 v[40:43], a[192:195], v[172:175], v[40:43]\n\t" \
  "buffer_load_dwordx4 a[184:187], v202, s[44:47], s76 offen offset:2048 sc1\n\t" \
  "v_mfma_f32_16x16x32_bf16 v[44:47], a[196:199], v[172:175], v[44:47]\n\t" \
  "ds_read_b64_tr_b16 a[236:237], v201 offset:25088\n\t" \
  "v_mfma_f32_16x16x32_bf16 v[48:51], a[200:203], v[172:175], v[48:51]\n\t" \
  "ds_read_b64_tr_b16 a[238:239], v201 offset:25344\n\t" \
  "v_mfma_f32_16x16x32_bf16 v[52:55], a[204:207], v[172:175], v[52:55]\n\t" \
  "ds_read_b64_tr_b16 a[240:241], v200 offset:25600\n\t" \
  "v_mfma_f32_16x16x32_bf16 v[56:59], a[208:211], v[172:175], v[56:59]\n\t" \
  "buffer_load_dwordx4 a[188:191], v202, s[44:47], s76 offen offset:3072 sc1\n\t" \
  "v_mfma_f32_16x16x32_bf16 v[60:63], a[212:215], v[172:175], v[60:63]\n\t" \
  "ds_read_b64_tr_b16 a[242:243], v200 offset:25856\n\t" \
  "v_mfma_f32_16x16x32_bf16 v[64:67], a[216:219], v[172:175], v[64:67]\n\t" \
  "ds_read_b64_tr_b16 a[244:245], v201 offset:25600\n\t" \
  "v_mfma_f32_16x16x32_bf16 v[68:71], a[220:223], v[172:175], v[68:71]\n\t" \
  "s_mov_b32 s76, 32768\n\t" \
  "ds_read_b64_tr_b16 a[246:247], v201 offset:25856\n\t" \
  "s_waitcnt vmcnt(25)\n\t" \
  "v_mfma_f32_16x16x32_bf16 v[72:75], a[192:195], v[176:179], v[72:75]\n\t" \
  "buffer_load_dwordx4 a[0:3], v202, s[44:47], s76 offen offset:0 sc1\n\t" \
  "v_mfma_f32_16x16x32_bf16 v[76:79], a[196:199], v[176:179], v[76:79]\n\t" \
  "ds_read_b64_tr_b16 a[248:249], v200 offset:26112\n\t" \
  "v_mfma_f32_16x16x32_bf16 v[80:83], a[200:203], v[176:179], v[80:83]\n\t" \
  "ds_read_b64_tr_b16 a[250:251], v200 offset:26368\n\t" \
  "v_mfma_f32_16x16x32_bf16 v[84:87], a[204:207], v[176:179], v[84:87]\n\t" \
  "ds_read_b64_tr_b16 a[252:253], v201 offset:26112\n\t" \
  "v_mfma_f32_16x16x32_bf16 v[88:91], a[208:211], v[176:179], v[88:91]\n\t" \
  "buffer_load_dwordx4 a[4:7], v202, s[44:47], s76 offen offset:1024 sc1\n\t" \
  "v_mfma_f32_16x16x32_bf16 v[92:95], a[212:215], v[176:179], v[92:95]\n\t" \
  "ds_read_b64_tr_b16 a[254:255], v201 offset:26368\n\t" \
  "v_mfma_f32_16x16x32_bf16 v[96:99], a[216:219], v[176:179], v[96:99]\n\t" \
  "buffer_load_dwordx4 a[8:11], v202, s[44:47], s76 offen offset:2048 sc1\n\t" \
  "v_mfma_f32_16x16x32_bf16 v[100:103], a[220:223], v[176:179], v[100:103]\n\t" \
  "s_waitcnt vmcnt(27)\n\t" \
  "v_mfma_f32_16x16x32_bf16 v[104:107], a[192:195], v[180:183], v[104:107]\n\t" \
  "buffer_load_dwordx4 a[12:15], v202, s[44:47], s76 offen offset:3072 sc1\n\t" \
  "v_mfma_f32_16x16x32_bf16 v[108:111], a[196:199], v[180:183], v[108:111]\n\t" \
  "v_mfma_f32_16x16x32_bf16 v[112:115], a[200:203], v[180:183], v[112:115]\n\t" \
  "v_mfma_f32_16x16x32_bf16 v[116:119], a[204:207], v[180:183], v[116:119]\n\t" \
  "v_mfma_f32_16x16x32_bf16 v[120:123], a[208:211], v[180:183], v[120:123]\n\t" \
  "v_mfma_f32_16x16x32_bf16 v[124:127], a[212:215], v[180:183], v[124:127]\n\t" \
  "v_add_u32 v200, 0x8000, v200\n\t" \
  "v_mfma_f32_16x16x32_bf16 v[128:131], a[216:219], v[180:183], v[128:131]\n\t" \
  "v_and_b32 v200, 0x1ffff, v200\n\t" \
  "v_mfma_f32_16x16x32_bf16 v[132:135], a[220:223], v[180:183], v[132:135]\n\t" \
  "v_xor_b32 v201, 32, v200\n\t" \
  "s_waitcnt vmcnt(27) lgkmcnt(2)\n\t" \
  "v_mfma_f32_16x16x32_bf16 v[8:11], a[224:227], v[184:187], v[8:11]\n\t" \
  "s_mov_b32 s76, 36864\n\t" \
  "buffer_load_dwordx4 a[16:19], v202, s[44:47], s76 offen offset:0 sc1\n\t" \
  "v_mfma_f32_16x16x32_bf16 v[12:15], a[228:231], v[184:187], v[12:15]\n\t" \
  "ds_read_b64_tr_b16 a[192:193], v200 offset:0\n\t" \
  "v_mfma_f32_16x16x32_bf16 v[16:19], a[232:235], v[184:187], v[16:19]\n\t" \
  "ds_read_b64_tr_b16 a[194:195], v200 offset:256\n\t" \
  "v_mfma_f32_16x16x32_bf16 v[20:23], a[236:239], v[184:187], v[20:23]\n\t" \
  "s_add_u32 m0, s80, 98304\n\t" \
  "s_mov_b32 s75, 3145728\n\t" \
  "ds_read_b64_tr_b16 a[196:197], v201 offset:0\n\t" \
  "v_mfma_f32_16x16x32_bf16 v[24:27], a[240:243], v[184:187], v[24:27]\n\t" \
  "buffer_load_dwordx4 v208, s[40:43], s75 offen offset:0 lds\n\t" \
  "v_mfma_f32_16x16x32_bf16 v[28:31], a[244:247], v[184:187], v[28:31]\n\t" \
  "ds_read_b64_tr_b16 a[198:199], v201 offset:256\n\t" \
  "v_mfma_f32_16x16x32_bf16 v[32:35], a[248:251], v[184:187], v[32:35]\n\t" \
  "ds_read_b64_tr_b16 a[200:201], v200 offset:512\n\t" \
  "s_waitcnt lgkmcnt(5)\n\t" \
  "v_mfma_f32_16x16x32_bf16 v[36:39], a[252:255], v[184:187], v[36:39]\n\t" \
  "ds_read_b64_tr_b16 a[202:203], v200 offset:768\n\t" \
  "s_waitcnt vmcnt(28)\n\t" \
  "v_mfma_f32_16x16x32_bf16 v[40:43], a[224:227], v[188:191], v[40:43]\n\t" \
  "buffer_load_dwordx4 a[20:23], v202, s[44:47], s76 offen offset:1024 sc1\n\t" \
  "v_mfma_f32_16x16x32_bf16 v[44:47], a[228:231], v[188:191], v[44:47]\n\t" \
  "ds_read_b64_tr_b16 a[204:205], v201 offset:512\n\t" \
  "v_mfma_f32_16x16x32_bf16 v[48:51], a[232:235], v[188:191], v[48:51]\n\t" \
  "ds_read_b64_tr_b16 a[206:207], v201 offset:768\n\t" \
  "v_mfma_f32_16x16x32_bf16 v[52:55], a[236:239], v[188:191], v[52:55]\n\t" \
  "ds_read_b64_tr_b16 a[208:209], v200 offset:1024\n\t" \
  "v_mfma_f32_16x16x32_bf16 v[56:59], a[240:243], v[188:191], v[56:59]\n\t" \
  "buffer_load_dwordx4 v209, s[40:43], s75 offen offset:1024 lds\n\t" \
  "v_mfma_f32_16x16x32_bf16 v[60:63], a[244:247], v[188:191], v[60:63]\n\t" \
  "ds_read_b64_tr_b16 a[210:211], v200 offset:1280\n\t" \
  "v_mfma_f32_16x16x32_bf16 v[64:67], a[248:251], v[188:191], v[64:67]\n\t" \
  "ds_read_b64_tr_b16 a[212:213], v201 offset:1024\n\t" \
  "v_mfma_f32_16x16x32_bf16 v[68:71], a[252:255], v[188:191], v[68:71]\n\t" \
  "ds_read_b64_tr_b16 a[214:215], v201 offset:1280\n\t" \
  "s_waitcnt vmcnt(29)\n\t" \
  "v_mfma_f32_16x16x32_bf16 v[72:75], a[224:227], v[192:195], v[72:75]\n\t" \
  "buffer_load_dwordx4 a[24:27], v202, s[44:47], s76 offen offset:2048 sc1\n\t" \
  "v_mfma_f32_16x16x32_bf16 v[76:79], a[228:231], v[192:195], v[76:79]\n\t" \
  "ds_read_b64_tr_b16 a[216:217], v200 offset:1536\n\t" \
  "v_mfma_f32_16x16x32_bf16 v[80:83], a[232:235], v[192:195], v[80:83]\n\t" \
  "ds_read_b64_tr_b16 a[218:219], v200 offset:1792\n\t" \
  "v_mfma_f32_16x16x32_bf16 v[84:87], a[236:239], v[192:195], v[84:87]\n\t" \
  "ds_read_b64_tr_b16 a[220:221], v201 offset:1536\n\t" \
  "v_mfma_f32_16x16x32_bf16 v[88:91], a[240:243], v[192:195], v[88:91]\n\t" \
  "buffer_load_dwordx4 v210, s[40:43], s75 offen offset:2048 lds\n\t" \
  "v_mfma_f32_16x16x32_bf16 v[92:95], a[244:247], v[192:195], v[92:95]\n\t" \
  "ds_read_b64_tr_b16 a[222:223], v201 offset:1792\n\t" \
  "v_mfma_f32_16x16x32_bf16 v[96:99], a[248:251], v[192:195], v[96:99]\n\t" \
  "buffer_load_dwordx4 a[28:31], v202, s[44:47], s76 offen offset:3072 sc1\n\t" \
  "v_mfma_f32_16x16x32_bf16 v[100:103], a[252:255], v[192:195], v[100:103]\n\t" \
  "s_waitcnt vmcnt(31)\n\t" \
  "v_mfma_f32_16x16x32_bf16 v[104:107], a[224:227], v[196:199], v[104:107]\n\t" \
  "buffer_load_dwordx4 v211, s[40:43], s75 offen offset:3072 lds\n\t" \
  "v_mfma_f32_16x16x32_bf16 v[108:111], a[228:231], v[196:199], v[108:111]\n\t" \
  "v_mfma_f32_16x16x32_bf16 v[112:115], a[232:235], v[196:199], v[112:115]\n\t" \
  "v_mfma_f32_16x16x32_bf16 v[116:119], a[236:239], v[196:199], v[116:119]\n\t" \
  "v_mfma_f32_16x16x32_bf16 v[120:123], a[240:243], v[196:199], v[120:123]\n\t" \
  "v_mfma_f32_16x16x32_bf16 v[124:127], a[244:247], v[196:199], v[124:127]\n\t" \
  "v_mfma_f32_16x16x32_bf16 v[128:131], a[248:251], v[196:199], v[128:131]\n\t" \
  "v_mfma_f32_16x16x32_bf16 v[132:135], a[252:255], v[196:199], v[132:135]\n\t" \
  "s_waitcnt vmcnt(31) lgkmcnt(3)\n\t" \
  "v_mfma_f32_16x16x32_bf16 v[8:11], a[192:195], a[128:131], v[8:11]\n\t" \
  "s_mov_b32 s76, 40960\n\t" \
  "buffer_load_dwordx4 a[32:35], v202, s[44:47], s76 offen offset:0 sc1\n\t" \
  "v_mfma_f32_16x16x32_bf16 v[12:15], a[196:199], a[128:131], v[12:15]\n\t" \
  "ds_read_b64_tr_b16 a[224:225], v200 offset:8192\n\t" \
  "v_mfma_f32_16x16x32_bf16 v[16:19], a[200:203], a[128:131], v[16:19]\n\t" \
  "ds_read_b64_tr_b16 a[226:227], v200 offset:8448\n\t" \
  "v_mfma_f32_16x16x32_bf16 v[20:23], a[204:207], a[128:131], v[20:23]\n\t" \
  "ds_read_b64_tr_b16 a[228:229], v201 offset:8192\n\t" \
  "v_mfma_f32_16x16x32_bf16 v[24:27], a[208:211], a[128:131], v[24:27]\n\t" \
  "buffer_load_dwordx4 a[36:39], v202, s[44:47], s76 offen offset:1024 sc1\n\t" \
  "v_mfma_f32_16x16x32_bf16 v[28:31], a[212:215], a[128:131], v[28:31]\n\t" \
  "ds_read_b64_tr_b16 a[230:231], v201 offset:8448\n\t" \
  "s_waitcnt lgkmcnt(4)\n\t" \
  "v_mfma_f32_16x16x32_bf16 v[32:35], a[216:219], a[128:131], v[32:35]\n\t" \
  "ds_read_b64_tr_b16 a[232:233], v200 offset:8704\n\t" \
  "v_mfma_f32_16x16x32_bf16 v[36:39], a[220:223], a[128:131], v[36:39]\n\t" \
  "ds_read_b64_tr_b16 a[234:235], v200 offset:8960\n\t" \
  "s_waitcnt vmcnt(32)\n\t" \
  "v_mfma_f32_16x16x32_bf16 v[40:43], a[192:195], a[132:135], v[40:43]\n\t" \
  "buffer_load_dwordx4 a[40:43], v202, s[44:47], s76 offen offset:2048 sc1\n\t" \
  "v_mfma_f32_16x16x32_bf16 v[44:47], a[196:199], a[132:135], v[44:47]\n\t" \
  "ds_read_b64_tr_b16 a[236:237], v201 offset:8704\n\t" \
  "v_mfma_f32_16x16x32_bf16 v[48:51], a[200:203], a[132:135], v[48:51]\n\t" \
  "ds_read_b64_tr_b16 a[238:239], v201 offset:8960\n\t" \
  "v_mfma_f32_16x16x32_bf16 v[52:55], a[204:207], a[132:135], v[52:55]\n\t" \
  "ds_read_b64_tr_b16 a[240:241], v200 offset:9216\n\t" \
  "v_mfma_f32_16x16x32_bf16 v[56:59], a[208:211], a[132:135], v[56:59]\n\t" \
  "buffer_load_dwordx4 a[44:47], v202, s[44:47], s76 offen offset:3072 sc1\n\t" \
  "v_mfma_f32_16x16x32_bf16 v[60:63], a[212:215], a[132:135], v[60:63]\n\t" \
  "ds_read_b64_tr_b16 a[242:243], v200 offset:9472\n\t" \
  "v_mfma_f32_16x16x32_bf16 v[64:67], a[216:219], a[132:135], v[64:67]\n\t" \
  "ds_read_b64_tr_b16 a[244:245], v201 offset:9216\n\t" \
  "v_mfma_f32_16x16x32_bf16 v[68:71], a[220:223], a[132:135], v[68:71]\n\t" \
  "s_mov_b32 s76, 45056\n\t" \
  "ds_read_b64_tr_b16 a[246:247], v201 offset:9472\n\t" \
  "s_waitcnt vmcnt(33)\n\t" \
  "v_mfma_f32_16x16x32_bf16 v[72:75], a[192:195], a[136:139], v[72:75]\n\t" \
  "buffer_load_dwordx4 a[48:51], v202, s[44:47], s76 offen offset:0 sc1\n\t" \
  "v_mfma_f32_16x16x32_bf16 v[76:79], a[196:199], a[136:139], v[76:79]\n\t" \
  "ds_read_b64_tr_b16 a[248:249], v200 offset:9728\n\t" \
  "v_mfma_f32_16x16x32_bf16 v[80:83], a[200:203], a[136:139], v[80:83]\n\t" \
  "ds_read_b64_tr_b16 a[250:251], v200 offset:9984\n\t" \
  "v_mfma_f32_16x16x32_bf16 v[84:87], a[204:207], a[136:139], v[84:87]\n\t" \
  "ds_read_b64_tr_b16 a[252:253], v201 offset:9728\n\t" \
  "v_mfma_f32_16x16x32_bf16 v[88:91], a[208:211], a[136:139], v[88:91]\n\t" \
  "buffer_load_dwordx4 a[52:55], v202, s[44:47], s76 offen offset:1024 sc1\n\t" \
  "v_mfma_f32_16x16x32_bf16 v[92:95], a[212:215], a[136:139], v[92:95]\n\t" \
  "ds_read_b64_tr_b16 a[254:255], v201 offset:9984\n\t" \
  "v_mfma_f32_16x16x32_bf16 v[96:99], a[216:219], a[136:139], v[96:99]\n\t" \
  "buffer_load_dwordx4 a[56:59], v202, s[44:47], s76 offen offset:2048 sc1\n\t" \
  "v_mfma_f32_16x16x32_bf16 v[100:103], a[220:223], a[136:139], v[100:103]\n\t" \
  "s_waitcnt vmcnt(35)\n\t" \
  "v_mfma_f32_16x16x32_bf16 v[104:107], a[192:195], a[140:143], v[104:107]\n\t" \
  "buffer_load_dwordx4 a[60:63], v202, s[44:47], s76 offen offset:3072 sc1\n\t" \
  "v_mfma_f32_16x16x32_bf16 v[108:111], a[196:199], a[140:143], v[108:111]\n\t" \
  "v_mfma_f32_16x16x32_bf16 v[112:115], a[200:203], a[140:143], v[112:115]\n\t" \
  "v_mfma_f32_16x16x32_bf16 v[116:119], a[204:207], a[140:143], v[116:119]\n\t" \
  "v_mfma_f32_16x16x32_bf16 v[120:123], a[208:211], a[140:143], v[120:123]\n\t" \
  "v_mfma_f32_16x16x32_bf16 v[124:127], a[212:215], a[140:143], v[124:127]\n\t" \
  "v_mfma_f32_16x16x32_bf16 v[128:131], a[216:219], a[140:143], v[128:131]\n\t" \
  "v_mfma_f32_16x16x32_bf16 v[132:135], a[220:223], a[140:143], v[132:135]\n\t" \
  "s_waitcnt vmcnt(35) lgkmcnt(3)\n\t" \
  "v_mfma_f32_16x16x32_bf16 v[8:11], a[224:227], a[144:147], v[8:11]\n\t" \
  "s_mov_b32 s76, 49152\n\t" \
  "buffer_load_dwordx4 a[64:67], v202, s[44:47], s76 offen offset:0 sc1\n\t" \
  "v_mfma_f32_16x16x32_bf16 v[12:15], a[228:231], a[144:147], v[12:15]\n\t" \
  "ds_read_b64_tr_b16 a[192:193], v200 offset:16384\n\t" \
  "v_mfma_f32_16x16x32_bf16 v[16:19], a[232:235], a[144:147], v[16:19]\n\t" \
  "ds_read_b64_tr_b16 a[194:195], v200 offset:16640\n\t" \
  "v_mfma_f32_16x16x32_bf16 v[20:23], a[236:239], a[144:147], v[20:23]\n\t" \
  "ds_read_b64_tr_b16 a[196:197], v201 offset:16384\n\t" \
  "v_mfma_f32_16x16x32_bf16 v[24:27], a[240:243], a[144:147], v[24:27]\n\t" \
  "buffer_load_dwordx4 a[68:71], v202, s[44:47], s76 offen offset:1024 sc1\n\t" \
  "v_mfma_f32_16x16x32_bf16 v[28:31], a[244:247], a[144:147], v[28:31]\n\t" \
  "ds_read_b64_tr_b16 a[198:199], v201 offset:16640\n\t" \
  "s_waitcnt lgkmcnt(4)\n\t" \
  "v_mfma_f32_16x16x32_bf16 v[32:35], a[248:251], a[144:147], v[32:35]\n\t" \
  "ds_read_b64_tr_b16 a[200:201], v200 offset:16896\n\t" \
  "v_mfma_f32_16x16x32_bf16 v[36:39], a[252:255], a[144:147], v[36:39]\n\t" \
  "ds_read_b64_tr_b16 a[202:203], v200 offset:17152\n\t" \
  "s_waitcnt vmcnt(36)\n\t" \
  "v_mfma_f32_16x16x32_bf16 v[40:43], a[224:227], a[148:151], v[40:43]\n\t" \
  "buffer_load_dwordx4 a[72:75], v202, s[44:47], s76 offen offset:2048 sc1\n\t" \
  "v_mfma_f32_16x16x32_bf16 v[44:47], a[228:231], a[148:151], v[44:47]\n\t" \
  "ds_read_b64_tr_b16 a[204:205], v201 offset:16896\n\t" \
  "v_mfma_f32_16x16x32_bf16 v[48:51], a[232:235], a[148:151], v[48:51]\n\t" \
  "ds_read_b64_tr_b16 a[206:207], v201 offset:17152\n\t" \
  "v_mfma_f32_16x16x32_bf16 v[52:55], a[236:239], a[148:151], v[52:55]\n\t" \
  "ds_read_b64_tr_b16 a[208:209], v200 offset:17408\n\t" \
  "v_mfma_f32_16x16x32_bf16 v[56:59], a[240:243], a[148:151], v[56:59]\n\t" \
  "buffer_load_dwordx4 a[76:79], v202, s[44:47], s76 offen offset:3072 sc1\n\t" \
  "v_mfma_f32_16x16x32_bf16 v[60:63], a[244:247], a[148:151], v[60:63]\n\t" \
  "ds_read_b64_tr_b16 a[210:211], v200 offset:17664\n\t" \
  "v_mfma_f32_16x16x32_bf16 v[64:67], a[248:251], a[148:151], v[64:67]\n\t" \
  "ds_read_b64_tr_b16 a[212:213], v201 offset:17408\n\t" \
  "v_mfma_f32_16x16x32_bf16 v[68:71], a[252:255], a[148:151], v[68:71]\n\t" \
  "s_mov_b32 s76, 53248\n\t" \
  "ds_read_b64_tr_b16 a[214:215], v201 offset:17664\n\t" \
  "s_waitcnt vmcnt(37)\n\t" \
  "v_mfma_f32_16x16x32_bf16 v[72:75], a[224:227], a[152:155], v[72:75]\n\t" \
  "buffer_load_dwordx4 a[80:83], v202, s[44:47], s76 offen offset:0 sc1\n\t" \
  "v_mfma_f32_16x16x32_bf16 v[76:79], a[228:231], a[152:155], v[76:79]\n\t" \
  "ds_read_b64_tr_b16 a[216:217], v200 offset:17920\n\t" \
  "v_mfma_f32_16x16x32_bf16 v[80:83], a[232:235], a[152:155], v[80:83]\n\t" \
  "ds_read_b64_tr_b16 a[218:219], v200 offset:18176\n\t" \
  "v_mfma_f32_16x16x32_bf16 v[84:87], a[236:239], a[152:155], v[84:87]\n\t" \
  "ds_read_b64_tr_b16 a[220:221], v201 offset:17920\n\t" \
  "v_mfma_f32_16x16x32_bf16 v[88:91], a[240:243], a[152:155], v[88:91]\n\t" \
  "buffer_load_dwordx4 a[84:87], v202, s[44:47], s76 offen offset:1024 sc1\n\t" \
  "v_mfma_f32_16x16x32_bf16 v[92:95], a[244:247], a[152:155], v[92:95]\n\t" \
  "ds_read_b64_tr_b16 a[222:223], v201 offset:18176\n\t" \
  "v_mfma_f32_16x16x32_bf16 v[96:99], a[248:251], a[152:155], v[96:99]\n\t" \
  "buffer_load_dwordx4 a[88:91], v202, s[44:47], s76 offen offset:2048 sc1\n\t" \
  "v_mfma_f32_16x16x32_bf16 v[100:103], a[252:255], a[152:155], v[100:103]\n\t" \
  "s_waitcnt vmcnt(39)\n\t" \
  "v_mfma_f32_16x16x32_bf16 v[104:107], a[224:227], a[156:159], v[104:107]\n\t" \
  "buffer_load_dwordx4 a[92:95], v202, s[44:47], s76 offen offset:3072 sc1\n\t" \
  "v_mfma_f32_16x16x32_bf16 v[108:111], a[228:231], a[156:159], v[108:111]\n\t" \
  "v_mfma_f32_16x16x32_bf16 v[112:115], a[232:235], a[156:159], v[112:115]\n\t" \
  "v_mfma_f32_16x16x32_bf16 v[116:119], a[236:239], a[156:159], v[116:119]\n\t" \
  "v_mfma_f32_16x16x32_bf16 v[120:123], a[240:243], a[156:159], v[120:123]\n\t" \
  "v_mfma_f32_16x16x32_bf16 v[124:127], a[244:247], a[156:159], v[124:127]\n\t" \
  "v_mfma_f32_16x16x32_bf16 v[128:131], a[248:251], a[156:159], v[128:131]\n\t" \
  "v_mfma_f32_16x16x32_bf16 v[132:135], a[252:255], a[156:159], v[132:135]\n\t" \
  "s_waitcnt vmcnt(39) lgkmcnt(3)\n\t" \
  "v_mfma_f32_16x16x32_bf16 v[8:11], a[192:195], a[160:163], v[8:11]\n\t" \
  "s_mov_b32 s76, 57344\n\t" \
  "buffer_load_dwordx4 a[96:99], v202, s[44:47], s76 offen offset:0 sc1\n\t" \
  "v_mfma_f32_16x16x32_bf16 v[12:15], a[196:199], a[160:163], v[12:15]\n\t" \
  "ds_read_b64_tr_b16 a[224:225], v200 offset:24576\n\t" \
  "v_mfma_f32_16x16x32_bf16 v[16:19], a[200:203], a[160:163], v[16:19]\n\t" \
  "ds_read_b64_tr_b16 a[226:227], v200 offset:24832\n\t" \
  "v_mfma_f32_16x16x32_bf16 v[20:23], a[204:207], a[160:163], v[20:23]\n\t" \
  "s_add_u32 m0, s80, 114688\n\t" \
  "s_mov_b32 s75, 3670016\n\t" \
  "ds_read_b64_tr_b16 a[228:229], v201 offset:24576\n\t" \
  "v_mfma_f32_16x16x32_bf16 v[24:27], a[208:211], a[160:163], v[24:27]\n\t" \
  "buffer_load_dwordx4 v208, s[40:43], s75 offen offset:0 lds\n\t" \
  "v_mfma_f32_16x16x32_bf16 v[28:31], a[212:215], a[160:163], v[28:31]\n\t" \
  "ds_read_b64_tr_b16 a[230:231], v201 offset:24832\n\t" \
  "s_waitcnt lgkmcnt(4)\n\t" \
  "v_mfma_f32_16x16x32_bf16 v[32:35], a[216:219], a[160:163], v[32:35]\n\t" \
  "ds_read_b64_tr_b16 a[232:233], v200 offset:25088\n\t" \
  "v_mfma_f32_16x16x32_bf16 v[36:39], a[220:223], a[160:163], v[36:39]\n\t" \
  "ds_read_b64_tr_b16 a[234:235], v200 offset:25344\n\t" \
  "s_waitcnt vmcnt(39)\n\t" \
  "v_mfma_f32_16x16x32_bf16 v[40:43], a[192:195], a[164:167], v[40:43]\n\t" \
  "buffer_load_dwordx4 a[100:103], v202, s[44:47], s76 offen offset:1024 sc1\n\t" \
  "v_mfma_f32_16x16x32_bf16 v[44:47], a[196:199], a[164:167], v[44:47]\n\t" \
  "ds_read_b64_tr_b16 a[236:237], v201 offset:25088\n\t" \
  "v_mfma_f32_16x16x32_bf16 v[48:51], a[200:203], a[164:167], v[48:51]\n\t" \
  "ds_read_b64_tr_b16 a[238:239], v201 offset:25344\n\t" \
  "v_mfma_f32_16x16x32_bf16 v[52:55], a[204:207], a[164:167], v[52:55]\n\t" \
  "ds_read_b64_tr_b16 a[240:241], v200 offset:25600\n\t" \
  "v_mfma_f32_16x16x32_bf16 v[56:59], a[208:211], a[164:167], v[56:59]\n\t" \
  "buffer_load_dwordx4 v209, s[40:43], s75 offen offset:1024 lds\n\t" \
  "v_mfma_f32_16x16x32_bf16 v[60:63], a[212:215], a[164:167], v[60:63]\n\t" \
  "ds_read_b64_tr_b16 a[242:243], v200 offset:25856\n\t" \
  "v_mfma_f32_16x16x32_bf16 v[64:67], a[216:219], a[164:167], v[64:67]\n\t" \
  "ds_read_b64_tr_b16 a[244:245], v201 offset:25600\n\t" \
  "v_mfma_f32_16x16x32_bf16 v[68:71], a[220:223], a[164:167], v[68:71]\n\t" \
  "ds_read_b64_tr_b16 a[246:247], v201 offset:25856\n\t" \
  "s_waitcnt vmcnt(39)\n\t" \
  "v_mfma_f32_16x16x32_bf16 v[72:75], a[192:195], a[168:171], v[72:75]\n\t" \
  "buffer_load_dwordx4 a[104:107], v202, s[44:47], s76 offen offset:2048 sc1\n\t" \
  "v_mfma_f32_16x16x32_bf16 v[76:79], a[196:199], a[168:171], v[76:79]\n\t" \
  "ds_read_b64_tr_b16 a[248:249], v200 offset:26112\n\t" \
  "v_mfma_f32_16x16x32_bf16 v[80:83], a[200:203], a[168:171], v[80:83]\n\t" \
  "ds_read_b64_tr_b16 a[250:251], v200 offset:26368\n\t" \
  "v_mfma_f32_16x16x32_bf16 v[84:87], a[204:207], a[168:171], v[84:87]\n\t" \
  "ds_read_b64_tr_b16 a[252:253], v201 offset:26112\n\t" \
  "v_mfma_f32_16x16x32_bf16 v[88:91], a[208:211], a[168:171], v[88:91]\n\t" \
  "buffer_load_dwordx4 v210, s[40:43], s75 offen offset:2048 lds\n\t" \
  "v_mfma_f32_16x16x32_bf16 v[92:95], a[212:215], a[168:171], v[92:95]\n\t" \
  "ds_read_b64_tr_b16 a[254:255], v201 offset:26368\n\t" \
  "v_mfma_f32_16x16x32_bf16 v[96:99], a[216:219], a[168:171], v[96:99]\n\t" \
  "buffer_load_dwordx4 a[108:111], v202, s[44:47], s76 offen offset:3072 sc1\n\t" \
  "v_mfma_f32_16x16x32_bf16 v[100:103], a[220:223], a[168:171], v[100:103]\n\t" \
  "s_waitcnt vmcnt(40)\n\t" \
  "v_mfma_f32_16x16x32_bf16 v[104:107], a[192:195], a[172:175], v[104:107]\n\t" \
  "buffer_load_dwordx4 v211, s[40:43], s75 offen offset:3072 lds\n\t" \
  "v_mfma_f32_16x16x32_bf16 v[108:111], a[196:199], a[172:175], v[108:111]\n\t" \
  "v_mfma_f32_16x16x32_bf16 v[112:115], a[200:203], a[172:175], v[112:115]\n\t" \
  "v_mfma_f32_16x16x32_bf16 v[116:119], a[204:207], a[172:175], v[116:119]\n\t" \
  "v_mfma_f32_16x16x32_bf16 v[120:123], a[208:211], a[172:175], v[120:123]\n\t" \
  "v_mfma_f32_16x16x32_bf16 v[124:127], a[212:215], a[172:175], v[124:127]\n\t" \
  "v_add_u32 v200, 0x8000, v200\n\t" \
  "v_mfma_f32_16x16x32_bf16 v[128:131], a[216:219], a[172:175], v[128:131]\n\t" \
  "v_and_b32 v200, 0x1ffff, v200\n\t" \
  "v_mfma_f32_16x16x32_bf16 v[132:135], a[220:223], a[172:175], v[132:135]\n\t" \
  "v_xor_b32 v201, 32, v200\n\t" \
  "s_waitcnt vmcnt(39) lgkmcnt(2)\n\t" \
  "v_mfma_f32_16x16x32_bf16 v[8:11], a[224:227], a[176:179], v[8:11]\n\t" \
  "s_barrier\n\t" \
  "s_mov_b32 s76, 61440\n\t" \
  "buffer_load_dwordx4 a[112:115], v202, s[44:47], s76 offen offset:0 sc1\n\t" \
  "v_mfma_f32_16x16x32_bf16 v[12:15], a[228:231], a[176:179], v[12:15]\n\t" \
  "ds_read_b64_tr_b16 a[192:193], v200 offset:0\n\t" \
  "v_mfma_f32_16x16x32_bf16 v[16:19], a[232:235], a[176:179], v[16:19]\n\t" \
  "ds_read_b64_tr_b16 a[194:195], v200 offset:256\n\t" \
  "v_mfma_f32_16x16x32_bf16 v[20:23], a[236:239], a[176:179], v[20:23]\n\t" \
  "ds_read_b64_tr_b16 a[196:197], v201 offset:0\n\t" \
  "v_mfma_f32_16x16x32_bf16 v[24:27], a[240:243], a[176:179], v[24:27]\n\t" \
  "buffer_load_dwordx4 a[116:119], v202, s[44:47], s76 offen offset:1024 sc1\n\t" \
  "v_mfma_f32_16x16x32_bf16 v[28:31], a[244:247], a[176:179], v[28:31]\n\t" \
  "ds_read_b64_tr_b16 a[198:199], v201 offset:256\n\t" \
  "v_mfma_f32_16x16x32_bf16 v[32:35], a[248:251], a[176:179], v[32:35]\n\t" \
  "ds_read_b64_tr_b16 a[200:201], v200 offset:512\n\t" \
  "s_waitcnt lgkmcnt(5)\n\t" \
  "v_mfma_f32_16x16x32_bf16 v[36:39], a[252:255], a[176:179], v[36:39]\n\t" \
  "ds_read_b64_tr_b16 a[202:203], v200 offset:768\n\t" \
  "s_waitcnt vmcnt(40)\n\t" \
  "v_mfma_f32_16x16x32_bf16 v[40:43], a[224:227], a[180:183], v[40:43]\n\t" \
  "buffer_load_dwordx4 a[120:123], v202, s[44:47], s76 offen offset:2048 sc1\n\t" \
  "v_mfma_f32_16x16x32_bf16 v[44:47], a[228:231], a[180:183], v[44:47]\n\t" \
  "ds_read_b64_tr_b16 a[204:205], v201 offset:512\n\t" \
  "v_mfma_f32_16x16x32_bf16 v[48:51], a[232:235], a[180:183], v[48:51]\n\t" \
  "ds_read_b64_tr_b16 a[206:207], v201 offset:768\n\t" \
  "v_mfma_f32_16x16x32_bf16 v[52:55], a[236:239], a[180:183], v[52:55]\n\t" \
  "ds_read_b64_tr_b16 a[208:209], v200 offset:1024\n\t" \
  "v_mfma_f32_16x16x32_bf16 v[56:59], a[240:243], a[180:183], v[56:59]\n\t" \
  "buffer_load_dwordx4 a[124:127], v202, s[44:47], s76 offen offset:3072 sc1\n\t" \
  "v_mfma_f32_16x16x32_bf16 v[60:63], a[244:247], a[180:183], v[60:63]\n\t" \
  "ds_read_b64_tr_b16 a[210:211], v200 offset:1280\n\t" \
  "v_mfma_f32_16x16x32_bf16 v[64:67], a[248:251], a[180:183], v[64:67]\n\t" \
  "ds_read_b64_tr_b16 a[212:213], v201 offset:1024\n\t" \
  "v_mfma_f32_16x16x32_bf16 v[68:71], a[252:255], a[180:183], v[68:71]\n\t" \
  "s_mov_b32 s76, 65536\n\t" \
  "ds_read_b64_tr_b16 a[214:215], v201 offset:1280\n\t" \
  "s_waitcnt vmcnt(41)\n\t" \
  "v_mfma_f32_16x16x32_bf16 v[72:75], a[224:227], a[184:187], v[72:75]\n\t" \
  "buffer_load_dwordx4 v[136:139], v202, s[44:47], s76 offen offset:0 sc1\n\t" \
  "v_mfma_f32_16x16x32_bf16 v[76:79], a[228:231], a[184:187], v[76:79]\n\t" \
  "ds_read_b64_tr_b16 a[216:217], v200 offset:1536\n\t" \
  "v_mfma_f32_16x16x32_bf16 v[80:83], a[232:235], a[184:187], v[80:83]\n\t" \
  "ds_read_b64_tr_b16 a[218:219], v200 offset:1792\n\t" \
  "v_mfma_f32_16x16x32_bf16 v[84:87], a[236:239], a[184:187], v[84:87]\n\t" \
  "ds_read_b64_tr_b16 a[220:221], v201 offset:1536\n\t" \
  "v_mfma_f32_16x16x32_bf16 v[88:91], a[240:243], a[184:187], v[88:91]\n\t" \
  "buffer_load_dwordx4 v[140:143], v202, s[44:47], s76 offen offset:1024 sc1\n\t" \
  "v_mfma_f32_16x16x32_bf16 v[92:95], a[244:247], a[184:187], v[92:95]\n\t" \
  "ds_read_b64_tr_b16 a[222:223], v201 offset:1792\n\t" \
  "v_mfma_f32_16x16x32_bf16 v[96:99], a[248:251], a[184:187], v[96:99]\n\t" \
  "buffer_load_dwordx4 v[144:147], v202, s[44:47], s76 offen offset:2048 sc1\n\t" \
  "v_mfma_f32_16x16x32_bf16 v[100:103], a[252:255], a[184:187], v[100:103]\n\t" \
  "s_waitcnt vmcnt(43)\n\t" \
  "v_mfma_f32_16x16x32_bf16 v[104:107], a[224:227], a[188:191], v[104:107]\n\t" \
  "buffer_load_dwordx4 v[148:151], v202, s[44:47], s76 offen offset:3072 sc1\n\t" \
  "v_mfma_f32_16x16x32_bf16 v[108:111], a[228:231], a[188:191], v[108:111]\n\t" \
  "v_mfma_f32_16x16x32_bf16 v[112:115], a[232:235], a[188:191], v[112:115]\n\t" \
  "v_mfma_f32_16x16x32_bf16 v[116:119], a[236:239], a[188:191], v[116:119]\n\t" \
  "v_mfma_f32_16x16x32_bf16 v[120:123], a[240:243], a[188:191], v[120:123]\n\t" \
  "v_mfma_f32_16x16x32_bf16 v[124:127], a[244:247], a[188:191], v[124:127]\n\t" \
  "v_mfma_f32_16x16x32_bf16 v[128:131], a[248:251], a[188:191], v[128:131]\n\t" \
  "v_mfma_f32_16x16x32_bf16 v[132:135], a[252:255], a[188:191], v[132:135]\n\t" \
  "s_waitcnt vmcnt(43) lgkmcnt(3)\n\t" \
  "v_mfma_f32_16x16x32_bf16 v[8:11], a[192:195], a[0:3], v[8:11]\n\t" \
  "s_mov_b32 s76, 69632\n\t" \
  "buffer_load_dwordx4 v[152:155], v202, s[44:47], s76 offen offset:0 sc1\n\t" \
  "v_mfma_f32_16x16x32_bf16 v[12:15], a[196:199], a[0:3], v[12:15]\n\t" \
  "s_mul_i32 s64, %[mt], 81920\n\t" \
  "s_mul_i32 s72, %[nt], 160\n\t" \
  "s_add_u32 s64, s64, s72\n\t" \
  "s_mov_b64 s[72:73], %[x]\n\t" \
  "s_add_u32 s64, s64, s72\n\t" \
  "s_addc_u32 s65, s73, 0\n\t" \
  "s_and_b32 s65, s65, 0xffff\n\t" \
  "s_mov_b32 s66, 0x13ba0\n\t" \
  "s_mov_b32 s67, 0x20000\n\t" \
  "s_mul_i32 s84, %[w], 3\n\t" \
  "s_add_u32 s84, s84, 0\n\t" \
  "s_min_u32 s84, s84, 9\n\t" \
  "s_lshl_b32 s84, s84, 6\n\t" \
  "ds_read_b64_tr_b16 a[224:225], v200 offset:8192\n\t" \
  "v_add_u32 v232, s84, v203\n\t" \
  "v_mfma_f32_16x16x32_bf16 v[16:19], a[200:203], a[0:3], v[16:19]\n\t" \
  "ds_read_b64_tr_b16 a[226:227], v200 offset:8448\n\t" \
  "v_mul_u32_u24 v233, 6554, v232\n\t" \
  "v_mfma_f32_16x16x32_bf16 v[20:23], a[204:207], a[0:3], v[20:23]\n\t" \
  "ds_read_b64_tr_b16 a[228:229], v201 offset:8192\n\t" \
  "v_lshrrev_b32 v233, 16, v233\n\t" \
  "v_mfma_f32_16x16x32_bf16 v[24:27], a[208:211], a[0:3], v[24:27]\n\t" \
  "buffer_load_dwordx4 v[156:159], v202, s[44:47], s76 offen offset:1024 sc1\n\t" \
  "v_mfma_f32_16x16x32_bf16 v[28:31], a[212:215], a[0:3], v[28:31]\n\t" \
  "ds_read_b64_tr_b16 a[230:231], v201 offset:8448\n\t" \
  "v_mul_u32_u24 v233, 1120, v233\n\t" \
  "s_waitcnt lgkmcnt(4)\n\t" \
  "v_mfma_f32_16x16x32_bf16 v[32:35], a[216:219], a[0:3], v[32:35]\n\t" \
  "ds_read_b64_tr_b16 a[232:233], v200 offset:8704\n\t" \
  "v_lshl_add_u32 v217, v232, 4, v233\n\t" \
  "v_mfma_f32_16x16x32_bf16 v[36:39], a[220:223], a[0:3], v[36:39]\n\t" \
  "s_mul_i32 s84, %[w], 3\n\t" \
  "s_add_u32 s84, s84, 1\n\t" \
  "s_min_u32 s84, s84, 9\n\t" \
  "s_lshl_b32 s84, s84, 6\n\t" \
  "ds_read_b64_tr_b16 a[234:235], v200 offset:8960\n\t" \
  "v_add_u32 v232, s84, v203\n\t" \
  "s_waitcnt vmcnt(44)\n\t" \
  "v_mfma_f32_16x16x32_bf16 v[40:43], a[192:195], a[4:7], v[40:43]\n\t" \
  "buffer_load_dwordx4 v[160:163], v202, s[44:47], s76 offen offset:2048 sc1\n\t" \
  "v_mfma_f32_16x16x32_bf16 v[44:47], a[196:199], a[4:7], v[44:47]\n\t" \
  "ds_read_b64_tr_b16 a[236:237], v201 offset:8704\n\t" \
  "v_mul_u32_u24 v233, 6554, v232\n\t" \
  "v_mfma_f32_16x16x32_bf16 v[48:51], a[200:203], a[4:7], v[48:51]\n\t" \
  "ds_read_b64_tr_b16 a[238:239], v201 offset:8960\n\t" \
  "v_lshrrev_b32 v233, 16, v233\n\t" \
  "v_mfma_f32_16x16x32_bf16 v[52:55], a[204:207], a[4:7], v[52:55]\n\t" \
  "ds_read_b64_tr_b16 a[240:241], v200 offset:9216\n\t" \
  "v_mul_u32_u24 v233, 1120, v233\n\t" \
  "v_mfma_f32_16x16x32_bf16 v[56:59], a[208:211], a[4:7], v[56:59]\n\t" \
  "buffer_load_dwordx4 v[164:167], v202, s[44:47], s76 offen offset:3072 sc1\n\t" \
  "v_mfma_f32_16x16x32_bf16 v[60:63], a[212:215], a[4:7], v[60:63]\n\t" \
  "ds_read_b64_tr_b16 a[242:243], v200 offset:9472\n\t" \
  "v_lshl_add_u32 v218, v232, 4, v233\n\t" \
  "v_mfma_f32_16x16x32_bf16 v[64:67], a[216:219], a[4:7], v[64:67]\n\t" \
  "s_mul_i32 s84, %[w], 3\n\t" \
  "s_add_u32 s84, s84, 2\n\t" \
  "s_min_u32 s84, s84, 9\n\t" \
  "s_lshl_b32 s84, s84, 6\n\t" \
  "ds_read_b64_tr_b16 a[244:245], v201 offset:9216\n\t" \
  "v_add_u32 v232, s84, v203\n\t" \
  "v_mfma_f32_16x16x32_bf16 v[68:71], a[220:223], a[4:7], v[68:71]\n\t" \
  "s_mov_b32 s76, 73728\n\t" \
  "ds_read_b64_tr_b16 a[246:247], v201 offset:9472\n\t" \
  "v_mul_u32_u24 v233, 6554, v232\n\t" \
  "s_waitcnt vmcnt(45)\n\t" \
  "v_mfma_f32_16x16x32_bf16 v[72:75], a[192:195], a[8:11], v[72:75]\n\t" \
  "buffer_load_dwordx4 v[168:171], v202, s[44:47], s76 offen offset:0 sc1\n\t" \
  "v_mfma_f32_16x16x32_bf16 v[76:79], a[196:199], a[8:11], v[76:79]\n\t" \
  "ds_read_b64_tr_b16 a[248:249], v200 offset:9728\n\t" \
  "v_lshrrev_b32 v233, 16, v233\n\t" \
  "v_mfma_f32_16x16x32_bf16 v[80:83], a[200:203], a[8:11], v[80:83]\n\t" \
  "ds_read_b64_tr_b16 a[250:251], v200 offset:9984\n\t" \
  "v_mul_u32_u24 v233, 1120, v233\n\t" \
  "v_mfma_f32_16x16x32_bf16 v[84:87], a[204:207], a[8:11], v[84:87]\n\t" \
  "ds_read_b64_tr_b16 a[252:253], v201 offset:9728\n\t" \
  "v_lshl_add_u32 v219, v232, 4, v233\n\t" \
  "v_mfma_f32_16x16x32_bf16 v[88:91], a[208:211], a[8:11], v[88:91]\n\t" \
  "buffer_load_dwordx4 v[172:175], v202, s[44:47], s76 offen offset:1024 sc1\n\t" \
  "v_mfma_f32_16x16x32_bf16 v[92:95], a[212:215], a[8:11], v[92:95]\n\t" \
  "ds_read_b64_tr_b16 a[254:255], v201 offset:9984\n\t" \
  "v_mfma_f32_16x16x32_bf16 v[96:99], a[216:219], a[8:11], v[96:99]\n\t" \
  "buffer_load_dwordx4 v[176:179], v202, s[44:47], s76 offen offset:2048 sc1\n\t" \
  "v_mfma_f32_16x16x32_bf16 v[100:103], a[220:223], a[8:11], v[100:103]\n\t" \
  "s_waitcnt vmcnt(47)\n\t" \
  "v_mfma_f32_16x16x32_bf16 v[104:107], a[192:195], a[12:15], v[104:107]\n\t" \
  "buffer_load_dwordx4 v[180:183], v202, s[44:47], s76 offen offset:3072 sc1\n\t" \
  "v_mfma_f32_16x16x32_bf16 v[108:111], a[196:199], a[12:15], v[108:111]\n\t" \
  "v_mfma_f32_16x16x32_bf16 v[112:115], a[200:203], a[12:15], v[112:115]\n\t" \
  "v_mfma_f32_16x16x32_bf16 v[116:119], a[204:207], a[12:15], v[116:119]\n\t" \
  "v_mfma_f32_16x16x32_bf16 v[120:123], a[208:211], a[12:15], v[120:123]\n\t" \
  "v_mfma_f32_16x16x32_bf16 v[124:127], a[212:215], a[12:15], v[124:127]\n\t" \
  "v_mfma_f32_16x16x32_bf16 v[128:131], a[216:219], a[12:15], v[128:131]\n\t" \
  "v_mfma_f32_16x16x32_bf16 v[132:135], a[220:223], a[12:15], v[132:135]\n\t" \
  "s_waitcnt vmcnt(47) lgkmcnt(2)\n\t" \
  "v_mfma_f32_16x16x32_bf16 v[8:11], a[224:227], a[16:19], v[8:11]\n\t" \
  "s_mov_b32 s76, 77824\n\t" \
  "buffer_load_dwordx4 v[184:187], v202, s[44:47], s76 offen offset:0 sc1\n\t" \
  "v_mfma_f32_16x16x32_bf16 v[12:15], a[228:231], a[16:19], v[12:15]\n\t" \
  "ds_read_b64_tr_b16 a[192:193], v200 offset:16384\n\t" \
  "v_mfma_f32_16x16x32_bf16 v[16:19], a[232:235], a[16:19], v[16:19]\n\t" \
  "ds_read_b64_tr_b16 a[194:195], v200 offset:16640\n\t" \
  "v_mfma_f32_16x16x32_bf16 v[20:23], a[236:239], a[16:19], v[20:23]\n\t" \
  "s_add_u32 m0, s80, 0\n\t" \
  "s_mov_b32 s75, 4194304\n\t" \
  "ds_read_b64_tr_b16 a[196:197], v201 offset:16384\n\t" \
  "v_mfma_f32_16x16x32_bf16 v[24:27], a[240:243], a[16:19], v[24:27]\n\t" \
  "buffer_load_dwordx4 v208, s[40:43], s75 offen offset:0 lds\n\t" \
  "v_mfma_f32_16x16x32_bf16 v[28:31], a[244:247], a[16:19], v[28:31]\n\t" \
  "ds_read_b64_tr_b16 a[198:199], v201 offset:16640\n\t" \
  "v_mfma_f32_16x16x32_bf16 v[32:35], a[248:251], a[16:19], v[32:35]\n\t" \
  "ds_read_b64_tr_b16 a[200:201], v200 offset:16896\n\t" \
  "s_waitcnt lgkmcnt(5)\n\t" \
  "v_mfma_f32_16x16x32_bf16 v[36:39], a[252:255], a[16:19], v[36:39]\n\t" \
  "ds_read_b64_tr_b16 a[202:203], v200 offset:17152\n\t" \
  "s_waitcnt vmcnt(47)\n\t" \
  "v_mfma_f32_16x16x32_bf16 v[40:43], a[224:227], a[20:23], v[40:43]\n\t" \
  "buffer_load_dwordx4 v[188:191], v202, s[44:47], s76 offen offset:1024 sc1\n\t" \
  "v_mfma_f32_16x16x32_bf16 v[44:47], a[228:231], a[20:23], v[44:47]\n\t" \
  "ds_read_b64_tr_b16 a[204:205], v201 offset:16896\n\t" \
  "v_mfma_f32_16x16x32_bf16 v[48:51], a[232:235], a[20:23], v[48:51]\n\t" \
  "ds_read_b64_tr_b16 a[206:207], v201 offset:17152\n\t" \
  "v_mfma_f32_16x16x32_bf16 v[52:55], a[236:239], a[20:23], v[52:55]\n\t" \
  "ds_read_b64_tr_b16 a[208:209], v200 offset:17408\n\t" \
  "v_mfma_f32_16x16x32_bf16 v[56:59], a[240:243], a[20:23], v[56:59]\n\t" \
  "buffer_load_dwordx4 v209, s[40:43], s75 offen offset:1024 lds\n\t" \
  "v_mfma_f32_16x16x32_bf16 v[60:63], a[244:247], a[20:23], v[60:63]\n\t" \
  "ds_read_b64_tr_b16 a[210:211], v200 offset:17664\n\t" \
  "v_mfma_f32_16x16x32_bf16 v[64:67], a[248:251], a[20:23], v[64:67]\n\t" \
  "ds_read_b64_tr_b16 a[212:213], v201 offset:17408\n\t" \
  "v_mfma_f32_16x16x32_bf16 v[68:71], a[252:255], a[20:23], v[68:71]\n\t" \
  "ds_read_b64_tr_b16 a[214:215], v201 offset:17664\n\t" \
  "s_waitcnt vmcnt(47)\n\t" \
  "v_mfma_f32_16x16x32_bf16 v[72:75], a[224:227], a[24:27], v[72:75]\n\t" \
  "buffer_load_dwordx4 v[192:195], v202, s[44:47], s76 offen offset:2048 sc1\n\t" \
  "v_mfma_f32_16x16x32_bf16 v[76:79], a[228:231], a[24:27], v[76:79]\n\t" \
  "ds_read_b64_tr_b16 a[216:217], v200 offset:17920\n\t" \
  "v_mfma_f32_16x16x32_bf16 v[80:83], a[232:235], a[24:27], v[80:83]\n\t" \
  "ds_read_b64_tr_b16 a[218:219], v200 offset:18176\n\t" \
  "v_mfma_f32_16x16x32_bf16 v[84:87], a[236:239], a[24:27], v[84:87]\n\t" \
  "ds_read_b64_tr_b16 a[220:221], v201 offset:17920\n\t" \
  "v_mfma_f32_16x16x32_bf16 v[88:91], a[240:243], a[24:27], v[88:91]\n\t" \
  "buffer_load_dwordx4 v210, s[40:43], s75 offen offset:2048 lds\n\t" \
  "v_mfma_f32_16x16x32_bf16 v[92:95], a[244:247], a[24:27], v[92:95]\n\t" \
  "ds_read_b64_tr_b16 a[222:223], v201 offset:18176\n\t" \
  "v_mfma_f32_16x16x32_bf16 v[96:99], a[248:251], a[24:27], v[96:99]\n\t" \
  "buffer_load_dwordx4 v[196:199], v202, s[44:47], s76 offen offset:3072 sc1\n\t" \
  "v_mfma_f32_16x16x32_bf16 v[100:103], a[252:255], a[24:27], v[100:103]\n\t" \
  "s_waitcnt vmcnt(48)\n\t" \
  "v_mfma_f32_16x16x32_bf16 v[104:107], a[224:227], a[28:31], v[104:107]\n\t" \
  "buffer_load_dwordx4 v211, s[40:43], s75 offen offset:3072 lds\n\t" \
  "v_mfma_f32_16x16x32_bf16 v[108:111], a[228:231], a[28:31], v[108:111]\n\t" \
  "v_accvgpr_write_b32 a24, 0\n\t" \
  "v_accvgpr_write_b32 a25, 0\n\t" \
  "v_mfma_f32_16x16x32_bf16 v[112:115], a[232:235], a[28:31], v[112:115]\n\t" \
  "v_accvgpr_write_b32 a26, 0\n\t" \
  "v_accvgpr_write_b32 a27, 0\n\t" \
  "v_mfma_f32_16x16x32_bf16 v[116:119], a[236:239], a[28:31], v[116:119]\n\t" \
  "v_mfma_f32_16x16x32_bf16 v[120:123], a[240:243], a[28:31], v[120:123]\n\t" \
  "v_mfma_f32_16x16x32_bf16 v[124:127], a[244:247], a[28:31], v[124:127]\n\t" \
  "v_mfma_f32_16x16x32_bf16 v[128:131], a[248:251], a[28:31], v[128:131]\n\t" \
  "v_mfma_f32_16x16x32_bf16 v[132:135], a[252:255], a[28:31], v[132:135]\n\t" \
  "s_waitcnt vmcnt(47) lgkmcnt(1)\n\t" \
  "v_mfma_f32_16x16x32_bf16 v[8:11], a[192:195], a[32:35], v[8:11]\n\t" \
  "s_add_u32 m0, s80, 16384\n\t" \
  "s_mov_b32 s75, 4718592\n\t" \
  "buffer_load_dwordx4 v208, s[40:43], s75 offen offset:0 lds\n\t" \
  "v_mfma_f32_16x16x32_bf16 v[12:15], a[196:199], a[32:35], v[12:15]\n\t" \
  "ds_read_b64_tr_b16 a[224:225], v200 offset:24576\n\t" \
  "v_accvgpr_write_b32 a28, 0\n\t" \
  "v_mfma_f32_16x16x32_bf16 v[16:19], a[200:203], a[32:35], v[16:19]\n\t" \
  "ds_read_b64_tr_b16 a[226:227], v200 offset:24832\n\t" \
  "v_accvgpr_write_b32 a29, 0\n\t" \
  "v_mfma_f32_16x16x32_bf16 v[20:23], a[204:207], a[32:35], v[20:23]\n\t" \
  "ds_read_b64_tr_b16 a[228:229], v201 offset:24576\n\t" \
  "v_accvgpr_write_b32 a30, 0\n\t" \
  "v_mfma_f32_16x16x32_bf16 v[24:27], a[208:211], a[32:35], v[24:27]\n\t" \
  "buffer_load_dwordx4 v209, s[40:43], s75 offen offset:1024 lds\n\t" \
  "v_mfma_f32_16x16x32_bf16 v[28:31], a[212:215], a[32:35], v[28:31]\n\t" \
  "ds_read_b64_tr_b16 a[230:231], v201 offset:24832\n\t" \
  "v_accvgpr_write_b32 a31, 0\n\t" \
  "v_mfma_f32_16x16x32_bf16 v[32:35], a[216:219], a[32:35], v[32:35]\n\t" \
  "ds_read_b64_tr_b16 a[232:233], v200 offset:25088\n\t" \
  "v_accvgpr_write_b32 a0, 0\n\t" \
  "s_waitcnt lgkmcnt(5)\n\t" \
  "v_mfma_f32_16x16x32_bf16 v[36:39], a[220:223], a[32:35], v[36:39]\n\t" \
  "ds_read_b64_tr_b16 a[234:235], v200 offset:25344\n\t" \
  "v_accvgpr_write_b32 a1, 0\n\t" \
  "s_waitcnt vmcnt(48)\n\t" \
  "v_mfma_f32_16x16x32_bf16 v[40:43], a[192:195], a[36:39], v[40:43]\n\t" \
  "buffer_load_dwordx4 v210, s[40:43], s75 offen offset:2048 lds\n\t" \
  "v_mfma_f32_16x16x32_bf16 v[44:47], a[196:199], a[36:39], v[44:47]\n\t" \
  "ds_read_b64_tr_b16 a[236:237], v201 offset:25088\n\t" \
  "v_accvgpr_write_b32 a2, 0\n\t" \
  "v_mfma_f32_16x16x32_bf16 v[48:51], a[200:203], a[36:39], v[48:51]\n\t" \
  "ds_read_b64_tr_b16 a[238:239], v201 offset:25344\n\t" \
  "v_accvgpr_write_b32 a3, 0\n\t" \
  "v_mfma_f32_16x16x32_bf16 v[52:55], a[204:207], a[36:39], v[52:55]\n\t" \
  "ds_read_b64_tr_b16 a[240:241], v200 offset:25600\n\t" \
  "v_accvgpr_write_b32 a4, 0\n\t" \
  "v_mfma_f32_16x16x32_bf16 v[56:59], a[208:211], a[36:39], v[56:59]\n\t" \
  "buffer_load_dwordx4 v211, s[40:43], s75 offen offset:3072 lds\n\t" \
  "v_mfma_f32_16x16x32_bf16 v[60:63], a[212:215], a[36:39], v[60:63]\n\t" \
  "ds_read_b64_tr_b16 a[242:243], v200 offset:25856\n\t" \
  "v_accvgpr_write_b32 a5, 0\n\t" \
  "v_mfma_f32_16x16x32_bf16 v[64:67], a[216:219], a[36:39], v[64:67]\n\t" \
  "ds_read_b64_tr_b16 a[244:245], v201 offset:25600\n\t" \
  "v_accvgpr_write_b32 a6, 0\n\t" \
  "v_mfma_f32_16x16x32_bf16 v[68:71], a[220:223], a[36:39], v[68:71]\n\t" \
  "s_add_u32 s88, s92, 0\n\t" \
  "s_add_u32 m0, s90, 0\n\t" \
  "ds_read_b64_tr_b16 a[246:247], v201 offset:25856\n\t" \
  "v_accvgpr_write_b32 a7, 0\n\t" \
  "s_waitcnt vmcnt(49)\n\t" \
  "v_mfma_f32_16x16x32_bf16 v[72:75], a[192:195], a[40:43], v[72:75]\n\t" \
  "buffer_load_dwordx4 v213, s[52:55], s88 offen nt lds\n\t" \
  "v_mfma_f32_16x16x32_bf16 v[76:79], a[196:199], a[40:43], v[76:79]\n\t" \
  "ds_read_b64_tr_b16 a[248:249], v200 offset:26112\n\t" \
  "v_accvgpr_write_b32 a8, 0\n\t" \
  "v_mfma_f32_16x16x32_bf16 v[80:83], a[200:203], a[40:43], v[80:83]\n\t" \
  "ds_read_b64_tr_b16 a[250:251], v200 offset:26368\n\t" \
  "v_accvgpr_write_b32 a9, 0\n\t" \
  "v_mfma_f32_16x16x32_bf16 v[84:87], a[204:207], a[40:43], v[84:87]\n\t" \
  "ds_read_b64_tr_b16 a[252:253], v201 offset:26112\n\t" \
  "v_accvgpr_write_b32 a10, 0\n\t" \
  "v_mfma_f32_16x16x32_bf16 v[88:91], a[208:211], a[40:43], v[88:91]\n\t" \
  "v_accvgpr_write_b32 a11, 0\n\t" \
  "v_accvgpr_write_b32 a12, 0\n\t" \
  "v_mfma_f32_16x16x32_bf16 v[92:95], a[212:215], a[40:43], v[92:95]\n\t" \
  "ds_read_b64_tr_b16 a[254:255], v201 offset:26368\n\t" \
  "v_accvgpr_write_b32 a13, 0\n\t" \
  "v_mfma_f32_16x16x32_bf16 v[96:99], a[216:219], a[40:43], v[96:99]\n\t" \
  "v_accvgpr_write_b32 a14, 0\n\t" \
  "v_accvgpr_write_b32 a15, 0\n\t" \
  "v_mfma_f32_16x16x32_bf16 v[100:103], a[220:223], a[40:43], v[100:103]\n\t" \
  "v_accvgpr_write_b32 a16, 0\n\t" \
  "v_accvgpr_write_b32 a17, 0\n\t" \
  "s_waitcnt vmcnt(49)\n\t" \
  "v_mfma_f32_16x16x32_bf16 v[104:107], a[192:195], a[44:47], v[104:107]\n\t" \
  "v_accvgpr_write_b32 a18, 0\n\t" \
  "v_accvgpr_write_b32 a19, 0\n\t" \
  "v_mfma_f32_16x16x32_bf16 v[108:111], a[196:199], a[44:47], v[108:111]\n\t" \
  "v_accvgpr_write_b32 a20, 0\n\t" \
  "v_accvgpr_write_b32 a21, 0\n\t" \
  "v_mfma_f32_16x16x32_bf16 v[112:115], a[200:203], a[44:47], v[112:115]\n\t" \
  "v_accvgpr_write_b32 a22, 0\n\t" \
  "v_accvgpr_write_b32 a23, 0\n\t" \
  "v_mfma_f32_16x16x32_bf16 v[116:119], a[204:207], a[44:47], v[116:119]\n\t" \
  "v_accvgpr_write_b32 a32, 0\n\t" \
  "v_accvgpr_write_b32 a33, 0\n\t" \
  "v_mfma_f32_16x16x32_bf16 v[120:123], a[208:211], a[44:47], v[120:123]\n\t" \
  "v_accvgpr_write_b32 a34, 0\n\t" \
  "v_accvgpr_write_b32 a35, 0\n\t" \
  "v_mfma_f32_16x16x32_bf16 v[124:127], a[212:215], a[44:47], v[124:127]\n\t" \
  "v_accvgpr_write_b32 a36, 0\n\t" \
  "v_accvgpr_write_b32 a37, 0\n\t" \
  "v_add_u32 v200, 0x8000, v200\n\t" \
  "v_mfma_f32_16x16x32_bf16 v[128:131], a[216:219], a[44:47], v[128:131]\n\t" \
  "v_accvgpr_write_b32 a38, 0\n\t" \
  "v_accvgpr_write_b32 a39, 0\n\t" \
  "v_and_b32 v200, 0x1ffff, v200\n\t" \
  "v_mfma_f32_16x16x32_bf16 v[132:135], a[220:223], a[44:47], v[132:135]\n\t" \
  "v_accvgpr_write_b32 a40, 0\n\t" \
  "v_accvgpr_write_b32 a41, 0\n\t" \
  "v_xor_b32 v201, 32, v200\n\t" \
  "s_waitcnt vmcnt(48) lgkmcnt(0)\n\t" \
  "v_mfma_f32_16x16x32_bf16 v[8:11], a[224:227], a[48:51], v[8:11]\n\t" \
  "s_barrier\n\t" \
  "s_mov_b32 m0, s79\n\t" \
  "s_add_u32 s79, s79, 0x8000\n\t" \
  "buffer_load_dwordx4 v204, s[36:39], s74 offen offset:0 lds\n\t" \
  "v_mfma_f32_16x16x32_bf16 v[12:15], a[228:231], a[48:51], v[12:15]\n\t" \
  "ds_read_b64_tr_b16 a[192:193], v200 offset:0\n\t" \
  "v_accvgpr_write_b32 a42, 0\n\t" \
  "v_mfma_f32_16x16x32_bf16 v[16:19], a[232:235], a[48:51], v[16:19]\n\t" \
  "ds_read_b64_tr_b16 a[194:195], v200 offset:256\n\t" \
  "v_accvgpr_write_b32 a43, 0\n\t" \
  "v_mfma_f32_16x16x32_bf16 v[20:23], a[236:239], a[48:51], v[20:23]\n\t" \
  "s_and_b32 s79, s79, 0x1ffff\n\t" \
  "ds_read_b64_tr_b16 a[196:197], v201 offset:0\n\t" \
  "v_accvgpr_write_b32 a44, 0\n\t" \
  "v_mfma_f32_16x16x32_bf16 v[24:27], a[240:243], a[48:51], v[24:27]\n\t" \
  "buffer_load_dwordx4 v205, s[36:39], s74 offen offset:1024 lds\n\t" \
  "v_mfma_f32_16x16x32_bf16 v[28:31], a[244:247], a[48:51], v[28:31]\n\t" \
  "ds_read_b64_tr_b16 a[198:199], v201 offset:256\n\t" \
  "v_accvgpr_write_b32 a45, 0\n\t" \
  "v_mfma_f32_16x16x32_bf16 v[32:35], a[248:251], a[48:51], v[32:35]\n\t" \
  "ds_read_b64_tr_b16 a[200:201], v200 offset:512\n\t" \
  "v_accvgpr_write_b32 a46, 0\n\t" \
  "v_mfma_f32_16x16x32_bf16 v[36:39], a[252:255], a[48:51], v[36:39]\n\t" \
  "ds_read_b64_tr_b16 a[202:203], v200 offset:768\n\t" \
  "v_accvgpr_write_b32 a47, 0\n\t" \
  "s_waitcnt vmcnt(49)\n\t" \
  "v_mfma_f32_16x16x32_bf16 v[40:43], a[224:227], a[52:55], v[40:43]\n\t" \
  "buffer_load_dwordx4 v206, s[36:39], s74 offen offset:2048 lds\n\t" \
  "v_mfma_f32_16x16x32_bf16 v[44:47], a[228:231], a[52:55], v[44:47]\n\t" \
  "ds_read_b64_tr_b16 a[204:205], v201 offset:512\n\t" \
  "v_accvgpr_write_b32 a48, 0\n\t" \
  "v_mfma_f32_16x16x32_bf16 v[48:51], a[232:235], a[52:55], v[48:51]\n\t" \
  "ds_read_b64_tr_b16 a[206:207], v201 offset:768\n\t" \
  "v_accvgpr_write_b32 a49, 0\n\t" \
  "v_mfma_f32_16x16x32_bf16 v[52:55], a[236:239], a[52:55], v[52:55]\n\t" \
  "ds_read_b64_tr_b16 a[208:209], v200 offset:1024\n\t" \
  "v_accvgpr_write_b32 a50, 0\n\t" \
  "v_mfma_f32_16x16x32_bf16 v[56:59], a[240:243], a[52:55], v[56:59]\n\t" \
  "buffer_load_dwordx4 v207, s[36:39], s74 offen offset:3072 lds\n\t" \
  "s_add_u32 s74, s74, s77\n\t" \
  "v_mfma_f32_16x16x32_bf16 v[60:63], a[244:247], a[52:55], v[60:63]\n\t" \
  "s_mul_i32 s40, %[head], 524288\n\t" \
  "s_add_u32 s40, s40, 393216\n\t" \
  "s_mov_b64 s[72:73], %[vc]\n\t" \
  "s_add_u32 s40, s40, s72\n\t" \
  "s_addc_u32 s41, s73, 0\n\t" \
  "s_sub_u32 s40, s40, 1024\n\t" \
  "s_subb_u32 s41, s41, 0\n\t" \
  "s_and_b32 s41, s41, 0xffff\n\t" \
  "s_mov_b32 s42, 0x20400\n\t" \
  "s_mov_b32 s43, 0x20000\n\t" \
  "s_mov_b32 s75, 0\n\t" \
  "s_mov_b32 s78, 0x4000\n\t" \
  "ds_read_b64_tr_b16 a[210:211], v200 offset:1280\n\t" \
  "v_bfe_u32 v232, v203, 2, 3\n\t" \
  "v_mfma_f32_16x16x32_bf16 v[64:67], a[248:251], a[52:55], v[64:67]\n\t" \
  "s_mul_i32 s84, %[w], 16\n\t" \
  "ds_read_b64_tr_b16 a[212:213], v201 offset:1024\n\t" \
  "v_add_u32 v232, s84, v232\n\t" \
  "v_mfma_f32_16x16x32_bf16 v[68:71], a[252:255], a[52:55], v[68:71]\n\t" \
  "ds_read_b64_tr_b16 a[214:215], v201 offset:1280\n\t" \
  "v_mul_u32_u24 v234, 256, v232\n\t" \
  "s_waitcnt vmcnt(50)\n\t" \
  "v_mfma_f32_16x16x32_bf16 v[72:75], a[224:227], a[56:59], v[72:75]\n\t" \
  "v_lshrrev_b32 v232, 3, v203\n\t" \
  "v_accvgpr_write_b32 a51, 0\n\t" \
  "v_mfma_f32_16x16x32_bf16 v[76:79], a[228:231], a[56:59], v[76:79]\n\t" \
  "ds_read_b64_tr_b16 a[216:217], v200 offset:1536\n\t" \
  "v_and_b32 v232, 2, v232\n\t" \
  "v_mfma_f32_16x16x32_bf16 v[80:83], a[232:235], a[56:59], v[80:83]\n\t" \
  "ds_read_b64_tr_b16 a[218:219], v200 offset:1792\n\t" \
  "v_and_b32 v233, 3, v203\n\t" \
  "v_mfma_f32_16x16x32_bf16 v[84:87], a[236:239], a[56:59], v[84:87]\n\t" \
  "ds_read_b64_tr_b16 a[220:221], v201 offset:1536\n\t" \
  "v_xor_b32 v232, v232, v233\n\t" \
  "v_mfma_f32_16x16x32_bf16 v[88:91], a[240:243], a[56:59], v[88:91]\n\t" \
  "v_lshl_add_u32 v234, v232, 4, v234\n\t" \
  "v_accvgpr_write_b32 a52, 0\n\t" \
  "v_mfma_f32_16x16x32_bf16 v[92:95], a[244:247], a[56:59], v[92:95]\n\t" \
  "ds_read_b64_tr_b16 a[222:223], v201 offset:1792\n\t" \
  "v_lshrrev_b32 v233, 5, v203\n\t" \
  "v_mfma_f32_16x16x32_bf16 v[96:99], a[248:251], a[56:59], v[96:99]\n\t" \
  "v_lshl_add_u32 v234, v233, 6, v234\n\t" \
  "v_accvgpr_write_b32 a53, 0\n\t" \
  "v_mfma_f32_16x16x32_bf16 v[100:103], a[252:255], a[56:59], v[100:103]\n\t" \
  "v_add_u32 v208, 1024, v234\n\t" \
  "v_accvgpr_write_b32 a54, 0\n\t" \
  "s_waitcnt vmcnt(49)\n\t" \
  "v_mfma_f32_16x16x32_bf16 v[104:107], a[224:227], a[60:63], v[104:107]\n\t" \
  "v_add_u32 v209, 128, v234\n\t" \
  "v_accvgpr_write_b32 a55, 0\n\t" \
  "v_mfma_f32_16x16x32_bf16 v[108:111], a[228:231], a[60:63], v[108:111]\n\t" \
  "v_mov_b32 v210, v208\n\t" \
  "v_accvgpr_write_b32 a56, 0\n\t" \
  "v_mfma_f32_16x16x32_bf16 v[112:115], a[232:235], a[60:63], v[112:115]\n\t" \
  "v_mov_b32 v211, v209\n\t" \
  "v_accvgpr_write_b32 a57, 0\n\t" \
  "v_mfma_f32_16x16x32_bf16 v[116:119], a[236:239], a[60:63], v[116:119]\n\t" \
  "s_lshl_b32 s80, %[w], 12\n\t" \
  "s_add_u32 s80, s80, 49152\n\t" \
  "v_accvgpr_write_b32 a58, 0\n\t" \
  "v_mfma_f32_16x16x32_bf16 v[120:123], a[240:243], a[60:63], v[120:123]\n\t" \
  "s_add_u32 s88, s92, 128\n\t" \
  "s_add_u32 m0, s90, 4096\n\t" \
  "s_nop 0\n\t" \
  "buffer_load_dwordx4 v213, s[52:55], s88 offen nt lds\n\t" \
  "v_mfma_f32_16x16x32_bf16 v[124:127], a[244:247], a[60:63], v[124:127]\n\t" \
  "v_accvgpr_write_b32 a59, 0\n\t" \
  "v_mfma_f32_16x16x32_bf16 v[128:131], a[248:251], a[60:63], v[128:131]\n\t" \
  "v_mfma_f32_16x16x32_bf16 v[132:135], a[252:255], a[60:63], v[132:135]\n\t" \
  "s_waitcnt vmcnt(49) lgkmcnt(0)\n\t" \
  "v_mfma_f32_16x16x32_bf16 v[8:11], a[192:195], a[64:67], v[8:11]\n\t" \
  "s_mul_i32 s84, %[w], 3\n\t" \
  "s_add_u32 s84, s84, 0\n\t" \
  "s_min_u32 s84, s84, 9\n\t" \
  "s_lshl_b32 s84, s84, 10\n\t" \
  "s_add_u32 m0, s84, 152576\n\t" \
  "s_nop 0\n\t" \
  "buffer_load_dwordx4 v217, s[64:67], 0 offen lds\n\t" \
  "v_mfma_f32_16x16x32_bf16 v[12:15], a[196:199], a[64:67], v[12:15]\n\t" \
  "ds_read_b64_tr_b16 a[224:225], v200 offset:8192\n\t" \
  "v_accvgpr_write_b32 a60, 0\n\t" \
  "v_mfma_f32_16x16x32_bf16 v[16:19], a[200:203], a[64:67], v[16:19]\n\t" \
  "ds_read_b64_tr_b16 a[226:227], v200 offset:8448\n\t" \
  "v_accvgpr_write_b32 a61, 0\n\t" \
  "v_mfma_f32_16x16x32_bf16 v[20:23], a[204:207], a[64:67], v[20:23]\n\t" \
  "ds_read_b64_tr_b16 a[228:229], v201 offset:8192\n\t" \
  "v_accvgpr_write_b32 a62, 0\n\t" \
  "v_mfma_f32_16x16x32_bf16 v[24:27], a[208:211], a[64:67], v[24:27]\n\t" \
  "s_mul_i32 s84, %[w], 3\n\t" \
  "s_add_u32 s84, s84, 1\n\t" \
  "s_min_u32 s84, s84, 9\n\t" \
  "s_lshl_b32 s84, s84, 10\n\t" \
  "s_add_u32 m0, s84, 152576\n\t" \
  "s_nop 0\n\t" \
  "buffer_load_dwordx4 v218, s[64:67], 0 offen lds\n\t" \
  "v_mfma_f32_16x16x32_bf16 v[28:31], a[212:215], a[64:67], v[28:31]\n\t" \
  "ds_read_b64_tr_b16 a[230:231], v201 offset:8448\n\t" \
  "v_accvgpr_write_b32 a63, 0\n\t" \
  "v_mfma_f32_16x16x32_bf16 v[32:35], a[216:219], a[64:67], v[32:35]\n\t" \
  "ds_read_b64_tr_b16 a[232:233], v200 offset:8704\n\t" \
  "v_mfma_f32_16x16x32_bf16 v[36:39], a[220:223], a[64:67], v[36:39]\n\t" \
  "ds_read_b64_tr_b16 a[234:235], v200 offset:8960\n\t" \
  "s_waitcnt vmcnt(50)\n\t" \
  "v_mfma_f32_16x16x32_bf16 v[40:43], a[192:195], a[68:71], v[40:43]\n\t" \
  "s_mul_i32 s84, %[w], 3\n\t" \
  "s_add_u32 s84, s84, 2\n\t" \
  "s_min_u32 s84, s84, 9\n\t" \
  "s_lshl_b32 s84, s84, 10\n\t" \
  "s_add_u32 m0, s84, 152576\n\t" \
  "s_nop 0\n\t" \
  "buffer_load_dwordx4 v219, s[64:67], 0 offen lds\n\t" \
  "v_mfma_f32_16x16x32_bf16 v[44:47], a[196:199], a[68:71], v[44:47]\n\t" \
  "ds_read_b64_tr_b16 a[236:237], v201 offset:8704\n\t" \
  "v_accvgpr_write_b32 a64, 0\n\t" \
  "v_mfma_f32_16x16x32_bf16 v[48:51], a[200:203], a[68:71], v[48:51]\n\t" \
  "ds_read_b64_tr_b16 a[238:239], v201 offset:8960\n\t" \
  "v_accvgpr_write_b32 a65, 0\n\t" \
  "v_mfma_f32_16x16x32_bf16 v[52:55], a[204:207], a[68:71], v[52:55]\n\t" \
  "s_add_u32 s88, s92, 256\n\t" \
  "s_add_u32 m0, s90, 8192\n\t" \
  "ds_read_b64_tr_b16 a[240:241], v200 offset:9216\n\t" \
  "v_accvgpr_write_b32 a66, 0\n\t" \
  "v_mfma_f32_16x16x32_bf16 v[56:59], a[208:211], a[68:71], v[56:59]\n\t" \
  "buffer_load_dwordx4 v213, s[52:55], s88 offen nt lds\n\t" \
  "v_mfma_f32_16x16x32_bf16 v[60:63], a[212:215], a[68:71], v[60:63]\n\t" \
  "ds_read_b64_tr_b16 a[242:243], v200 offset:9472\n\t" \
  "v_accvgpr_write_b32 a67, 0\n\t" \
  "v_mfma_f32_16x16x32_bf16 v[64:67], a[216:219], a[68:71], v[64:67]\n\t" \
  "ds_read_b64_tr_b16 a[244:245], v201 offset:9216\n\t" \
  "v_mfma_f32_16x16x32_bf16 v[68:71], a[220:223], a[68:71], v[68:71]\n\t" \
  "ds_read_b64_tr_b16 a[246:247], v201 offset:9472\n\t" \
  "s_waitcnt vmcnt(51)\n\t" \
  "v_mfma_f32_16x16x32_bf16 v[72:75], a[192:195], a[72:75], v[72:75]\n\t" \
  "v_accvgpr_write_b32 a68, 0\n\t" \
  "v_accvgpr_write_b32 a69, 0\n\t" \
  "v_mfma_f32_16x16x32_bf16 v[76:79], a[196:199], a[72:75], v[76:79]\n\t" \
  "ds_read_b64_tr_b16 a[248:249], v200 offset:9728\n\t" \
  "v_accvgpr_write_b32 a70, 0\n\t" \
  "v_mfma_f32_16x16x32_bf16 v[80:83], a[200:203], a[72:75], v[80:83]\n\t" \
  "ds_read_b64_tr_b16 a[250:251], v200 offset:9984\n\t" \
  "v_accvgpr_write_b32 a71, 0\n\t" \
  "v_mfma_f32_16x16x32_bf16 v[84:87], a[204:207], a[72:75], v[84:87]\n\t" \
  "ds_read_b64_tr_b16 a[252:253], v201 offset:9728\n\t" \
  "v_mfma_f32_16x16x32_bf16 v[88:91], a[208:211], a[72:75], v[88:91]\n\t" \
  "v_mfma_f32_16x16x32_bf16 v[92:95], a[212:215], a[72:75], v[92:95]\n\t" \
  "ds_read_b64_tr_b16 a[254:255], v201 offset:9984\n\t" \
  "v_mfma_f32_16x16x32_bf16 v[96:99], a[216:219], a[72:75], v[96:99]\n\t" \
  "v_mfma_f32_16x16x32_bf16 v[100:103], a[220:223], a[72:75], v[100:103]\n\t" \
  "s_waitcnt vmcnt(50)\n\t" \
  "v_mfma_f32_16x16x32_bf16 v[104:107], a[192:195], a[76:79], v[104:107]\n\t" \
  "v_accvgpr_write_b32 a72, 0\n\t" \
  "v_accvgpr_write_b32 a73, 0\n\t" \
  "v_mfma_f32_16x16x32_bf16 v[108:111], a[196:199], a[76:79], v[108:111]\n\t" \
  "v_accvgpr_write_b32 a74, 0\n\t" \
  "v_accvgpr_write_b32 a75, 0\n\t" \
  "v_mfma_f32_16x16x32_bf16 v[112:115], a[200:203], a[76:79], v[112:115]\n\t" \
  "v_mfma_f32_16x16x32_bf16 v[116:119], a[204:207], a[76:79], v[116:119]\n\t" \
  "v_mfma_f32_16x16x32_bf16 v[120:123], a[208:211], a[76:79], v[120:123]\n\t" \
  "v_mfma_f32_16x16x32_bf16 v[124:127], a[212:215], a[76:79], v[124:127]\n\t" \
  "v_mfma_f32_16x16x32_bf16 v[128:131], a[216:219], a[76:79], v[128:131]\n\t" \
  "v_mfma_f32_16x16x32_bf16 v[132:135], a[220:223], a[76:79], v[132:135]\n\t" \
  "s_waitcnt vmcnt(49) lgkmcnt(2)\n\t" \
  "v_mfma_f32_16x16x32_bf16 v[8:11], a[224:227], a[80:83], v[8:11]\n\t" \
  "s_waitcnt vmcnt(38)\n\t" \
  "s_barrier\n\t" \
  "s_add_u32 s88, s92, 384\n\t" \
  "s_add_u32 m0, s90, 12288\n\t" \
  "s_nop 0\n\t" \
  "buffer_load_dwordx4 v213, s[52:55], s88 offen nt lds\n\t" \
  "v_mfma_f32_16x16x32_bf16 v[12:15], a[228:231], a[80:83], v[12:15]\n\t" \
  "ds_read_b64_tr_b16 a[192:193], v200 offset:16384\n\t" \
  "v_accvgpr_write_b32 a76, 0\n\t" \
  "v_mfma_f32_16x16x32_bf16 v[16:19], a[232:235], a[80:83], v[16:19]\n\t" \
  "ds_read_b64_tr_b16 a[194:195], v200 offset:16640\n\t" \
  "v_accvgpr_write_b32 a77, 0\n\t" \
  "v_mfma_f32_16x16x32_bf16 v[20:23], a[236:239], a[80:83], v[20:23]\n\t" \
  "s_mov_b32 m0, s80\n\t" \
  "s_add_u32 s80, s80, 0x8000\n\t" \
  "ds_read_b64_tr_b16 a[196:197], v201 offset:16384\n\t" \
  "v_accvgpr_write_b32 a78, 0\n\t" \
  "v_mfma_f32_16x16x32_bf16 v[24:27], a[240:243], a[80:83], v[24:27]\n\t" \
  "buffer_load_dwordx4 v208, s[40:43], s75 offen offset:0 lds\n\t" \
  "v_mfma_f32_16x16x32_bf16 v[28:31], a[244:247], a[80:83], v[28:31]\n\t" \
  "ds_read_b64_tr_b16 a[198:199], v201 offset:16640\n\t" \
  "v_accvgpr_write_b32 a79, 0\n\t" \
  "v_mfma_f32_16x16x32_bf16 v[32:35], a[248:251], a[80:83], v[32:35]\n\t" \
  "ds_read_b64_tr_b16 a[200:201], v200 offset:16896\n\t" \
  "s_waitcnt lgkmcnt(5)\n\t" \
  "v_mfma_f32_16x16x32_bf16 v[36:39], a[252:255], a[80:83], v[36:39]\n\t" \
  "s_and_b32 s80, s80, 0x1ffff\n\t" \
  "ds_read_b64_tr_b16 a[202:203], v200 offset:17152\n\t" \
  "v_mfma_f32_16x16x32_bf16 v[40:43], a[224:227], a[84:87], v[40:43]\n\t" \
  "buffer_load_dwordx4 v209, s[40:43], s75 offen offset:1024 lds\n\t" \
  "v_mfma_f32_16x16x32_bf16 v[44:47], a[228:231], a[84:87], v[44:47]\n\t" \
  "ds_read_b64_tr_b16 a[204:205], v201 offset:16896\n\t" \
  "v_accvgpr_write_b32 a80, 0\n\t" \
  "v_mfma_f32_16x16x32_bf16 v[48:51], a[232:235], a[84:87], v[48:51]\n\t" \
  "ds_read_b64_tr_b16 a[206:207], v201 offset:17152\n\t" \
  "v_accvgpr_write_b32 a81, 0\n\t" \
  "v_mfma_f32_16x16x32_bf16 v[52:55], a[236:239], a[84:87], v[52:55]\n\t" \
  "ds_read_b64_tr_b16 a[208:209], v200 offset:17408\n\t" \
  "v_accvgpr_write_b32 a82, 0\n\t" \
  "v_mfma_f32_16x16x32_bf16 v[56:59], a[240:243], a[84:87], v[56:59]\n\t" \
  "buffer_load_dwordx4 v210, s[40:43], s75 offen offset:2048 lds\n\t" \
  "v_mfma_f32_16x16x32_bf16 v[60:63], a[244:247], a[84:87], v[60:63]\n\t" \
  "ds_read_b64_tr_b16 a[210:211], v200 offset:17664\n\t" \
  "v_accvgpr_write_b32 a83, 0\n\t" \
  "v_mfma_f32_16x16x32_bf16 v[64:67], a[248:251], a[84:87], v[64:67]\n\t" \
  "ds_read_b64_tr_b16 a[212:213], v201 offset:17408\n\t" \
  "v_mfma_f32_16x16x32_bf16 v[68:71], a[252:255], a[84:87], v[68:71]\n\t" \
  "ds_read_b64_tr_b16 a[214:215], v201 offset:17664\n\t" \
  "v_mfma_f32_16x16x32_bf16 v[72:75], a[224:227], a[88:91], v[72:75]\n\t" \
  "buffer_load_dwordx4 v211, s[40:43], s75 offen offset:3072 lds\n\t" \
  "s_add_u32 s75, s75, s78\n\t" \
  "v_mfma_f32_16x16x32_bf16 v[76:79], a[228:231], a[88:91], v[76:79]\n\t" \
  "ds_read_b64_tr_b16 a[216:217], v200 offset:17920\n\t" \
  "v_accvgpr_write_b32 a84, 0\n\t" \
  "v_mfma_f32_16x16x32_bf16 v[80:83], a[232:235], a[88:91], v[80:83]\n\t" \
  "ds_read_b64_tr_b16 a[218:219], v200 offset:18176\n\t" \
  "v_accvgpr_write_b32 a85, 0\n\t" \
  "v_mfma_f32_16x16x32_bf16 v[84:87], a[236:239], a[88:91], v[84:87]\n\t" \
  "ds_read_b64_tr_b16 a[220:221], v201 offset:17920\n\t" \
  "v_accvgpr_write_b32 a86, 0\n\t" \
  "v_mfma_f32_16x16x32_bf16 v[88:91], a[240:243], a[88:91], v[88:91]\n\t" \
  "v_accvgpr_write_b32 a87, 0\n\t" \
  "v_mfma_f32_16x16x32_bf16 v[92:95], a[244:247], a[88:91], v[92:95]\n\t" \
  "ds_read_b64_tr_b16 a[222:223], v201 offset:18176\n\t" \
  "v_mfma_f32_16x16x32_bf16 v[96:99], a[248:251], a[88:91], v[96:99]\n\t" \
  "v_mfma_f32_16x16x32_bf16 v[100:103], a[252:255], a[88:91], v[100:103]\n\t" \
  "v_mfma_f32_16x16x32_bf16 v[104:107], a[224:227], a[92:95], v[104:107]\n\t" \
  "v_accvgpr_write_b32 a88, 0\n\t" \
  "v_accvgpr_write_b32 a89, 0\n\t" \
  "v_mfma_f32_16x16x32_bf16 v[108:111], a[228:231], a[92:95], v[108:111]\n\t" \
  "v_accvgpr_write_b32 a90, 0\n\t" \
  "v_accvgpr_write_b32 a91, 0\n\t" \
  "v_mfma_f32_16x16x32_bf16 v[112:115], a[232:235], a[92:95], v[112:115]\n\t" \
  "v_mfma_f32_16x16x32_bf16 v[116:119], a[236:239], a[92:95], v[116:119]\n\t" \
  "v_mfma_f32_16x16x32_bf16 v[120:123], a[240:243], a[92:95], v[120:123]\n\t" \
  "v_mfma_f32_16x16x32_bf16 v[124:127], a[244:247], a[92:95], v[124:127]\n\t" \
  "v_mfma_f32_16x16x32_bf16 v[128:131], a[248:251], a[92:95], v[128:131]\n\t" \
  "v_mfma_f32_16x16x32_bf16 v[132:135], a[252:255], a[92:95], v[132:135]\n\t" \
  "s_waitcnt lgkmcnt(2)\n\t" \
  "v_mfma_f32_16x16x32_bf16 v[8:11], a[192:195], a[96:99], v[8:11]\n\t" \
  "s_add_u32 s88, s92, 512\n\t" \
  "s_add_u32 m0, s90, 16384\n\t" \
  "s_nop 0\n\t" \
  "buffer_load_dwordx4 v213, s[52:55], s88 offen nt lds\n\t" \
  "v_mfma_f32_16x16x32_bf16 v[12:15], a[196:199], a[96:99], v[12:15]\n\t" \
  "ds_read_b64_tr_b16 a[224:225], v200 offset:24576\n\t" \
  "v_accvgpr_write_b32 a92, 0\n\t" \
  "v_mfma_f32_16x16x32_bf16 v[16:19], a[200:203], a[96:99], v[16:19]\n\t" \
  "ds_read_b64_tr_b16 a[226:227], v200 offset:24832\n\t" \
  "v_accvgpr_write_b32 a93, 0\n\t" \
  "v_mfma_f32_16x16x32_bf16 v[20:23], a[204:207], a[96:99], v[20:23]\n\t" \
  "ds_read_b64_tr_b16 a[228:229], v201 offset:24576\n\t" \
  "v_accvgpr_write_b32 a94, 0\n\t" \
  "v_mfma_f32_16x16x32_bf16 v[24:27], a[208:211], a[96:99], v[24:27]\n\t" \
  "v_accvgpr_write_b32 a95, 0\n\t" \
  "v_mfma_f32_16x16x32_bf16 v[28:31], a[212:215], a[96:99], v[28:31]\n\t" \
  "ds_read_b64_tr_b16 a[230:231], v201 offset:24832\n\t" \
  "v_mfma_f32_16x16x32_bf16 v[32:35], a[216:219], a[96:99], v[32:35]\n\t" \
  "ds_read_b64_tr_b16 a[232:233], v200 offset:25088\n\t" \
  "s_waitcnt lgkmcnt(5)\n\t" \
  "v_mfma_f32_16x16x32_bf16 v[36:39], a[220:223], a[96:99], v[36:39]\n\t" \
  "ds_read_b64_tr_b16 a[234:235], v200 offset:25344\n\t" \
  "v_mfma_f32_16x16x32_bf16 v[40:43], a[192:195], a[100:103], v[40:43]\n\t" \
  "v_accvgpr_write_b32 a96, 0\n\t" \
  "v_accvgpr_write_b32 a97, 0\n\t" \
  "v_mfma_f32_16x16x32_bf16 v[44:47], a[196:199], a[100:103], v[44:47]\n\t" \
  "ds_read_b64_tr_b16 a[236:237], v201 offset:25088\n\t" \
  "v_accvgpr_write_b32 a98, 0\n\t" \
  "v_mfma_f32_16x16x32_bf16 v[48:51], a[200:203], a[100:103], v[48:51]\n\t" \
  "ds_read_b64_tr_b16 a[238:239], v201 offset:25344\n\t" \
  "v_accvgpr_write_b32 a99, 0\n\t" \
  "v_mfma_f32_16x16x32_bf16 v[52:55], a[204:207], a[100:103], v[52:55]\n\t" \
  "ds_read_b64_tr_b16 a[240:241], v200 offset:25600\n\t" \
  "v_mfma_f32_16x16x32_bf16 v[56:59], a[208:211], a[100:103], v[56:59]\n\t" \
  "v_mfma_f32_16x16x32_bf16 v[60:63], a[212:215], a[100:103], v[60:63]\n\t" \
  "ds_read_b64_tr_b16 a[242:243], v200 offset:25856\n\t" \
  "v_mfma_f32_16x16x32_bf16 v[64:67], a[216:219], a[100:103], v[64:67]\n\t" \
  "ds_read_b64_tr_b16 a[244:245], v201 offset:25600\n\t" \
  "v_mfma_f32_16x16x32_bf16 v[68:71], a[220:223], a[100:103], v[68:71]\n\t" \
  "ds_read_b64_tr_b16 a[246:247], v201 offset:25856\n\t" \
  "v_mfma_f32_16x16x32_bf16 v[72:75], a[192:195], a[104:107], v[72:75]\n\t" \
  "v_accvgpr_write_b32 a100, 0\n\t" \
  "v_accvgpr_write_b32 a101, 0\n\t" \
  "v_mfma_f32_16x16x32_bf16 v[76:79], a[196:199], a[104:107], v[76:79]\n\t" \
  "ds_read_b64_tr_b16 a[248:249], v200 offset:26112\n\t" \
  "v_accvgpr_write_b32 a102, 0\n\t" \
  "v_mfma_f32_16x16x32_bf16 v[80:83], a[200:203], a[104:107], v[80:83]\n\t" \
  "ds_read_b64_tr_b16 a[250:251], v200 offset:26368\n\t" \
  "v_accvgpr_write_b32 a103, 0\n\t" \
  "v_mfma_f32_16x16x32_bf16 v[84:87], a[204:207], a[104:107], v[84:87]\n\t" \
  "ds_read_b64_tr_b16 a[252:253], v201 offset:26112\n\t" \
  "v_mfma_f32_16x16x32_bf16 v[88:91], a[208:211], a[104:107], v[88:91]\n\t" \
  "v_mfma_f32_16x16x32_bf16 v[92:95], a[212:215], a[104:107], v[92:95]\n\t" \
  "ds_read_b64_tr_b16 a[254:255], v201 offset:26368\n\t" \
  "v_mfma_f32_16x16x32_bf16 v[96:99], a[216:219], a[104:107], v[96:99]\n\t" \
  "v_mfma_f32_16x16x32_bf16 v[100:103], a[220:223], a[104:107], v[100:103]\n\t" \
  "v_mfma_f32_16x16x32_bf16 v[104:107], a[192:195], a[108:111], v[104:107]\n\t" \
  "v_accvgpr_write_b32 a104, 0\n\t" \
  "v_accvgpr_write_b32 a105, 0\n\t" \
  "v_mfma_f32_16x16x32_bf16 v[108:111], a[196:199], a[108:111], v[108:111]\n\t" \
  "v_accvgpr_write_b32 a106, 0\n\t" \
  "v_accvgpr_write_b32 a107, 0\n\t" \
  "v_mfma_f32_16x16x32_bf16 v[112:115], a[200:203], a[108:111], v[112:115]\n\t" \
  "v_mfma_f32_16x16x32_bf16 v[116:119], a[204:207], a[108:111], v[116:119]\n\t" \
  "v_mfma_f32_16x16x32_bf16 v[120:123], a[208:211], a[108:111], v[120:123]\n\t" \
  "v_mfma_f32_16x16x32_bf16 v[124:127], a[212:215], a[108:111], v[124:127]\n\t" \
  "v_add_u32 v200, 0x8000, v200\n\t" \
  "v_mfma_f32_16x16x32_bf16 v[128:131], a[216:219], a[108:111], v[128:131]\n\t" \
  "v_and_b32 v200, 0x1ffff, v200\n\t" \
  "v_mfma_f32_16x16x32_bf16 v[132:135], a[220:223], a[108:111], v[132:135]\n\t" \
  "v_xor_b32 v201, 32, v200\n\t" \
  "s_waitcnt vmcnt(43) lgkmcnt(1)\n\t" \
  "v_mfma_f32_16x16x32_bf16 v[8:11], a[224:227], a[112:115], v[8:11]\n\t" \
  "s_waitcnt vmcnt(20)\n\t" \
  "s_barrier\n\t" \
  "s_mov_b32 m0, s79\n\t" \
  "s_add_u32 s79, s79, 0x8000\n\t" \
  "buffer_load_dwordx4 v204, s[36:39], s74 offen offset:0 lds\n\t" \
  "v_mfma_f32_16x16x32_bf16 v[12:15], a[228:231], a[112:115], v[12:15]\n\t" \
  "ds_read_b64_tr_b16 a[192:193], v200 offset:0\n\t" \
  "v_accvgpr_write_b32 a108, 0\n\t" \
  "v_mfma_f32_16x16x32_bf16 v[16:19], a[232:235], a[112:115], v[16:19]\n\t" \
  "ds_read_b64_tr_b16 a[194:195], v200 offset:256\n\t" \
  "v_accvgpr_write_b32 a109, 0\n\t" \
  "v_mfma_f32_16x16x32_bf16 v[20:23], a[236:239], a[112:115], v[20:23]\n\t" \
  "s_and_b32 s79, s79, 0x1ffff\n\t" \
  "ds_read_b64_tr_b16 a[196:197], v201 offset:0\n\t" \
  "v_accvgpr_write_b32 a110, 0\n\t" \
  "v_mfma_f32_16x16x32_bf16 v[24:27], a[240:243], a[112:115], v[24:27]\n\t" \
  "buffer_load_dwordx4 v205, s[36:39], s74 offen offset:1024 lds\n\t" \
  "v_mfma_f32_16x16x32_bf16 v[28:31], a[244:247], a[112:115], v[28:31]\n\t" \
  "ds_read_b64_tr_b16 a[198:199], v201 offset:256\n\t" \
  "v_accvgpr_write_b32 a111, 0\n\t" \
  "v_mfma_f32_16x16x32_bf16 v[32:35], a[248:251], a[112:115], v[32:35]\n\t" \
  "ds_read_b64_tr_b16 a[200:201], v200 offset:512\n\t" \
  "s_waitcnt lgkmcnt(5)\n\t" \
  "v_mfma_f32_16x16x32_bf16 v[36:39], a[252:255], a[112:115], v[36:39]\n\t" \
  "ds_read_b64_tr_b16 a[202:203], v200 offset:768\n\t" \
  "v_mfma_f32_16x16x32_bf16 v[40:43], a[224:227], a[116:119], v[40:43]\n\t" \
  "buffer_load_dwordx4 v206, s[36:39], s74 offen offset:2048 lds\n\t" \
  "v_mfma_f32_16x16x32_bf16 v[44:47], a[228:231], a[116:119], v[44:47]\n\t" \
  "ds_read_b64_tr_b16 a[204:205], v201 offset:512\n\t" \
  "v_accvgpr_write_b32 a112, 0\n\t" \
  "v_mfma_f32_16x16x32_bf16 v[48:51], a[232:235], a[116:119], v[48:51]\n\t" \
  "ds_read_b64_tr_b16 a[206:207], v201 offset:768\n\t" \
  "v_accvgpr_write_b32 a113, 0\n\t" \
  "v_mfma_f32_16x16x32_bf16 v[52:55], a[236:239], a[116:119], v[52:55]\n\t" \
  "ds_read_b64_tr_b16 a[208:209], v200 offset:1024\n\t" \
  "v_accvgpr_write_b32 a114, 0\n\t" \
  "v_mfma_f32_16x16x32_bf16 v[56:59], a[240:243], a[116:119], v[56:59]\n\t" \
  "buffer_load_dwordx4 v207, s[36:39], s74 offen offset:3072 lds\n\t" \
  "s_add_u32 s74, s74, s77\n\t" \
  "v_mfma_f32_16x16x32_bf16 v[60:63], a[244:247], a[116:119], v[60:63]\n\t" \
  "ds_read_b64_tr_b16 a[210:211], v200 offset:1280\n\t" \
  "v_accvgpr_write_b32 a115, 0\n\t" \
  "v_mfma_f32_16x16x32_bf16 v[64:67], a[248:251], a[116:119], v[64:67]\n\t" \
  "ds_read_b64_tr_b16 a[212:213], v201 offset:1024\n\t" \
  "v_mfma_f32_16x16x32_bf16 v[68:71], a[252:255], a[116:119], v[68:71]\n\t" \
  "ds_read_b64_tr_b16 a[214:215], v201 offset:1280\n\t" \
  "v_mfma_f32_16x16x32_bf16 v[72:75], a[224:227], a[120:123], v[72:75]\n\t" \
  "v_accvgpr_write_b32 a116, 0\n\t" \
  "v_accvgpr_write_b32 a117, 0\n\t" \
  "v_mfma_f32_16x16x32_bf16 v[76:79], a[228:231], a[120:123], v[76:79]\n\t" \
  "ds_read_b64_tr_b16 a[216:217], v200 offset:1536\n\t" \
  "v_accvgpr_write_b32 a118, 0\n\t" \
  "v_mfma_f32_16x16x32_bf16 v[80:83], a[232:235], a[120:123], v[80:83]\n\t" \
  "ds_read_b64_tr_b16 a[218:219], v200 offset:1792\n\t" \
  "v_accvgpr_write_b32 a119, 0\n\t" \
  "v_mfma_f32_16x16x32_bf16 v[84:87], a[236:239], a[120:123], v[84:87]\n\t" \
  "ds_read_b64_tr_b16 a[220:221], v201 offset:1536\n\t" \
  "v_mfma_f32_16x16x32_bf16 v[88:91], a[240:243], a[120:123], v[88:91]\n\t" \
  "v_mfma_f32_16x16x32_bf16 v[92:95], a[244:247], a[120:123], v[92:95]\n\t" \
  "ds_read_b64_tr_b16 a[222:223], v201 offset:1792\n\t" \
  "v_mfma_f32_16x16x32_bf16 v[96:99], a[248:251], a[120:123], v[96:99]\n\t" \
  "v_mfma_f32_16x16x32_bf16 v[100:103], a[252:255], a[120:123], v[100:103]\n\t" \
  "v_mfma_f32_16x16x32_bf16 v[104:107], a[224:227], a[124:127], v[104:107]\n\t" \
  "v_accvgpr_write_b32 a120, 0\n\t" \
  "v_accvgpr_write_b32 a121, 0\n\t" \
  "v_mfma_f32_16x16x32_bf16 v[108:111], a[228:231], a[124:127], v[108:111]\n\t" \
  "v_accvgpr_write_b32 a122, 0\n\t" \
  "v_accvgpr_write_b32 a123, 0\n\t" \
  "v_mfma_f32_16x16x32_bf16 v[112:115], a[232:235], a[124:127], v[112:115]\n\t" \
  "v_mfma_f32_16x16x32_bf16 v[116:119], a[236:239], a[124:127], v[116:119]\n\t" \
  "v_mfma_f32_16x16x32_bf16 v[120:123], a[240:243], a[124:127], v[120:123]\n\t" \
  "v_mfma_f32_16x16x32_bf16 v[124:127], a[244:247], a[124:127], v[124:127]\n\t" \
  "v_mfma_f32_16x16x32_bf16 v[128:131], a[248:251], a[124:127], v[128:131]\n\t" \
  "v_mfma_f32_16x16x32_bf16 v[132:135], a[252:255], a[124:127], v[132:135]\n\t" \
  "s_waitcnt lgkmcnt(2)\n\t" \
  "v_mfma_f32_16x16x32_bf16 v[8:11], a[192:195], v[136:139], v[8:11]\n\t" \
  "s_mov_b32 m0, s80\n\t" \
  "s_add_u32 s80, s80, 0x8000\n\t" \
  "buffer_load_dwordx4 v208, s[40:43], s75 offen offset:0 lds\n\t" \
  "v_mfma_f32_16x16x32_bf16 v[12:15], a[196:199], v[136:139], v[12:15]\n\t" \
  "ds_read_b64_tr_b16 a[224:225], v200 offset:8192\n\t" \
  "v_accvgpr_write_b32 a124, 0\n\t" \
  "v_mfma_f32_16x16x32_bf16 v[16:19], a[200:203], v[136:139], v[16:19]\n\t" \
  "ds_read_b64_tr_b16 a[226:227], v200 offset:8448\n\t" \
  "v_accvgpr_write_b32 a125, 0\n\t" \
  "v_mfma_f32_16x16x32_bf16 v[20:23], a[204:207], v[136:139], v[20:23]\n\t" \
  "s_and_b32 s80, s80, 0x1ffff\n\t" \
  "ds_read_b64_tr_b16 a[228:229], v201 offset:8192\n\t" \
  "v_accvgpr_write_b32 a126, 0\n\t" \
  "v_mfma_f32_16x16x32_bf16 v[24:27], a[208:211], v[136:139], v[24:27]\n\t" \
  "buffer_load_dwordx4 v209, s[40:43], s75 offen offset:1024 lds\n\t" \
  "v_mfma_f32_16x16x32_bf16 v[28:31], a[212:215], v[136:139], v[28:31]\n\t" \
  "ds_read_b64_tr_b16 a[230:231], v201 offset:8448\n\t" \
  "v_accvgpr_write_b32 a127, 0\n\t" \
  "v_mfma_f32_16x16x32_bf16 v[32:35], a[216:219], v[136:139], v[32:35]\n\t" \
  "ds_read_b64_tr_b16 a[232:233], v200 offset:8704\n\t" \
  "s_waitcnt lgkmcnt(5)\n\t" \
  "v_mfma_f32_16x16x32_bf16 v[36:39], a[220:223], v[136:139], v[36:39]\n\t" \
  "ds_read_b64_tr_b16 a[234:235], v200 offset:8960\n\t" \
  "v_mfma_f32_16x16x32_bf16 v[40:43], a[192:195], v[140:143], v[40:43]\n\t" \
  "buffer_load_dwordx4 v210, s[40:43], s75 offen offset:2048 lds\n\t" \
  "v_mfma_f32_16x16x32_bf16 v[44:47], a[196:199], v[140:143], v[44:47]\n\t" \
  "ds_read_b64_tr_b16 a[236:237], v201 offset:8704\n\t" \
  "v_mfma_f32_16x16x32_bf16 v[48:51], a[200:203], v[140:143], v[48:51]\n\t" \
  "ds_read_b64_tr_b16 a[238:239], v201 offset:8960\n\t" \
  "v_mfma_f32_16x16x32_bf16 v[52:55], a[204:207], v[140:143], v[52:55]\n\t" \
  "ds_read_b64_tr_b16 a[240:241], v200 offset:9216\n\t" \
  "v_mfma_f32_16x16x32_bf16 v[56:59], a[208:211], v[140:143], v[56:59]\n\t" \
  "buffer_load_dwordx4 v211, s[40:43], s75 offen offset:3072 lds\n\t" \
  "s_add_u32 s75, s75, s78\n\t" \
  "v_mfma_f32_16x16x32_bf16 v[60:63], a[212:215], v[140:143], v[60:63]\n\t" \
  "ds_read_b64_tr_b16 a[242:243], v200 offset:9472\n\t" \
  "v_mfma_f32_16x16x32_bf16 v[64:67], a[216:219], v[140:143], v[64:67]\n\t" \
  "ds_read_b64_tr_b16 a[244:245], v201 offset:9216\n\t" \
  "v_mfma_f32_16x16x32_bf16 v[68:71], a[220:223], v[140:143], v[68:71]\n\t" \
  "ds_read_b64_tr_b16 a[246:247], v201 offset:9472\n\t" \
  "v_mfma_f32_16x16x32_bf16 v[72:75], a[192:195], v[144:147], v[72:75]\n\t" \
  "v_mfma_f32_16x16x32_bf16 v[76:79], a[196:199], v[144:147], v[76:79]\n\t" \
  "ds_read_b64_tr_b16 a[248:249], v200 offset:9728\n\t" \
  "v_mfma_f32_16x16x32_bf16 v[80:83], a[200:203], v[144:147], v[80:83]\n\t" \
  "ds_read_b64_tr_b16 a[250:251], v200 offset:9984\n\t" \
  "v_mfma_f32_16x16x32_bf16 v[84:87], a[204:207], v[144:147], v[84:87]\n\t" \
  "ds_read_b64_tr_b16 a[252:253], v201 offset:9728\n\t" \
  "v_mfma_f32_16x16x32_bf16 v[88:91], a[208:211], v[144:147], v[88:91]\n\t" \
  "v_mfma_f32_16x16x32_bf16 v[92:95], a[212:215], v[144:147], v[92:95]\n\t" \
  "ds_read_b64_tr_b16 a[254:255], v201 offset:9984\n\t" \
  "v_mfma_f32_16x16x32_bf16 v[96:99], a[216:219], v[144:147], v[96:99]\n\t" \
  "v_mfma_f32_16x16x32_bf16 v[100:103], a[220:223], v[144:147], v[100:103]\n\t" \
  "v_mfma_f32_16x16x32_bf16 v[104:107], a[192:195], v[148:151], v[104:107]\n\t" \
  "v_mfma_f32_16x16x32_bf16 v[108:111], a[196:199], v[148:151], v[108:111]\n\t" \
  "v_mfma_f32_16x16x32_bf16 v[112:115], a[200:203], v[148:151], v[112:115]\n\t" \
  "v_mfma_f32_16x16x32_bf16 v[116:119], a[204:207], v[148:151], v[116:119]\n\t" \
  "v_mfma_f32_16x16x32_bf16 v[120:123], a[208:211], v[148:151], v[120:123]\n\t" \
  "v_mfma_f32_16x16x32_bf16 v[124:127], a[212:215], v[148:151], v[124:127]\n\t" \
  "v_mfma_f32_16x16x32_bf16 v[128:131], a[216:219], v[148:151], v[128:131]\n\t" \
  "v_mfma_f32_16x16x32_bf16 v[132:135], a[220:223], v[148:151], v[132:135]\n\t" \
  "s_waitcnt lgkmcnt(4)\n\t" \
  "v_mfma_f32_16x16x32_bf16 v[8:11], a[224:227], v[152:155], v[8:11]\n\t" \
  "s_waitcnt vmcnt(24)\n\t" \
  "s_barrier\n\t" \
  "s_mov_b32 m0, s79\n\t" \
  "s_add_u32 s79, s79, 0x8000\n\t" \
  "buffer_load_dwordx4 v204, s[36:39], s74 offen offset:0 lds\n\t" \
  "v_mfma_f32_16x16x32_bf16 v[12:15], a[228:231], v[152:155], v[12:15]\n\t" \
  "ds_read_b64_tr_b16 a[192:193], v200 offset:16384\n\t" \
  "v_mfma_f32_16x16x32_bf16 v[16:19], a[232:235], v[152:155], v[16:19]\n\t" \
  "ds_read_b64_tr_b16 a[194:195], v200 offset:16640\n\t" \
  "v_mfma_f32_16x16x32_bf16 v[20:23], a[236:239], v[152:155], v[20:23]\n\t" \
  "s_and_b32 s79, s79, 0x1ffff\n\t" \
  "ds_read_b64_tr_b16 a[196:197], v201 offset:16384\n\t" \
  "v_mfma_f32_16x16x32_bf16 v[24:27], a[240:243], v[152:155], v[24:27]\n\t" \
  "buffer_load_dwordx4 v205, s[36:39], s74 offen offset:1024 lds\n\t" \
  "v_mfma_f32_16x16x32_bf16 v[28:31], a[244:247], v[152:155], v[28:31]\n\t" \
  "ds_read_b64_tr_b16 a[198:199], v201 offset:16640\n\t" \
  "s_waitcnt lgkmcnt(4)\n\t" \
  "v_mfma_f32_16x16x32_bf16 v[32:35], a[248:251], v[152:155], v[32:35]\n\t" \
  "ds_read_b64_tr_b16 a[200:201], v200 offset:16896\n\t" \
  "v_mfma_f32_16x16x32_bf16 v[36:39], a[252:255], v[152:155], v[36:39]\n\t" \
  "ds_read_b64_tr_b16 a[202:203], v200 offset:17152\n\t" \
  "v_mfma_f32_16x16x32_bf16 v[40:43], a[224:227], v[156:159], v[40:43]\n\t" \
  "buffer_load_dwordx4 v206, s[36:39], s74 offen offset:2048 lds\n\t" \
  "v_mfma_f32_16x16x32_bf16 v[44:47], a[228:231], v[156:159], v[44:47]\n\t" \
  "ds_read_b64_tr_b16 a[204:205], v201 offset:16896\n\t" \
  "v_mfma_f32_16x16x32_bf16 v[48:51], a[232:235], v[156:159], v[48:51]\n\t" \
  "ds_read_b64_tr_b16 a[206:207], v201 offset:17152\n\t" \
  "v_mfma_f32_16x16x32_bf16 v[52:55], a[236:239], v[156:159], v[52:55]\n\t" \
  "ds_read_b64_tr_b16 a[208:209], v200 offset:17408\n\t" \
  "v_mfma_f32_16x16x32_bf16 v[56:59], a[240:243], v[156:159], v[56:59]\n\t" \
  "buffer_load_dwordx4 v207, s[36:39], s74 offen offset:3072 lds\n\t" \
  "s_add_u32 s74, s74, s77\n\t" \
  "v_mfma_f32_16x16x32_bf16 v[60:63], a[244:247], v[156:159], v[60:63]\n\t" \
  "ds_read_b64_tr_b16 a[210:211], v200 offset:17664\n\t" \
  "v_mfma_f32_16x16x32_bf16 v[64:67], a[248:251], v[156:159], v[64:67]\n\t" \
  "ds_read_b64_tr_b16 a[212:213], v201 offset:17408\n\t" \
  "v_mfma_f32_16x16x32_bf16 v[68:71], a[252:255], v[156:159], v[68:71]\n\t" \
  "ds_read_b64_tr_b16 a[214:215], v201 offset:17664\n\t" \
  "v_mfma_f32_16x16x32_bf16 v[72:75], a[224:227], v[160:163], v[72:75]\n\t" \
  "v_mfma_f32_16x16x32_bf16 v[76:79], a[228:231], v[160:163], v[76:79]\n\t" \
  "ds_read_b64_tr_b16 a[216:217], v200 offset:17920\n\t" \
  "v_mfma_f32_16x16x32_bf16 v[80:83], a[232:235], v[160:163], v[80:83]\n\t" \
  "ds_read_b64_tr_b16 a[218:219], v200 offset:18176\n\t" \
  "v_mfma_f32_16x16x32_bf16 v[84:87], a[236:239], v[160:163], v[84:87]\n\t" \
  "ds_read_b64_tr_b16 a[220:221], v201 offset:17920\n\t" \
  "v_mfma_f32_16x16x32_bf16 v[88:91], a[240:243], v[160:163], v[88:91]\n\t" \
  "v_mfma_f32_16x16x32_bf16 v[92:95], a[244:247], v[160:163], v[92:95]\n\t" \
  "ds_read_b64_tr_b16 a[222:223], v201 offset:18176\n\t" \
  "v_mfma_f32_16x16x32_bf16 v[96:99], a[248:251], v[160:163], v[96:99]\n\t" \
  "v_mfma_f32_16x16x32_bf16 v[100:103], a[252:255], v[160:163], v[100:103]\n\t" \
  "v_mfma_f32_16x16x32_bf16 v[104:107], a[224:227], v[164:167], v[104:107]\n\t" \
  "v_mfma_f32_16x16x32_bf16 v[108:111], a[228:231], v[164:167], v[108:111]\n\t" \
  "v_mfma_f32_16x16x32_bf16 v[112:115], a[232:235], v[164:167], v[112:115]\n\t" \
  "v_mfma_f32_16x16x32_bf16 v[116:119], a[236:239], v[164:167], v[116:119]\n\t" \
  "v_mfma_f32_16x16x32_bf16 v[120:123], a[240:243], v[164:167], v[120:123]\n\t" \
  "v_mfma_f32_16x16x32_bf16 v[124:127], a[244:247], v[164:167], v[124:127]\n\t" \
  "v_mfma_f32_16x16x32_bf16 v[128:131], a[248:251], v[164:167], v[128:131]\n\t" \
  "v_mfma_f32_16x16x32_bf16 v[132:135], a[252:255], v[164:167], v[132:135]\n\t" \
  "s_waitcnt lgkmcnt(4)\n\t" \
  "v_mfma_f32_16x16x32_bf16 v[8:11], a[192:195], v[168:171], v[8:11]\n\t" \
  "v_mfma_f32_16x16x32_bf16 v[12:15], a[196:199], v[168:171], v[12:15]\n\t" \
  "ds_read_b64_tr_b16 a[224:225], v200 offset:24576\n\t" \
  "v_mfma_f32_16x16x32_bf16 v[16:19], a[200:203], v[168:171], v[16:19]\n\t" \
  "ds_read_b64_tr_b16 a[226:227], v200 offset:24832\n\t" \
  "v_mfma_f32_16x16x32_bf16 v[20:23], a[204:207], v[168:171], v[20:23]\n\t" \
  "ds_read_b64_tr_b16 a[228:229], v201 offset:24576\n\t" \
  "v_mfma_f32_16x16x32_bf16 v[24:27], a[208:211], v[168:171], v[24:27]\n\t" \
  "v_mfma_f32_16x16x32_bf16 v[28:31], a[212:215], v[168:171], v[28:31]\n\t" \
  "ds_read_b64_tr_b16 a[230:231], v201 offset:24832\n\t" \
  "s_waitcnt lgkmcnt(4)\n\t" \
  "v_mfma_f32_16x16x32_bf16 v[32:35], a[216:219], v[168:171], v[32:35]\n\t" \
  "ds_read_b64_tr_b16 a[232:233], v200 offset:25088\n\t" \
  "v_mfma_f32_16x16x32_bf16 v[36:39], a[220:223], v[168:171], v[36:39]\n\t" \
  "ds_read_b64_tr_b16 a[234:235], v200 offset:25344\n\t" \
  "v_mfma_f32_16x16x32_bf16 v[40:43], a[192:195], v[172:175], v[40:43]\n\t" \
  "v_mfma_f32_16x16x32_bf16 v[44:47], a[196:199], v[172:175], v[44:47]\n\t" \
  "ds_read_b64_tr_b16 a[236:237], v201 offset:25088\n\t" \
  "v_mfma_f32_16x16x32_bf16 v[48:51], a[200:203], v[172:175], v[48:51]\n\t" \
  "ds_read_b64_tr_b16 a[238:239], v201 offset:25344\n\t" \
  "v_mfma_f32_16x16x32_bf16 v[52:55], a[204:207], v[172:175], v[52:55]\n\t" \
  "ds_read_b64_tr_b16 a[240:241], v200 offset:25600\n\t" \
  "v_mfma_f32_16x16x32_bf16 v[56:59], a[208:211], v[172:175], v[56:59]\n\t" \
  "v_mfma_f32_16x16x32_bf16 v[60:63], a[212:215], v[172:175], v[60:63]\n\t" \
  "ds_read_b64_tr_b16 a[242:243], v200 offset:25856\n\t" \
  "v_mfma_f32_16x16x32_bf16 v[64:67], a[216:219], v[172:175], v[64:67]\n\t" \
  "ds_read_b64_tr_b16 a[244:245], v201 offset:25600\n\t" \
  "v_mfma_f32_16x16x32_bf16 v[68:71], a[220:223], v[172:175], v[68:71]\n\t" \
  "ds_read_b64_tr_b16 a[246:247], v201 offset:25856\n\t" \
  "v_mfma_f32_16x16x32_bf16 v[72:75], a[192:195], v[176:179], v[72:75]\n\t" \
  "v_mfma_f32_16x16x32_bf16 v[76:79], a[196:199], v[176:179], v[76:79]\n\t" \
  "ds_read_b64_tr_b16 a[248:249], v200 offset:26112\n\t" \
  "v_mfma_f32_16x16x32_bf16 v[80:83], a[200:203], v[176:179], v[80:83]\n\t" \
  "ds_read_b64_tr_b16 a[250:251], v200 offset:26368\n\t" \
  "v_mfma_f32_16x16x32_bf16 v[84:87], a[204:207], v[176:179], v[84:87]\n\t" \
  "ds_read_b64_tr_b16 a[252:253], v201 offset:26112\n\t" \
  "v_mfma_f32_16x16x32_bf16 v[88:91], a[208:211], v[176:179], v[88:91]\n\t" \
  "v_mfma_f32_16x16x32_bf16 v[92:95], a[212:215], v[176:179], v[92:95]\n\t" \
  "ds_read_b64_tr_b16 a[254:255], v201 offset:26368\n\t" \
  "v_mfma_f32_16x16x32_bf16 v[96:99], a[216:219], v[176:179], v[96:99]\n\t" \
  "v_mfma_f32_16x16x32_bf16 v[100:103], a[220:223], v[176:179], v[100:103]\n\t" \
  "v_mfma_f32_16x16x32_bf16 v[104:107], a[192:195], v[180:183], v[104:107]\n\t" \
  "v_mfma_f32_16x16x32_bf16 v[108:111], a[196:199], v[180:183], v[108:111]\n\t" \
  "v_mfma_f32_16x16x32_bf16 v[112:115], a[200:203], v[180:183], v[112:115]\n\t" \
  "v_mfma_f32_16x16x32_bf16 v[116:119], a[204:207], v[180:183], v[116:119]\n\t" \
  "v_mfma_f32_16x16x32_bf16 v[120:123], a[208:211], v[180:183], v[120:123]\n\t" \
  "v_mfma_f32_16x16x32_bf16 v[124:127], a[212:215], v[180:183], v[124:127]\n\t" \
  "v_mfma_f32_16x16x32_bf16 v[128:131], a[216:219], v[180:183], v[128:131]\n\t" \
  "v_mfma_f32_16x16x32_bf16 v[132:135], a[220:223], v[180:183], v[132:135]\n\t" \
  "s_waitcnt lgkmcnt(4)\n\t" \
  "v_mfma_f32_16x16x32_bf16 v[8:11], a[224:227], v[184:187], v[8:11]\n\t" \
  "s_waitcnt vmcnt(12)\n\t" \
  "s_barrier\n\t" \
  "ds_read_b128 a[160:163], v220 offset:0\n\t" \
  "v_mfma_f32_16x16x32_bf16 v[12:15], a[228:231], v[184:187], v[12:15]\n\t" \
  "ds_read_b128 a[164:167], v221 offset:0\n\t" \
  "v_mfma_f32_16x16x32_bf16 v[16:19], a[232:235], v[184:187], v[16:19]\n\t" \
  "ds_read_b128 a[168:171], v222 offset:0\n\t" \
  "v_mfma_f32_16x16x32_bf16 v[20:23], a[236:239], v[184:187], v[20:23]\n\t" \
  "ds_read_b128 a[172:175], v223 offset:0\n\t" \
  "v_mfma_f32_16x16x32_bf16 v[24:27], a[240:243], v[184:187], v[24:27]\n\t" \
  "ds_read_b128 a[176:179], v220 offset:4096\n\t" \
  "v_mfma_f32_16x16x32_bf16 v[28:31], a[244:247], v[184:187], v[28:31]\n\t" \
  "ds_read_b128 a[180:183], v221 offset:4096\n\t" \
  "s_waitcnt lgkmcnt(6)\n\t" \
  "v_mfma_f32_16x16x32_bf16 v[32:35], a[248:251], v[184:187], v[32:35]\n\t" \
  "ds_read_b128 a[184:187], v222 offset:4096\n\t" \
  "v_mfma_f32_16x16x32_bf16 v[36:39], a[252:255], v[184:187], v[36:39]\n\t" \
  "ds_read_b128 a[188:191], v223 offset:4096\n\t" \
  "v_mfma_f32_16x16x32_bf16 v[40:43], a[224:227], v[188:191], v[40:43]\n\t" \
  "ds_read_b64_tr_b16 v[248:249], v214 offset:0\n\t" \
  "v_mfma_f32_16x16x32_bf16 v[44:47], a[228:231], v[188:191], v[44:47]\n\t" \
  "ds_read_b64_tr_b16 v[250:251], v214 offset:2048\n\t" \
  "v_mfma_f32_16x16x32_bf16 v[48:51], a[232:235], v[188:191], v[48:51]\n\t" \
  "s_add_u32 s85, %[w], 0\n\t" \
  "s_mul_i32 s86, s85, 52\n\t" \
  "s_lshr_b32 s86, s86, 8\n\t" \
  "s_mul_i32 s86, s86, 650240\n\t" \
  "s_lshl_b32 s85, s85, 10\n\t" \
  "s_add_u32 s85, s85, s86\n\t" \
  "s_add_u32 s89, s85, s87\n\t" \
  "s_waitcnt lgkmcnt(0)\n\t" \
  "buffer_store_dwordx4 v[248:251], v202, s[56:59], s89 offen sc1\n\t" \
  "v_mfma_f32_16x16x32_bf16 v[52:55], a[236:239], v[188:191], v[52:55]\n\t" \
  "ds_read_b64_tr_b16 v[252:253], v214 offset:4096\n\t" \
  "v_mfma_f32_16x16x32_bf16 v[56:59], a[240:243], v[188:191], v[56:59]\n\t" \
  "ds_read_b64_tr_b16 v[254:255], v214 offset:6144\n\t" \
  "v_cvt_pk_bf16_f32 v136, v8, v9\n\t" \
  "v_cvt_pk_bf16_f32 v137, v10, v11\n\t" \
  "v_mfma_f32_16x16x32_bf16 v[60:63], a[244:247], v[188:191], v[60:63]\n\t" \
  "s_add_u32 s85, %[w], 4\n\t" \
  "s_mul_i32 s86, s85, 52\n\t" \
  "s_lshr_b32 s86, s86, 8\n\t" \
  "s_mul_i32 s86, s86, 650240\n\t" \
  "s_lshl_b32 s85, s85, 10\n\t" \
  "s_add_u32 s85, s85, s86\n\t" \
  "s_add_u32 s89, s85, s87\n\t" \
  "s_waitcnt lgkmcnt(0)\n\t" \
  "buffer_store_dwordx4 v[252:255], v202, s[56:59], s89 offen sc1\n\t" \
  "v_cvt_pk_bf16_f32 v138, v12, v13\n\t" \
  "v_cvt_pk_bf16_f32 v139, v14, v15\n\t" \
  "v_mfma_f32_16x16x32_bf16 v[64:67], a[248:251], v[188:191], v[64:67]\n\t" \
  "ds_read_b64_tr_b16 v[240:241], v214 offset:8192\n\t" \
  "v_cvt_pk_bf16_f32 v140, v16, v17\n\t" \
  "v_cvt_pk_bf16_f32 v141, v18, v19\n\t" \
  "v_mfma_f32_16x16x32_bf16 v[68:71], a[252:255], v[188:191], v[68:71]\n\t" \
  "ds_read_b64_tr_b16 v[242:243], v214 offset:10240\n\t" \
  "v_cvt_pk_bf16_f32 v142, v20, v21\n\t" \
  "v_cvt_pk_bf16_f32 v143, v22, v23\n\t" \
  "v_mfma_f32_16x16x32_bf16 v[72:75], a[224:227], v[192:195], v[72:75]\n\t" \
  "s_add_u32 s85, %[w], 8\n\t" \
  "s_mul_i32 s86, s85, 52\n\t" \
  "s_lshr_b32 s86, s86, 8\n\t" \
  "s_mul_i32 s86, s86, 650240\n\t" \
  "s_lshl_b32 s85, s85, 10\n\t" \
  "s_add_u32 s85, s85, s86\n\t" \
  "s_add_u32 s89, s85, s87\n\t" \
  "s_waitcnt lgkmcnt(0)\n\t" \
  "buffer_store_dwordx4 v[240:243], v202, s[56:59], s89 offen sc1\n\t" \
  "v_permlane16_swap_b32 v136, v138\n\t" \
  "v_mfma_f32_16x16x32_bf16 v[76:79], a[228:231], v[192:195], v[76:79]\n\t" \
  "ds_read_b64_tr_b16 v[244:245], v214 offset:12288\n\t" \
  "v_permlane16_swap_b32 v137, v139\n\t" \
  "v_cvt_pk_bf16_f32 v144, v24, v25\n\t" \
  "v_mfma_f32_16x16x32_bf16 v[80:83], a[232:235], v[192:195], v[80:83]\n\t" \
  "ds_read_b64_tr_b16 v[246:247], v214 offset:14336\n\t" \
  "v_cvt_pk_bf16_f32 v145, v26, v27\n\t" \
  "v_cvt_pk_bf16_f32 v146, v28, v29\n\t" \
  "v_mfma_f32_16x16x32_bf16 v[84:87], a[236:239], v[192:195], v[84:87]\n\t" \
  "s_add_u32 s85, %[w], 12\n\t" \
  "s_mul_i32 s86, s85, 52\n\t" \
  "s_lshr_b32 s86, s86, 8\n\t" \
  "s_mul_i32 s86, s86, 650240\n\t" \
  "s_lshl_b32 s85, s85, 10\n\t" \
  "s_add_u32 s85, s85, s86\n\t" \
  "s_add_u32 s89, s85, s87\n\t" \
  "s_waitcnt lgkmcnt(0)\n\t" \
  "buffer_store_dwordx4 v[244:247], v202, s[56:59], s89 offen sc1\n\t" \
  "v_cvt_pk_bf16_f32 v147, v30, v31\n\t" \
  "v_permlane16_swap_b32 v140, v142\n\t" \
  "v_mfma_f32_16x16x32_bf16 v[88:91], a[240:243], v[192:195], v[88:91]\n\t" \
  "ds_read_b64_tr_b16 v[216:217], v214 offset:16384\n\t" \
  "v_permlane16_swap_b32 v141, v143\n\t" \
  "v_cvt_pk_bf16_f32 v148, v32, v33\n\t" \
  "v_mfma_f32_16x16x32_bf16 v[92:95], a[244:247], v[192:195], v[92:95]\n\t" \
  "ds_read_b64_tr_b16 v[218:219], v214 offset:18432\n\t" \
  "v_cvt_pk_bf16_f32 v149, v34, v35\n\t" \
  "v_mfma_f32_16x16x32_bf16 v[96:99], a[248:251], v[192:195], v[96:99]\n\t" \
  "s_add_u32 s85, %[w], 16\n\t" \
  "s_mul_i32 s86, s85, 52\n\t" \
  "s_lshr_b32 s86, s86, 8\n\t" \
  "s_mul_i32 s86, s86, 650240\n\t" \
  "s_lshl_b32 s85, s85, 10\n\t" \
  "s_add_u32 s85, s85, s86\n\t" \
  "s_add_u32 s89, s85, s87\n\t" \
  "s_waitcnt lgkmcnt(0)\n\t" \
  "buffer_store_dwordx4 v[216:219], v202, s[56:59], s89 offen sc1\n\t" \
  "v_cvt_pk_bf16_f32 v150, v36, v37\n\t" \
  "v_cvt_pk_bf16_f32 v151, v38, v39\n\t" \
  "v_mfma_f32_16x16x32_bf16 v[100:103], a[252:255], v[192:195], v[100:103]\n\t" \
  "v_permlane16_swap_b32 v144, v146\n\t" \
  "v_permlane16_swap_b32 v145, v147\n\t" \
  "v_mfma_f32_16x16x32_bf16 v[104:107], a[224:227], v[196:199], v[104:107]\n\t" \
  "v_permlane16_swap_b32 v148, v150\n\t" \
  "v_permlane16_swap_b32 v149, v151\n\t" \
  "v_mfma_f32_16x16x32_bf16 v[108:111], a[228:231], v[196:199], v[108:111]\n\t" \
  "v_cvt_pk_bf16_f32 v152, v40, v41\n\t" \
  "v_cvt_pk_bf16_f32 v153, v42, v43\n\t" \
  "v_mfma_f32_16x16x32_bf16 v[112:115], a[232:235], v[196:199], v[112:115]\n\t" \
  "v_cvt_pk_bf16_f32 v154, v44, v45\n\t" \
  "v_mfma_f32_16x16x32_bf16 v[116:119], a[236:239], v[196:199], v[116:119]\n\t" \
  "v_cvt_pk_bf16_f32 v155, v46, v47\n\t" \
  "v_cvt_pk_bf16_f32 v156, v48, v49\n\t" \
  "v_mfma_f32_16x16x32_bf16 v[120:123], a[240:243], v[196:199], v[120:123]\n\t" \
  "v_cvt_pk_bf16_f32 v157, v50, v51\n\t" \
  "v_cvt_pk_bf16_f32 v158, v52, v53\n\t" \
  "v_mfma_f32_16x16x32_bf16 v[124:127], a[244:247], v[196:199], v[124:127]\n\t" \
  "v_cvt_pk_bf16_f32 v159, v54, v55\n\t" \
  "v_permlane16_swap_b32 v152, v154\n\t" \
  "v_mfma_f32_16x16x32_bf16 v[128:131], a[248:251], v[196:199], v[128:131]\n\t" \
  "v_permlane16_swap_b32 v153, v155\n\t" \
  "v_cvt_pk_bf16_f32 v160, v56, v57\n\t" \
  "v_mfma_f32_16x16x32_bf16 v[132:135], a[252:255], v[196:199], v[132:135]\n\t" \
  "v_cvt_pk_bf16_f32 v161, v58, v59\n\t" \
  "v_mfma_f32_16x16x32_bf16 v[8:11], a[160:163], v[136:139], 0\n\t" \
  "v_cvt_pk_bf16_f32 v162, v60, v61\n\t" \
  "v_cvt_pk_bf16_f32 v163, v62, v63\n\t" \
  "v_mfma_f32_16x16x32_bf16 v[8:11], a[164:167], v[140:143], v[8:11]\n\t" \
  "v_permlane16_swap_b32 v156, v158\n\t" \
  "v_permlane16_swap_b32 v157, v159\n\t" \
  "v_mfma_f32_16x16x32_bf16 v[8:11], a[168:171], v[144:147], v[8:11]\n\t" \
  "v_cvt_pk_bf16_f32 v164, v64, v65\n\t" \
  "v_cvt_pk_bf16_f32 v165, v66, v67\n\t" \
  "v_mfma_f32_16x16x32_bf16 v[8:11], a[172:175], v[148:151], v[8:11]\n\t" \
  "v_cvt_pk_bf16_f32 v166, v68, v69\n\t" \
  "v_cvt_pk_bf16_f32 v167, v70, v71\n\t" \
  "v_mfma_f32_16x16x32_bf16 v[12:15], a[176:179], v[136:139], 0\n\t" \
  "v_permlane16_swap_b32 v160, v162\n\t" \
  "v_permlane16_swap_b32 v161, v163\n\t" \
  "v_mfma_f32_16x16x32_bf16 v[12:15], a[180:183], v[140:143], v[12:15]\n\t" \
  "v_permlane16_swap_b32 v164, v166\n\t" \
  "v_permlane16_swap_b32 v165, v167\n\t" \
  "v_mfma_f32_16x16x32_bf16 v[12:15], a[184:187], v[144:147], v[12:15]\n\t" \
  "v_cvt_pk_bf16_f32 v168, v72, v73\n\t" \
  "v_cvt_pk_bf16_f32 v169, v74, v75\n\t" \
  "v_cvt_pk_bf16_f32 v170, v76, v77\n\t" \
  "v_mfma_f32_16x16x32_bf16 v[12:15], a[188:191], v[148:151], v[12:15]\n\t" \
  "v_cvt_pk_bf16_f32 v171, v78, v79\n\t" \
  "v_cvt_pk_bf16_f32 v172, v80, v81\n\t" \
  "v_cvt_pk_bf16_f32 v173, v82, v83\n\t" \
  "v_mfma_f32_16x16x32_bf16 v[16:19], a[160:163], v[152:155], 0\n\t" \
  "v_cvt_pk_bf16_f32 v174, v84, v85\n\t" \
  "v_cvt_pk_bf16_f32 v175, v86, v87\n\t" \
  "v_mfma_f32_16x16x32_bf16 v[16:19], a[164:167], v[156:159], v[16:19]\n\t" \
  "v_permlane16_swap_b32 v168, v170\n\t" \
  "v_permlane16_swap_b32 v169, v171\n\t" \
  "v_cvt_pk_bf16_f32 v176, v88, v89\n\t" \
  "v_mfma_f32_16x16x32_bf16 v[16:19], a[168:171], v[160:163], v[16:19]\n\t" \
  "v_cvt_pk_bf16_f32 v177, v90, v91\n\t" \
  "v_cvt_pk_bf16_f32 v178, v92, v93\n\t" \
  "v_cvt_pk_bf16_f32 v179, v94, v95\n\t" \
  "v_mfma_f32_16x16x32_bf16 v[16:19], a[172:175], v[164:167], v[16:19]\n\t" \
  "v_permlane16_swap_b32 v172, v174\n\t" \
  "v_permlane16_swap_b32 v173, v175\n\t" \
  "v_mfma_f32_16x16x32_bf16 v[20:23], a[176:179], v[152:155], 0\n\t" \
  "v_cvt_pk_bf16_f32 v180, v96, v97\n\t" \
  "v_cvt_pk_bf16_f32 v181, v98, v99\n\t" \
  "v_cvt_pk_bf16_f32 v182, v100, v101\n\t" \
  "v_mfma_f32_16x16x32_bf16 v[20:23], a[180:183], v[156:159], v[20:23]\n\t" \
  "v_cvt_pk_bf16_f32 v183, v102, v103\n\t" \
  "v_permlane16_swap_b32 v176, v178\n\t" \
  "v_permlane16_swap_b32 v177, v179\n\t" \
  "v_mfma_f32_16x16x32_bf16 v[20:23], a[184:187], v[160:163], v[20:23]\n\t" \
  "v_permlane16_swap_b32 v180, v182\n\t" \
  "v_permlane16_swap_b32 v181, v183\n\t" \
  "v_mfma_f32_16x16x32_bf16 v[20:23], a[188:191], v[164:167], v[20:23]\n\t" \
  "v_cvt_pk_bf16_f32 v184, v104, v105\n\t" \
  "v_cvt_pk_bf16_f32 v185, v106, v107\n\t" \
  "v_cvt_pk_bf16_f32 v186, v108, v109\n\t" \
  "v_mfma_f32_16x16x32_bf16 v[24:27], a[160:163], v[168:171], 0\n\t" \
  "v_cvt_pk_bf16_f32 v187, v110, v111\n\t" \
  "v_cvt_pk_bf16_f32 v188, v112, v113\n\t" \
  "v_cvt_pk_bf16_f32 v189, v114, v115\n\t" \
  "v_mfma_f32_16x16x32_bf16 v[24:27], a[164:167], v[172:175], v[24:27]\n\t" \
  "v_cvt_pk_bf16_f32 v190, v116, v117\n\t" \
  "v_cvt_pk_bf16_f32 v191, v118, v119\n\t" \
  "v_permlane16_swap_b32 v184, v186\n\t" \
  "v_mfma_f32_16x16x32_bf16 v[24:27], a[168:171], v[176:179], v[24:27]\n\t" \
  "v_permlane16_swap_b32 v185, v187\n\t" \
  "v_cvt_pk_bf16_f32 v192, v120, v121\n\t" \
  "v_cvt_pk_bf16_f32 v193, v122, v123\n\t" \
  "v_mfma_f32_16x16x32_bf16 v[24:27], a[172:175], v[180:183], v[24:27]\n\t" \
  "v_cvt_pk_bf16_f32 v194, v124, v125\n\t" \
  "v_cvt_pk_bf16_f32 v195, v126, v127\n\t" \
  "v_permlane16_swap_b32 v188, v190\n\t" \
  "v_mfma_f32_16x16x32_bf16 v[28:31], a[176:179], v[168:171], 0\n\t" \
  "v_permlane16_swap_b32 v189, v191\n\t" \
  "v_cvt_pk_bf16_f32 v196, v128, v129\n\t" \
  "v_cvt_pk_bf16_f32 v197, v130, v131\n\t" \
  "v_mfma_f32_16x16x32_bf16 v[28:31], a[180:183], v[172:175], v[28:31]\n\t" \
  "v_cvt_pk_bf16_f32 v198, v132, v133\n\t" \
  "v_cvt_pk_bf16_f32 v199, v134, v135\n\t" \
  "v_permlane16_swap_b32 v192, v194\n\t" \
  "v_mfma_f32_16x16x32_bf16 v[28:31], a[184:187], v[176:179], v[28:31]\n\t" \
  "v_permlane16_swap_b32 v193, v195\n\t" \
  "v_permlane16_swap_b32 v196, v198\n\t" \
  "v_permlane16_swap_b32 v197, v199\n\t" \
  "v_mfma_f32_16x16x32_bf16 v[28:31], a[188:191], v[180:183], v[28:31]\n\t" \
  "s_mov_b32 s81, 0\n\t" \
  "v_mov_b32 v200, v215\n\t" \
  "v_xor_b32 v201, 32, v215\n\t" \
  "v_mfma_f32_16x16x32_bf16 v[32:35], a[160:163], v[184:187], 0\n\t" \
  "s_mov_b32 s83, 0x8000\n\t" \
  "ds_read_b128 a[128:131], v220 offset:8192\n\t" \
  "v_max3_f32 v232, v8, v9, v10\n\t" \
  "v_max3_f32 v234, v16, v17, v18\n\t" \
  "v_mfma_f32_16x16x32_bf16 v[32:35], a[164:167], v[188:191], v[32:35]\n\t" \
  "ds_read_b128 a[132:135], v221 offset:8192\n\t" \
  "v_max3_f32 v233, v11, v12, v13\n\t" \
  "v_max3_f32 v235, v19, v20, v21\n\t" \
  "v_mfma_f32_16x16x32_bf16 v[32:35], a[168:171], v[192:195], v[32:35]\n\t" \
  "ds_read_b128 a[136:139], v222 offset:8192\n\t" \
  "v_max3_f32 v232, v232, v14, v15\n\t" \
  "v_max3_f32 v234, v234, v22, v23\n\t" \
  "v_max_f32 v232, v232, v233\n\t" \
  "v_mfma_f32_16x16x32_bf16 v[32:35], a[172:175], v[196:199], v[32:35]\n\t" \
  "ds_read_b128 a[140:143], v223 offset:8192\n\t" \
  "v_max_f32 v234, v234, v235\n\t" \
  "v_mov_b32 v233, v232\n\t" \
  "v_mov_b32 v235, v234\n\t" \
  "v_mfma_f32_16x16x32_bf16 v[36:39], a[176:179], v[184:187], 0\n\t" \
  "ds_read_b128 a[144:147], v220 offset:12288\n\t" \
  "v_permlane16_swap_b32 v232, v233\n\t" \
  "v_permlane16_swap_b32 v234, v235\n\t" \
  "v_mfma_f32_16x16x32_bf16 v[36:39], a[180:183], v[188:191], v[36:39]\n\t" \
  "ds_read_b128 a[148:151], v221 offset:12288\n\t" \
  "v_max_f32 v232, v232, v233\n\t" \
  "v_max_f32 v234, v234, v235\n\t" \
  "v_mov_b32 v233, v232\n\t" \
  "v_mfma_f32_16x16x32_bf16 v[36:39], a[184:187], v[192:195], v[36:39]\n\t" \
  "ds_read_b128 a[152:155], v222 offset:12288\n\t" \
  "v_mov_b32 v235, v234\n\t" \
  "v_permlane32_swap_b32 v232, v233\n\t" \
  "s_nop 0\n\t" \
  "v_permlane32_swap_b32 v234, v235\n\t" \
  "v_mfma_f32_16x16x32_bf16 v[36:39], a[188:191], v[196:199], v[36:39]\n\t" \
  "ds_read_b128 a[156:159], v223 offset:12288\n\t" \
  "v_max_f32 v232, v232, v233\n\t" \
  "v_max_f32 v234, v234, v235\n\t" \
  "v_sub_f32 v232, 0xc2000000, v232\n\t" \
  "v_mov_b32 v120, 0\n\t" \
  "v_sub_f32 v234, 0xc2000000, v234\n\t" \
  "v_mov_b32 v121, 0\n\t" \
  "v_add_f32 v8, v232, v8\n\t" \
  "v_mov_b32 v122, 0\n\t" \
  "v_add_f32 v16, v234, v16\n\t" \
  "v_mov_b32 v123, 0\n\t" \
  "v_add_f32 v9, v232, v9\n\t" \
  "v_mov_b32 v124, 0\n\t" \
  "v_add_f32 v17, v234, v17\n\t" \
  "v_mov_b32 v125, 0\n\t" \
  "v_add_f32 v10, v232, v10\n\t" \
  "v_mov_b32 v126, 0\n\t" \
  "v_add_f32 v18, v234, v18\n\t" \
  "v_mov_b32 v127, 0\n\t" \
  "v_add_f32 v11, v232, v11\n\t" \
  "v_mov_b32 v128, 0\n\t" \
  "v_add_f32 v19, v234, v19\n\t" \
  "v_mov_b32 v129, 0\n\t" \
  "v_add_f32 v12, v232, v12\n\t" \
  "v_mov_b32 v130, 0\n\t" \
  "v_add_f32 v20, v234, v20\n\t" \
  "v_mov_b32 v131, 0\n\t" \
  "v_add_f32 v13, v232, v13\n\t" \
  "v_mov_b32 v132, 0\n\t" \
  "v_add_f32 v21, v234, v21\n\t" \
  "v_mov_b32 v133, 0\n\t" \
  "v_add_f32 v14, v232, v14\n\t" \
  "v_mov_b32 v134, 0\n\t" \
  "v_add_f32 v22, v234, v22\n\t" \
  "v_mov_b32 v135, 0\n\t" \
  "v_add_f32 v15, v232, v15\n\t" \
  "v_add_f32 v23, v234, v23\n\t" \
  "v_mov_b32 v72, v232\n\t" \
  "v_mov_b32 v76, v234\n\t" \
  "v_mov_b32 v73, v232\n\t" \
  "v_mov_b32 v77, v234\n\t" \
  "v_mov_b32 v74, v232\n\t" \
  "v_mov_b32 v78, v234\n\t" \
  "v_mov_b32 v75, v232\n\t" \
  "v_mov_b32 v79, v234\n\t" \
  "v_max3_f32 v236, v24, v25, v26\n\t" \
  "v_max3_f32 v238, v32, v33, v34\n\t" \
  "v_exp_f32 v8, v8\n\t" \
  "v_max3_f32 v237, v27, v28, v29\n\t" \
  "v_max3_f32 v239, v35, v36, v37\n\t" \
  "v_exp_f32 v9, v9\n\t" \
  "v_max3_f32 v236, v236, v30, v31\n\t" \
  "v_max3_f32 v238, v238, v38, v39\n\t" \
  "v_exp_f32 v10, v10\n\t" \
  "v_max_f32 v236, v236, v237\n\t" \
  "v_max_f32 v238, v238, v239\n\t" \
  "v_cvt_pk_bf16_f32 v88, v8, v9\n\t" \
  "v_mov_b32 v237, v236\n\t" \
  "v_mov_b32 v239, v238\n\t" \
  "v_exp_f32 v11, v11\n\t" \
  "v_permlane16_swap_b32 v236, v237\n\t" \
  "v_permlane16_swap_b32 v238, v239\n\t" \
  "v_exp_f32 v12, v12\n\t" \
  "v_max_f32 v236, v236, v237\n\t" \
  "v_max_f32 v238, v238, v239\n\t" \
  "v_cvt_pk_bf16_f32 v89, v10, v11\n\t" \
  "v_mov_b32 v237, v236\n\t" \
  "v_mov_b32 v239, v238\n\t" \
  "v_exp_f32 v13, v13\n\t" \
  "v_permlane32_swap_b32 v236, v237\n\t" \
  "v_permlane32_swap_b32 v238, v239\n\t" \
  "v_exp_f32 v14, v14\n\t" \
  "v_max_f32 v236, v236, v237\n\t" \
  "v_max_f32 v238, v238, v239\n\t" \
  "v_cvt_pk_bf16_f32 v90, v12, v13\n\t" \
  "v_sub_f32 v236, 0xc2000000, v236\n\t" \
  "v_sub_f32 v238, 0xc2000000, v238\n\t" \
  "v_exp_f32 v15, v15\n\t" \
  "v_add_f32 v24, v236, v24\n\t" \
  "v_add_f32 v32, v238, v32\n\t" \
  "v_exp_f32 v16, v16\n\t" \
  "v_add_f32 v25, v236, v25\n\t" \
  "v_add_f32 v33, v238, v33\n\t" \
  "v_cvt_pk_bf16_f32 v91, v14, v15\n\t" \
  "v_add_f32 v26, v236, v26\n\t" \
  "v_add_f32 v34, v238, v34\n\t" \
  "v_exp_f32 v17, v17\n\t" \
  "v_add_f32 v27, v236, v27\n\t" \
  "v_add_f32 v35, v238, v35\n\t" \
  "v_exp_f32 v18, v18\n\t" \
  "v_add_f32 v28, v236, v28\n\t" \
  "v_add_f32 v36, v238, v36\n\t" \
  "v_cvt_pk_bf16_f32 v92, v16, v17\n\t" \
  "v_add_f32 v29, v236, v29\n\t" \
  "v_add_f32 v37, v238, v37\n\t" \
  "v_exp_f32 v19, v19\n\t" \
  "v_add_f32 v30, v236, v30\n\t" \
  "v_add_f32 v38, v238, v38\n\t" \
  "v_exp_f32 v20, v20\n\t" \
  "v_add_f32 v31, v236, v31\n\t" \
  "v_add_f32 v39, v238, v39\n\t" \
  "v_cvt_pk_bf16_f32 v93, v18, v19\n\t" \
  "v_mov_b32 v80, v236\n\t" \
  "v_mov_b32 v84, v238\n\t" \
  "v_exp_f32 v21, v21\n\t" \
  "v_mov_b32 v81, v236\n\t" \
  "v_mov_b32 v85, v238\n\t" \
  "v_exp_f32 v22, v22\n\t" \
  "v_mov_b32 v82, v236\n\t" \
  "v_mov_b32 v86, v238\n\t" \
  "v_cvt_pk_bf16_f32 v94, v20, v21\n\t" \
  "v_mov_b32 v83, v236\n\t" \
  "v_mov_b32 v87, v238\n\t" \
  "v_exp_f32 v23, v23\n\t" \
  "s_nop 0\n\t" \
  "v_cvt_pk_bf16_f32 v95, v22, v23\n\t" \
  "s_mul_i32 s68, %[bid], 4\n\t" \
  "s_add_u32 s68, s68, 20480\n\t" \
  "s_mov_b64 s[72:73], %[ws]\n\t" \
  "s_add_u32 s68, s68, s72\n\t" \
  "s_addc_u32 s69, s73, 0\n\t" \
  "s_waitcnt vmcnt(0) lgkmcnt(0)\n\t" \
  "s_barrier\n\t" \
  "s_cmp_lg_u32 %[w], 0\n\t" \
  "s_cbranch_scc1 .Lfr%=\n\t" \
  "s_mov_b64 exec, 0x1\n\t" \
  "v_lshlrev_b32 v240, 2, v203\n\t" \
  "v_mov_b32 v241, %[tag]\n\t" \
  "global_store_dword v240, v241, s[68:69] sc1\n\t" \
  "s_mov_b64 exec, -1\n\t" \
  ".Lfr%=:\n\t" \
  ".Laloop%=:\n\t" \
  "s_waitcnt lgkmcnt(1)\n\t" \
  "v_mfma_f32_16x16x32_bf16 v[40:43], a[128:131], v[136:139], v[72:75]\n\t" \
  "s_waitcnt vmcnt(8)\n\t" \
  "s_barrier\n\t" \
  "v_exp_f32 v24, v24\n\t" \
  "v_mfma_f32_16x16x32_bf16 v[40:43], a[132:135], v[140:143], v[40:43]\n\t" \
  "ds_read_b64_tr_b16 a[160:161], v200 offset:0\n\t" \
  "v_mfma_f32_16x16x32_bf16 v[40:43], a[136:139], v[144:147], v[40:43]\n\t" \
  "s_add_u32 s84, s83, 0x8000\n\t" \
  "v_exp_f32 v25, v25\n\t" \
  "v_mfma_f32_16x16x32_bf16 v[40:43], a[140:143], v[148:151], v[40:43]\n\t" \
  "ds_read_b64_tr_b16 a[162:163], v200 offset:4096\n\t" \
  "v_cvt_pk_bf16_f32 v96, v24, v25\n\t" \
  "v_mfma_f32_16x16x32_bf16 v[44:47], a[144:147], v[136:139], v[72:75]\n\t" \
  "s_and_b32 s84, s84, 0x1ffff\n\t" \
  "v_exp_f32 v26, v26\n\t" \
  "v_mfma_f32_16x16x32_bf16 v[44:47], a[148:151], v[140:143], v[44:47]\n\t" \
  "ds_read_b64_tr_b16 a[164:165], v201 offset:0\n\t" \
  "v_mfma_f32_16x16x32_bf16 v[44:47], a[152:155], v[144:147], v[44:47]\n\t" \
  "s_sub_u32 s82, s84, s83\n\t" \
  "v_exp_f32 v27, v27\n\t" \
  "s_waitcnt lgkmcnt(3)\n\t" \
  "v_mfma_f32_16x16x32_bf16 v[44:47], a[156:159], v[148:151], v[44:47]\n\t" \
  "ds_read_b64_tr_b16 a[166:167], v201 offset:4096\n\t" \
  "v_cvt_pk_bf16_f32 v97, v26, v27\n\t" \
  "v_mfma_f32_16x16x32_bf16 v[48:51], a[128:131], v[152:155], v[76:79]\n\t" \
  "s_mov_b32 s83, s84\n\t" \
  "v_exp_f32 v28, v28\n\t" \
  "v_mfma_f32_16x16x32_bf16 v[48:51], a[132:135], v[156:159], v[48:51]\n\t" \
  "ds_read_b64_tr_b16 a[168:169], v200 offset:512\n\t" \
  "v_mfma_f32_16x16x32_bf16 v[48:51], a[136:139], v[160:163], v[48:51]\n\t" \
  "v_exp_f32 v29, v29\n\t" \
  "v_mfma_f32_16x16x32_bf16 v[48:51], a[140:143], v[164:167], v[48:51]\n\t" \
  "ds_read_b64_tr_b16 a[170:171], v200 offset:4608\n\t" \
  "v_cvt_pk_bf16_f32 v98, v28, v29\n\t" \
  "v_mfma_f32_16x16x32_bf16 v[52:55], a[144:147], v[152:155], v[76:79]\n\t" \
  "v_exp_f32 v30, v30\n\t" \
  "v_mfma_f32_16x16x32_bf16 v[52:55], a[148:151], v[156:159], v[52:55]\n\t" \
  "ds_read_b64_tr_b16 a[172:173], v201 offset:512\n\t" \
  "v_add_u32 v220, s82, v220\n\t" \
  "v_mfma_f32_16x16x32_bf16 v[52:55], a[152:155], v[160:163], v[52:55]\n\t" \
  "v_exp_f32 v31, v31\n\t" \
  "v_mfma_f32_16x16x32_bf16 v[52:55], a[156:159], v[164:167], v[52:55]\n\t" \
  "ds_read_b64_tr_b16 a[174:175], v201 offset:4608\n\t" \
  "v_cvt_pk_bf16_f32 v99, v30, v31\n\t" \
  "v_mfma_f32_16x16x32_bf16 v[56:59], a[128:131], v[168:171], v[80:83]\n\t" \
  "v_exp_f32 v32, v32\n\t" \
  "v_mfma_f32_16x16x32_bf16 v[56:59], a[132:135], v[172:175], v[56:59]\n\t" \
  "ds_read_b64_tr_b16 a[176:177], v200 offset:1024\n\t" \
  "v_add_u32 v221, s82, v221\n\t" \
  "v_mfma_f32_16x16x32_bf16 v[56:59], a[136:139], v[176:179], v[56:59]\n\t" \
  "v_exp_f32 v33, v33\n\t" \
  "v_mfma_f32_16x16x32_bf16 v[56:59], a[140:143], v[180:183], v[56:59]\n\t" \
  "ds_read_b64_tr_b16 a[178:179], v200 offset:5120\n\t" \
  "v_cvt_pk_bf16_f32 v100, v32, v33\n\t" \
  "v_mfma_f32_16x16x32_bf16 v[60:63], a[144:147], v[168:171], v[80:83]\n\t" \
  "v_exp_f32 v34, v34\n\t" \
  "v_mfma_f32_16x16x32_bf16 v[60:63], a[148:151], v[172:175], v[60:63]\n\t" \
  "ds_read_b64_tr_b16 a[180:181], v201 offset:1024\n\t" \
  "v_add_u32 v222, s82, v222\n\t" \
  "v_mfma_f32_16x16x32_bf16 v[60:63], a[152:155], v[176:179], v[60:63]\n\t" \
  "v_exp_f32 v35, v35\n\t" \
  "v_mfma_f32_16x16x32_bf16 v[60:63], a[156:159], v[180:183], v[60:63]\n\t" \
  "ds_read_b64_tr_b16 a[182:183], v201 offset:5120\n\t" \
  "v_cvt_pk_bf16_f32 v101, v34, v35\n\t" \
  "v_mfma_f32_16x16x32_bf16 v[64:67], a[128:131], v[184:187], v[84:87]\n\t" \
  "v_exp_f32 v36, v36\n\t" \
  "v_mfma_f32_16x16x32_bf16 v[64:67], a[132:135], v[188:191], v[64:67]\n\t" \
  "ds_read_b64_tr_b16 a[184:185], v200 offset:1536\n\t" \
  "v_add_u32 v223, s82, v223\n\t" \
  "v_mfma_f32_16x16x32_bf16 v[64:67], a[136:139], v[192:195], v[64:67]\n\t" \
  "v_exp_f32 v37, v37\n\t" \
  "v_mfma_f32_16x16x32_bf16 v[64:67], a[140:143], v[196:199], v[64:67]\n\t" \
  "ds_read_b64_tr_b16 a[186:187], v200 offset:5632\n\t" \
  "v_cvt_pk_bf16_f32 v102, v36, v37\n\t" \
  "v_mfma_f32_16x16x32_bf16 v[68:71], a[144:147], v[184:187], v[84:87]\n\t" \
  "v_exp_f32 v38, v38\n\t" \
  "v_mfma_f32_16x16x32_bf16 v[68:71], a[148:151], v[188:191], v[68:71]\n\t" \
  "ds_read_b64_tr_b16 a[188:189], v201 offset:1536\n\t" \
  "v_mfma_f32_16x16x32_bf16 v[68:71], a[152:155], v[192:195], v[68:71]\n\t" \
  "v_exp_f32 v39, v39\n\t" \
  "v_mfma_f32_16x16x32_bf16 v[68:71], a[156:159], v[196:199], v[68:71]\n\t" \
  "ds_read_b64_tr_b16 a[190:191], v201 offset:5632\n\t" \
  "v_cvt_pk_bf16_f32 v103, v38, v39\n\t" \
  "s_waitcnt lgkmcnt(4)\n\t" \
  "v_mfma_f32_16x16x32_bf16 a[0:3], a[160:163], v[88:91], a[0:3]\n\t" \
  "v_exp_f32 v40, v40\n\t" \
  "v_mfma_f32_16x16x32_bf16 a[4:7], a[164:167], v[88:91], a[4:7]\n\t" \
  "ds_read_b128 a[128:131], v220 offset:0\n\t" \
  "v_mfma_f32_16x16x32_bf16 a[8:11], a[168:171], v[88:91], a[8:11]\n\t" \
  "v_exp_f32 v41, v41\n\t" \
  "v_mfma_f32_16x16x32_bf16 a[12:15], a[172:175], v[88:91], a[12:15]\n\t" \
  "ds_read_b128 a[132:135], v221 offset:0\n\t" \
  "v_cvt_pk_bf16_f32 v104, v40, v41\n\t" \
  "v_mfma_f32_16x16x32_bf16 a[16:19], a[176:179], v[88:91], a[16:19]\n\t" \
  "s_mov_b32 m0, s80\n\t" \
  "s_add_u32 s80, s80, 0x8000\n\t" \
  "v_exp_f32 v42, v42\n\t" \
  "v_mfma_f32_16x16x32_bf16 a[20:23], a[180:183], v[88:91], a[20:23]\n\t" \
  "buffer_load_dwordx4 v208, s[40:43], s75 offen offset:0 lds\n\t" \
  "s_waitcnt lgkmcnt(3)\n\t" \
  "v_mfma_f32_16x16x32_bf16 a[24:27], a[184:187], v[88:91], a[24:27]\n\t" \
  "v_exp_f32 v43, v43\n\t" \
  "s_waitcnt lgkmcnt(2)\n\t" \
  "v_mfma_f32_16x16x32_bf16 a[28:31], a[188:191], v[88:91], a[28:31]\n\t" \
  "ds_read_b128 a[136:139], v222 offset:0\n\t" \
  "v_cvt_pk_bf16_f32 v105, v42, v43\n\t" \
  "v_mfma_f32_16x16x32_bf16 v[120:123], v[224:227], v[88:91], v[120:123]\n\t" \
  "v_exp_f32 v44, v44\n\t" \
  "v_mfma_f32_16x16x32_bf16 a[32:35], a[160:163], v[92:95], a[32:35]\n\t" \
  "v_mfma_f32_16x16x32_bf16 a[36:39], a[164:167], v[92:95], a[36:39]\n\t" \
  "v_exp_f32 v45, v45\n\t" \
  "v_mfma_f32_16x16x32_bf16 a[40:43], a[168:171], v[92:95], a[40:43]\n\t" \
  "ds_read_b128 a[140:143], v223 offset:0\n\t" \
  "v_cvt_pk_bf16_f32 v106, v44, v45\n\t" \
  "v_mfma_f32_16x16x32_bf16 a[44:47], a[172:175], v[92:95], a[44:47]\n\t" \
  "s_and_b32 s80, s80, 0x1ffff\n\t" \
  "v_exp_f32 v46, v46\n\t" \
  "v_mfma_f32_16x16x32_bf16 a[48:51], a[176:179], v[92:95], a[48:51]\n\t" \
  "buffer_load_dwordx4 v209, s[40:43], s75 offen offset:1024 lds\n\t" \
  "v_mfma_f32_16x16x32_bf16 a[52:55], a[180:183], v[92:95], a[52:55]\n\t" \
  "v_exp_f32 v47, v47\n\t" \
  "v_mfma_f32_16x16x32_bf16 a[56:59], a[184:187], v[92:95], a[56:59]\n\t" \
  "ds_read_b128 a[144:147], v220 offset:4096\n\t" \
  "v_cvt_pk_bf16_f32 v107, v46, v47\n\t" \
  "v_mfma_f32_16x16x32_bf16 a[60:63], a[188:191], v[92:95], a[60:63]\n\t" \
  "v_exp_f32 v48, v48\n\t" \
  "v_mfma_f32_16x16x32_bf16 v[124:127], v[224:227], v[92:95], v[124:127]\n\t" \
  "v_mfma_f32_16x16x32_bf16 a[64:67], a[160:163], v[96:99], a[64:67]\n\t" \
  "v_exp_f32 v49, v49\n\t" \
  "v_mfma_f32_16x16x32_bf16 a[68:71], a[164:167], v[96:99], a[68:71]\n\t" \
  "ds_read_b128 a[148:151], v221 offset:4096\n\t" \
  "v_cvt_pk_bf16_f32 v108, v48, v49\n\t" \
  "v_mfma_f32_16x16x32_bf16 a[72:75], a[168:171], v[96:99], a[72:75]\n\t" \
  "v_exp_f32 v50, v50\n\t" \
  "v_mfma_f32_16x16x32_bf16 a[76:79], a[172:175], v[96:99], a[76:79]\n\t" \
  "buffer_load_dwordx4 v210, s[40:43], s75 offen offset:2048 lds\n\t" \
  "v_mfma_f32_16x16x32_bf16 a[80:83], a[176:179], v[96:99], a[80:83]\n\t" \
  "v_exp_f32 v51, v51\n\t" \
  "v_mfma_f32_16x16x32_bf16 a[84:87], a[180:183], v[96:99], a[84:87]\n\t" \
  "ds_read_b128 a[152:155], v222 offset:4096\n\t" \
  "v_cvt_pk_bf16_f32 v109, v50, v51\n\t" \
  "v_mfma_f32_16x16x32_bf16 a[88:91], a[184:187], v[96:99], a[88:91]\n\t" \
  "v_exp_f32 v52, v52\n\t" \
  "v_mfma_f32_16x16x32_bf16 a[92:95], a[188:191], v[96:99], a[92:95]\n\t" \
  "v_mfma_f32_16x16x32_bf16 v[128:131], v[224:227], v[96:99], v[128:131]\n\t" \
  "v_exp_f32 v53, v53\n\t" \
  "v_mfma_f32_16x16x32_bf16 a[96:99], a[160:163], v[100:103], a[96:99]\n\t" \
  "ds_read_b128 a[156:159], v223 offset:4096\n\t" \
  "v_cvt_pk_bf16_f32 v110, v52, v53\n\t" \
  "v_mfma_f32_16x16x32_bf16 a[100:103], a[164:167], v[100:103], a[100:103]\n\t" \
  "v_exp_f32 v54, v54\n\t" \
  "v_mfma_f32_16x16x32_bf16 a[104:107], a[168:171], v[100:103], a[104:107]\n\t" \
  "v_mfma_f32_16x16x32_bf16 a[108:111], a[172:175], v[100:103], a[108:111]\n\t" \
  "v_exp_f32 v55, v55\n\t" \
  "v_mfma_f32_16x16x32_bf16 a[112:115], a[176:179], v[100:103], a[112:115]\n\t" \
  "v_cvt_pk_bf16_f32 v111, v54, v55\n\t" \
  "v_mfma_f32_16x16x32_bf16 a[116:119], a[180:183], v[100:103], a[116:119]\n\t" \
  "v_mfma_f32_16x16x32_bf16 a[120:123], a[184:187], v[100:103], a[120:123]\n\t" \
  "buffer_load_dwordx4 v211, s[40:43], s75 offen offset:3072 lds\n\t" \
  "s_add_u32 s75, s75, s78\n\t" \
  "v_mfma_f32_16x16x32_bf16 a[124:127], a[188:191], v[100:103], a[124:127]\n\t" \
  "v_mfma_f32_16x16x32_bf16 v[132:135], v[224:227], v[100:103], v[132:135]\n\t" \
  "s_waitcnt lgkmcnt(1)\n\t" \
  "v_mfma_f32_16x16x32_bf16 v[8:11], a[128:131], v[136:139], v[72:75]\n\t" \
  "v_exp_f32 v56, v56\n\t" \
  "v_mfma_f32_16x16x32_bf16 v[8:11], a[132:135], v[140:143], v[8:11]\n\t" \
  "ds_read_b64_tr_b16 a[160:161], v200 offset:8192\n\t" \
  "v_mfma_f32_16x16x32_bf16 v[8:11], a[136:139], v[144:147], v[8:11]\n\t" \
  "v_exp_f32 v57, v57\n\t" \
  "v_mfma_f32_16x16x32_bf16 v[8:11], a[140:143], v[148:151], v[8:11]\n\t" \
  "ds_read_b64_tr_b16 a[162:163], v200 offset:12288\n\t" \
  "v_cvt_pk_bf16_f32 v112, v56, v57\n\t" \
  "v_mfma_f32_16x16x32_bf16 v[12:15], a[144:147], v[136:139], v[72:75]\n\t" \
  "v_exp_f32 v58, v58\n\t" \
  "v_mfma_f32_16x16x32_bf16 v[12:15], a[148:151], v[140:143], v[12:15]\n\t" \
  "ds_read_b64_tr_b16 a[164:165], v201 offset:8192\n\t" \
  "v_mfma_f32_16x16x32_bf16 v[12:15], a[152:155], v[144:147], v[12:15]\n\t" \
  "v_exp_f32 v59, v59\n\t" \
  "s_waitcnt lgkmcnt(3)\n\t" \
  "v_mfma_f32_16x16x32_bf16 v[12:15], a[156:159], v[148:151], v[12:15]\n\t" \
  "ds_read_b64_tr_b16 a[166:167], v201 offset:12288\n\t" \
  "v_cvt_pk_bf16_f32 v113, v58, v59\n\t" \
  "v_mfma_f32_16x16x32_bf16 v[16:19], a[128:131], v[152:155], v[76:79]\n\t" \
  "v_exp_f32 v60, v60\n\t" \
  "v_mfma_f32_16x16x32_bf16 v[16:19], a[132:135], v[156:159], v[16:19]\n\t" \
  "ds_read_b64_tr_b16 a[168:169], v200 offset:8704\n\t" \
  "v_mfma_f32_16x16x32_bf16 v[16:19], a[136:139], v[160:163], v[16:19]\n\t" \
  "v_exp_f32 v61, v61\n\t" \
  "v_mfma_f32_16x16x32_bf16 v[16:19], a[140:143], v[164:167], v[16:19]\n\t" \
  "ds_read_b64_tr_b16 a[170:171], v200 offset:12800\n\t" \
  "v_cvt_pk_bf16_f32 v114, v60, v61\n\t" \
  "v_mfma_f32_16x16x32_bf16 v[20:23], a[144:147], v[152:155], v[76:79]\n\t" \
  "v_exp_f32 v62, v62\n\t" \
  "v_mfma_f32_16x16x32_bf16 v[20:23], a[148:151], v[156:159], v[20:23]\n\t" \
  "ds_read_b64_tr_b16 a[172:173], v201 offset:8704\n\t" \
  "v_mfma_f32_16x16x32_bf16 v[20:23], a[152:155], v[160:163], v[20:23]\n\t" \
  "v_exp_f32 v63, v63\n\t" \
  "v_mfma_f32_16x16x32_bf16 v[20:23], a[156:159], v[164:167], v[20:23]\n\t" \
  "ds_read_b64_tr_b16 a[174:175], v201 offset:12800\n\t" \
  "v_cvt_pk_bf16_f32 v115, v62, v63\n\t" \
  "v_mfma_f32_16x16x32_bf16 v[24:27], a[128:131], v[168:171], v[80:83]\n\t" \
  "v_exp_f32 v64, v64\n\t" \
  "v_mfma_f32_16x16x32_bf16 v[24:27], a[132:135], v[172:175], v[24:27]\n\t" \
  "ds_read_b64_tr_b16 a[176:177], v200 offset:9216\n\t" \
  "v_mfma_f32_16x16x32_bf16 v[24:27], a[136:139], v[176:179], v[24:27]\n\t" \
  "v_exp_f32 v65, v65\n\t" \
  "v_mfma_f32_16x16x32_bf16 v[24:27], a[140:143], v[180:183], v[24:27]\n\t" \
  "ds_read_b64_tr_b16 a[178:179], v200 offset:13312\n\t" \
  "v_cvt_pk_bf16_f32 v116, v64, v65\n\t" \
  "v_mfma_f32_16x16x32_bf16 v[28:31], a[144:147], v[168:171], v[80:83]\n\t" \
  "v_exp_f32 v66, v66\n\t" \
  "v_mfma_f32_16x16x32_bf16 v[28:31], a[148:151], v[172:175], v[28:31]\n\t" \
  "ds_read_b64_tr_b16 a[180:181], v201 offset:9216\n\t" \
  "v_mfma_f32_16x16x32_bf16 v[28:31], a[152:155], v[176:179], v[28:31]\n\t" \
  "v_exp_f32 v67, v67\n\t" \
  "v_mfma_f32_16x16x32_bf16 v[28:31], a[156:159], v[180:183], v[28:31]\n\t" \
  "ds_read_b64_tr_b16 a[182:183], v201 offset:13312\n\t" \
  "v_cvt_pk_bf16_f32 v117, v66, v67\n\t" \
  "v_mfma_f32_16x16x32_bf16 v[32:35], a[128:131], v[184:187], v[84:87]\n\t" \
  "v_exp_f32 v68, v68\n\t" \
  "v_mfma_f32_16x16x32_bf16 v[32:35], a[132:135], v[188:191], v[32:35]\n\t" \
  "ds_read_b64_tr_b16 a[184:185], v200 offset:9728\n\t" \
  "v_mfma_f32_16x16x32_bf16 v[32:35], a[136:139], v[192:195], v[32:35]\n\t" \
  "v_exp_f32 v69, v69\n\t" \
  "v_mfma_f32_16x16x32_bf16 v[32:35], a[140:143], v[196:199], v[32:35]\n\t" \
  "ds_read_b64_tr_b16 a[186:187], v200 offset:13824\n\t" \
  "v_cvt_pk_bf16_f32 v118, v68, v69\n\t" \
  "v_mfma_f32_16x16x32_bf16 v[36:39], a[144:147], v[184:187], v[84:87]\n\t" \
  "v_exp_f32 v70, v70\n\t" \
  "v_mfma_f32_16x16x32_bf16 v[36:39], a[148:151], v[188:191], v[36:39]\n\t" \
  "ds_read_b64_tr_b16 a[188:189], v201 offset:9728\n\t" \
  "v_mfma_f32_16x16x32_bf16 v[36:39], a[152:155], v[192:195], v[36:39]\n\t" \
  "v_exp_f32 v71, v71\n\t" \
  "v_mfma_f32_16x16x32_bf16 v[36:39], a[156:159], v[196:199], v[36:39]\n\t" \
  "ds_read_b64_tr_b16 a[190:191], v201 offset:13824\n\t" \
  "v_cvt_pk_bf16_f32 v119, v70, v71\n\t" \
  "s_waitcnt lgkmcnt(5)\n\t" \
  "v_mfma_f32_16x16x32_bf16 a[0:3], a[160:163], v[104:107], a[0:3]\n\t" \
  "v_exp_f32 v8, v8\n\t" \
  "v_mfma_f32_16x16x32_bf16 a[4:7], a[164:167], v[104:107], a[4:7]\n\t" \
  "ds_read_b128 a[128:131], v220 offset:8192\n\t" \
  "v_mfma_f32_16x16x32_bf16 a[8:11], a[168:171], v[104:107], a[8:11]\n\t" \
  "v_exp_f32 v9, v9\n\t" \
  "v_mfma_f32_16x16x32_bf16 a[12:15], a[172:175], v[104:107], a[12:15]\n\t" \
  "ds_read_b128 a[132:135], v221 offset:8192\n\t" \
  "v_cvt_pk_bf16_f32 v88, v8, v9\n\t" \
  "v_mfma_f32_16x16x32_bf16 a[16:19], a[176:179], v[104:107], a[16:19]\n\t" \
  "s_mov_b32 m0, s79\n\t" \
  "s_add_u32 s79, s79, 0x8000\n\t" \
  "v_exp_f32 v10, v10\n\t" \
  "s_waitcnt lgkmcnt(4)\n\t" \
  "v_mfma_f32_16x16x32_bf16 a[20:23], a[180:183], v[104:107], a[20:23]\n\t" \
  "buffer_load_dwordx4 v204, s[36:39], s74 offen offset:0 lds\n\t" \
  "v_mfma_f32_16x16x32_bf16 a[24:27], a[184:187], v[104:107], a[24:27]\n\t" \
  "v_exp_f32 v11, v11\n\t" \
  "s_waitcnt lgkmcnt(2)\n\t" \
  "v_mfma_f32_16x16x32_bf16 a[28:31], a[188:191], v[104:107], a[28:31]\n\t" \
  "ds_read_b128 a[136:139], v222 offset:8192\n\t" \
  "v_cvt_pk_bf16_f32 v89, v10, v11\n\t" \
  "v_mfma_f32_16x16x32_bf16 v[120:123], v[224:227], v[104:107], v[120:123]\n\t" \
  "v_exp_f32 v12, v12\n\t" \
  "v_mfma_f32_16x16x32_bf16 a[32:35], a[160:163], v[108:111], a[32:35]\n\t" \
  "v_add_u32 v200, 0x8000, v200\n\t" \
  "v_mfma_f32_16x16x32_bf16 a[36:39], a[164:167], v[108:111], a[36:39]\n\t" \
  "v_exp_f32 v13, v13\n\t" \
  "v_mfma_f32_16x16x32_bf16 a[40:43], a[168:171], v[108:111], a[40:43]\n\t" \
  "ds_read_b128 a[140:143], v223 offset:8192\n\t" \
  "v_cvt_pk_bf16_f32 v90, v12, v13\n\t" \
  "v_mfma_f32_16x16x32_bf16 a[44:47], a[172:175], v[108:111], a[44:47]\n\t" \
  "s_and_b32 s79, s79, 0x1ffff\n\t" \
  "v_exp_f32 v14, v14\n\t" \
  "v_mfma_f32_16x16x32_bf16 a[48:51], a[176:179], v[108:111], a[48:51]\n\t" \
  "buffer_load_dwordx4 v205, s[36:39], s74 offen offset:1024 lds\n\t" \
  "v_mfma_f32_16x16x32_bf16 a[52:55], a[180:183], v[108:111], a[52:55]\n\t" \
  "v_exp_f32 v15, v15\n\t" \
  "v_mfma_f32_16x16x32_bf16 a[56:59], a[184:187], v[108:111], a[56:59]\n\t" \
  "ds_read_b128 a[144:147], v220 offset:12288\n\t" \
  "v_cvt_pk_bf16_f32 v91, v14, v15\n\t" \
  "v_mfma_f32_16x16x32_bf16 a[60:63], a[188:191], v[108:111], a[60:63]\n\t" \
  "v_exp_f32 v16, v16\n\t" \
  "v_mfma_f32_16x16x32_bf16 v[124:127], v[224:227], v[108:111], v[124:127]\n\t" \
  "v_and_b32 v200, 0x1ffff, v200\n\t" \
  "v_mfma_f32_16x16x32_bf16 a[64:67], a[160:163], v[112:115], a[64:67]\n\t" \
  "v_exp_f32 v17, v17\n\t" \
  "v_mfma_f32_16x16x32_bf16 a[68:71], a[164:167], v[112:115], a[68:71]\n\t" \
  "ds_read_b128 a[148:151], v221 offset:12288\n\t" \
  "v_cvt_pk_bf16_f32 v92, v16, v17\n\t" \
  "v_mfma_f32_16x16x32_bf16 a[72:75], a[168:171], v[112:115], a[72:75]\n\t" \
  "s_add_u32 s81, s81, 1\n\t" \
  "v_exp_f32 v18, v18\n\t" \
  "v_mfma_f32_16x16x32_bf16 a[76:79], a[172:175], v[112:115], a[76:79]\n\t" \
  "buffer_load_dwordx4 v206, s[36:39], s74 offen offset:2048 lds\n\t" \
  "v_mfma_f32_16x16x32_bf16 a[80:83], a[176:179], v[112:115], a[80:83]\n\t" \
  "v_exp_f32 v19, v19\n\t" \
  "v_mfma_f32_16x16x32_bf16 a[84:87], a[180:183], v[112:115], a[84:87]\n\t" \
  "ds_read_b128 a[152:155], v222 offset:12288\n\t" \
  "v_cvt_pk_bf16_f32 v93, v18, v19\n\t" \
  "v_mfma_f32_16x16x32_bf16 a[88:91], a[184:187], v[112:115], a[88:91]\n\t" \
  "v_exp_f32 v20, v20\n\t" \
  "v_mfma_f32_16x16x32_bf16 a[92:95], a[188:191], v[112:115], a[92:95]\n\t" \
  "v_xor_b32 v201, 32, v200\n\t" \
  "v_mfma_f32_16x16x32_bf16 v[128:131], v[224:227], v[112:115], v[128:131]\n\t" \
  "v_exp_f32 v21, v21\n\t" \
  "v_mfma_f32_16x16x32_bf16 a[96:99], a[160:163], v[116:119], a[96:99]\n\t" \
  "ds_read_b128 a[156:159], v223 offset:12288\n\t" \
  "v_cvt_pk_bf16_f32 v94, v20, v21\n\t" \
  "v_mfma_f32_16x16x32_bf16 a[100:103], a[164:167], v[116:119], a[100:103]\n\t" \
  "v_exp_f32 v22, v22\n\t" \
  "v_mfma_f32_16x16x32_bf16 a[104:107], a[168:171], v[116:119], a[104:107]\n\t" \
  "v_mfma_f32_16x16x32_bf16 a[108:111], a[172:175], v[116:119], a[108:111]\n\t" \
  "v_exp_f32 v23, v23\n\t" \
  "v_mfma_f32_16x16x32_bf16 a[112:115], a[176:179], v[116:119], a[112:115]\n\t" \
  "v_cvt_pk_bf16_f32 v95, v22, v23\n\t" \
  "v_mfma_f32_16x16x32_bf16 a[116:119], a[180:183], v[116:119], a[116:119]\n\t" \
  "v_mfma_f32_16x16x32_bf16 a[120:123], a[184:187], v[116:119], a[120:123]\n\t" \
  "buffer_load_dwordx4 v207, s[36:39], s74 offen offset:3072 lds\n\t" \
  "s_add_u32 s74, s74, s77\n\t" \
  "v_mfma_f32_16x16x32_bf16 a[124:127], a[188:191], v[116:119], a[124:127]\n\t" \
  "v_mfma_f32_16x16x32_bf16 v[132:135], v[224:227], v[116:119], v[132:135]\n\t" \
  "s_cmp_lt_u32 s81, 5\n\t" \
  "s_cbranch_scc1 .Laloop%=\n\t" \
  "s_waitcnt lgkmcnt(1)\n\t" \
  "v_mfma_f32_16x16x32_bf16 v[40:43], a[128:131], v[136:139], v[72:75]\n\t" \
  "s_waitcnt vmcnt(8)\n\t" \
  "s_barrier\n\t" \
  "s_mov_b32 s68, 20480\n\t" \
  "s_mov_b64 s[72:73], %[ws]\n\t" \
  "s_add_u32 s68, s68, s72\n\t" \
  "s_addc_u32 s69, s73, 0\n\t" \
  "v_exp_f32 v24, v24\n\t" \
  "v_mfma_f32_16x16x32_bf16 v[40:43], a[132:135], v[140:143], v[40:43]\n\t" \
  "s_lshl_b32 s85, %[bid], 2\n\t" \
  "s_add_u32 s85, s85, %[w]\n\t" \
  "s_mul_i32 s85, s85, 17\n\t" \
  "ds_read_b64_tr_b16 a[160:161], v200 offset:0\n\t" \
  "v_mfma_f32_16x16x32_bf16 v[40:43], a[136:139], v[144:147], v[40:43]\n\t" \
  "s_add_u32 s84, s83, 0x8000\n\t" \
  "global_load_dwordx4 v[244:247], v202, s[68:69] sc1\n\t" \
  "v_exp_f32 v25, v25\n\t" \
  "v_mfma_f32_16x16x32_bf16 v[40:43], a[140:143], v[148:151], v[40:43]\n\t" \
  "ds_read_b64_tr_b16 a[162:163], v200 offset:4096\n\t" \
  "v_cvt_pk_bf16_f32 v96, v24, v25\n\t" \
  "v_mfma_f32_16x16x32_bf16 v[44:47], a[144:147], v[136:139], v[72:75]\n\t" \
  "s_and_b32 s84, s84, 0x1ffff\n\t" \
  "v_exp_f32 v26, v26\n\t" \
  "v_mfma_f32_16x16x32_bf16 v[44:47], a[148:151], v[140:143], v[44:47]\n\t" \
  "ds_read_b64_tr_b16 a[164:165], v201 offset:0\n\t" \
  "v_min_u32 v252, 16, v203\n\t" \
  "v_add_u32 v252, s85, v252\n\t" \
  "v_mfma_f32_16x16x32_bf16 v[44:47], a[152:155], v[144:147], v[44:47]\n\t" \
  "s_sub_u32 s82, s84, s83\n\t" \
  "v_exp_f32 v27, v27\n\t" \
  "s_waitcnt lgkmcnt(3)\n\t" \
  "v_mfma_f32_16x16x32_bf16 v[44:47], a[156:159], v[148:151], v[44:47]\n\t" \
  "ds_read_b64_tr_b16 a[166:167], v201 offset:4096\n\t" \
  "v_cvt_pk_bf16_f32 v97, v26, v27\n\t" \
  "v_mfma_f32_16x16x32_bf16 v[48:51], a[128:131], v[152:155], v[76:79]\n\t" \
  "s_mov_b32 s83, s84\n\t" \
  "v_exp_f32 v28, v28\n\t" \
  "v_mfma_f32_16x16x32_bf16 v[48:51], a[132:135], v[156:159], v[48:51]\n\t" \
  "ds_read_b64_tr_b16 a[168:169], v200 offset:512\n\t" \
  "v_min_u32 v252, 17279, v252\n\t" \
  "v_lshlrev_b32 v252, 7, v252\n\t" \
  "v_mfma_f32_16x16x32_bf16 v[48:51], a[136:139], v[160:163], v[48:51]\n\t" \
  "s_and_b32 s86, %[mt], 1\n\t" \
  "v_exp_f32 v29, v29\n\t" \
  "v_mfma_f32_16x16x32_bf16 v[48:51], a[140:143], v[164:167], v[48:51]\n\t" \
  "s_mul_i32 s36, %[nt], 655360\n\t" \
  "ds_read_b64_tr_b16 a[170:171], v200 offset:4608\n\t" \
  "v_cvt_pk_bf16_f32 v98, v28, v29\n\t" \
  "v_mfma_f32_16x16x32_bf16 v[52:55], a[144:147], v[152:155], v[76:79]\n\t" \
  "s_mul_i32 s72, s86, 327680\n\t" \
  "v_exp_f32 v30, v30\n\t" \
  "v_mfma_f32_16x16x32_bf16 v[52:55], a[148:151], v[156:159], v[52:55]\n\t" \
  "s_add_u32 s36, s36, s72\n\t" \
  "ds_read_b64_tr_b16 a[172:173], v201 offset:512\n\t" \
  "v_add_u32 v220, s82, v220\n\t" \
  "v_mfma_f32_16x16x32_bf16 v[52:55], a[152:155], v[160:163], v[52:55]\n\t" \
  "s_mul_i32 s72, %[w], 81920\n\t" \
  "v_exp_f32 v31, v31\n\t" \
  "v_mfma_f32_16x16x32_bf16 v[52:55], a[156:159], v[164:167], v[52:55]\n\t" \
  "s_add_u32 s36, s36, s72\n\t" \
  "ds_read_b64_tr_b16 a[174:175], v201 offset:4608\n\t" \
  "v_cvt_pk_bf16_f32 v99, v30, v31\n\t" \
  "v_mfma_f32_16x16x32_bf16 v[56:59], a[128:131], v[168:171], v[80:83]\n\t" \
  "s_add_u32 s36, s36, 29409280\n\t" \
  "v_exp_f32 v32, v32\n\t" \
  "v_mfma_f32_16x16x32_bf16 v[56:59], a[132:135], v[172:175], v[56:59]\n\t" \
  "s_mov_b64 s[72:73], %[ws]\n\t" \
  "ds_read_b64_tr_b16 a[176:177], v200 offset:1024\n\t" \
  "v_add_u32 v221, s82, v221\n\t" \
  "v_mfma_f32_16x16x32_bf16 v[56:59], a[136:139], v[176:179], v[56:59]\n\t" \
  "s_add_u32 s36, s36, s72\n\t" \
  "v_exp_f32 v33, v33\n\t" \
  "v_mfma_f32_16x16x32_bf16 v[56:59], a[140:143], v[180:183], v[56:59]\n\t" \
  "s_addc_u32 s37, s73, 0\n\t" \
  "ds_read_b64_tr_b16 a[178:179], v200 offset:5120\n\t" \
  "v_cvt_pk_bf16_f32 v100, v32, v33\n\t" \
  "v_mfma_f32_16x16x32_bf16 v[60:63], a[144:147], v[168:171], v[80:83]\n\t" \
  "s_and_b32 s37, s37, 0xffff\n\t" \
  "v_exp_f32 v34, v34\n\t" \
  "v_mfma_f32_16x16x32_bf16 v[60:63], a[148:151], v[172:175], v[60:63]\n\t" \
  "s_mov_b32 s38, 0x14000\n\t" \
  "ds_read_b64_tr_b16 a[180:181], v201 offset:1024\n\t" \
  "v_add_u32 v222, s82, v222\n\t" \
  "v_mfma_f32_16x16x32_bf16 v[60:63], a[152:155], v[176:179], v[60:63]\n\t" \
  "s_mov_b32 s39, 0x20000\n\t" \
  "v_exp_f32 v35, v35\n\t" \
  "v_mfma_f32_16x16x32_bf16 v[60:63], a[156:159], v[180:183], v[60:63]\n\t" \
  "ds_read_b64_tr_b16 a[182:183], v201 offset:5120\n\t" \
  "v_cvt_pk_bf16_f32 v101, v34, v35\n\t" \
  "v_mfma_f32_16x16x32_bf16 v[64:67], a[128:131], v[184:187], v[84:87]\n\t" \
  "v_exp_f32 v36, v36\n\t" \
  "v_mfma_f32_16x16x32_bf16 v[64:67], a[132:135], v[188:191], v[64:67]\n\t" \
  "ds_read_b64_tr_b16 a[184:185], v200 offset:1536\n\t" \
  "v_add_u32 v223, s82, v223\n\t" \
  "v_mfma_f32_16x16x32_bf16 v[64:67], a[136:139], v[192:195], v[64:67]\n\t" \
  "v_exp_f32 v37, v37\n\t" \
  "v_mfma_f32_16x16x32_bf16 v[64:67], a[140:143], v[196:199], v[64:67]\n\t" \
  "s_lshl_b32 s86, %[w], 12\n\t" \
  "ds_read_b64_tr_b16 a[186:187], v200 offset:5632\n\t" \
  "v_cvt_pk_bf16_f32 v102, v36, v37\n\t" \
  "v_mfma_f32_16x16x32_bf16 v[68:71], a[144:147], v[184:187], v[84:87]\n\t" \
  "v_exp_f32 v38, v38\n\t" \
  "v_mfma_f32_16x16x32_bf16 v[68:71], a[148:151], v[188:191], v[68:71]\n\t" \
  "ds_read_b64_tr_b16 a[188:189], v201 offset:1536\n\t" \
  "v_add_u32 v254, s86, v202\n\t" \
  "v_add_u32 v254, 0x8000, v254\n\t" \
  "v_mfma_f32_16x16x32_bf16 v[68:71], a[152:155], v[192:195], v[68:71]\n\t" \
  "v_exp_f32 v39, v39\n\t" \
  "v_mfma_f32_16x16x32_bf16 v[68:71], a[156:159], v[196:199], v[68:71]\n\t" \
  "ds_read_b64_tr_b16 a[190:191], v201 offset:5632\n\t" \
  "v_cvt_pk_bf16_f32 v103, v38, v39\n\t" \
  "s_waitcnt lgkmcnt(4)\n\t" \
  "v_mfma_f32_16x16x32_bf16 a[0:3], a[160:163], v[88:91], a[0:3]\n\t" \
  "v_exp_f32 v40, v40\n\t" \
  "v_mfma_f32_16x16x32_bf16 a[4:7], a[164:167], v[88:91], a[4:7]\n\t" \
  "ds_read_b128 a[128:131], v220 offset:0\n\t" \
  "v_mfma_f32_16x16x32_bf16 a[8:11], a[168:171], v[88:91], a[8:11]\n\t" \
  "v_exp_f32 v41, v41\n\t" \
  "v_mfma_f32_16x16x32_bf16 a[12:15], a[172:175], v[88:91], a[12:15]\n\t" \
  "ds_read_b128 a[132:135], v221 offset:0\n\t" \
  "v_cvt_pk_bf16_f32 v104, v40, v41\n\t" \
  "v_mfma_f32_16x16x32_bf16 a[16:19], a[176:179], v[88:91], a[16:19]\n\t" \
  "s_mov_b32 m0, s80\n\t" \
  "s_add_u32 s80, s80, 0x8000\n\t" \
  "v_exp_f32 v42, v42\n\t" \
  "v_mfma_f32_16x16x32_bf16 a[20:23], a[180:183], v[88:91], a[20:23]\n\t" \
  "buffer_load_dwordx4 v208, s[40:43], s75 offen offset:0 lds\n\t" \
  "s_waitcnt lgkmcnt(3)\n\t" \
  "v_mfma_f32_16x16x32_bf16 a[24:27], a[184:187], v[88:91], a[24:27]\n\t" \
  "v_exp_f32 v43, v43\n\t" \
  "s_waitcnt lgkmcnt(2)\n\t" \
  "v_mfma_f32_16x16x32_bf16 a[28:31], a[188:191], v[88:91], a[28:31]\n\t" \
  "ds_read_b128 a[136:139], v222 offset:0\n\t" \
  "v_cvt_pk_bf16_f32 v105, v42, v43\n\t" \
  "v_mfma_f32_16x16x32_bf16 v[120:123], v[224:227], v[88:91], v[120:123]\n\t" \
  "s_and_b32 s80, s80, 0x1ffff\n\t" \
  "v_exp_f32 v44, v44\n\t" \
  "v_mfma_f32_16x16x32_bf16 a[32:35], a[160:163], v[92:95], a[32:35]\n\t" \
  "buffer_load_dwordx4 v209, s[40:43], s75 offen offset:1024 lds\n\t" \
  "v_mfma_f32_16x16x32_bf16 a[36:39], a[164:167], v[92:95], a[36:39]\n\t" \
  "v_exp_f32 v45, v45\n\t" \
  "v_mfma_f32_16x16x32_bf16 a[40:43], a[168:171], v[92:95], a[40:43]\n\t" \
  "ds_read_b128 a[140:143], v223 offset:0\n\t" \
  "v_cvt_pk_bf16_f32 v106, v44, v45\n\t" \
  "v_mfma_f32_16x16x32_bf16 a[44:47], a[172:175], v[92:95], a[44:47]\n\t" \
  "v_exp_f32 v46, v46\n\t" \
  "v_mfma_f32_16x16x32_bf16 a[48:51], a[176:179], v[92:95], a[48:51]\n\t" \
  "buffer_load_dwordx4 v210, s[40:43], s75 offen offset:2048 lds\n\t" \
  "v_mfma_f32_16x16x32_bf16 a[52:55], a[180:183], v[92:95], a[52:55]\n\t" \
  "v_exp_f32 v47, v47\n\t" \
  "v_mfma_f32_16x16x32_bf16 a[56:59], a[184:187], v[92:95], a[56:59]\n\t" \
  "ds_read_b128 a[144:147], v220 offset:4096\n\t" \
  "v_cvt_pk_bf16_f32 v107, v46, v47\n\t" \
  "v_mfma_f32_16x16x32_bf16 a[60:63], a[188:191], v[92:95], a[60:63]\n\t" \
  "v_exp_f32 v48, v48\n\t" \
  "v_mfma_f32_16x16x32_bf16 v[124:127], v[224:227], v[92:95], v[124:127]\n\t" \
  "buffer_load_dwordx4 v211, s[40:43], s75 offen offset:3072 lds\n\t" \
  "s_add_u32 s75, s75, s78\n\t" \
  "v_mfma_f32_16x16x32_bf16 a[64:67], a[160:163], v[96:99], a[64:67]\n\t" \
  "v_exp_f32 v49, v49\n\t" \
  "v_mfma_f32_16x16x32_bf16 a[68:71], a[164:167], v[96:99], a[68:71]\n\t" \
  "ds_read_b128 a[148:151], v221 offset:4096\n\t" \
  "v_cvt_pk_bf16_f32 v108, v48, v49\n\t" \
  "v_mfma_f32_16x16x32_bf16 a[72:75], a[168:171], v[96:99], a[72:75]\n\t" \
  "v_exp_f32 v50, v50\n\t" \
  "v_mfma_f32_16x16x32_bf16 a[76:79], a[172:175], v[96:99], a[76:79]\n\t" \
  "s_waitcnt vmcnt(4)\n\t" \
  "v_xor_b32 v244, %[tag], v244\n\t" \
  "v_xor_b32 v245, %[tag], v245\n\t" \
  "v_xor_b32 v246, %[tag], v246\n\t" \
  "v_xor_b32 v247, %[tag], v247\n\t" \
  "v_or3_b32 v244, v244, v245, v246\n\t" \
  "v_or_b32 v244, v244, v247\n\t" \
  "v_cmp_ne_u32 vcc, 0, v244\n\t" \
  "s_cbranch_vccnz .Lprspin%=\n\t" \
  ".Lprback%=:\n\t" \
  "v_mfma_f32_16x16x32_bf16 a[80:83], a[176:179], v[96:99], a[80:83]\n\t" \
  "v_exp_f32 v51, v51\n\t" \
  "v_mfma_f32_16x16x32_bf16 a[84:87], a[180:183], v[96:99], a[84:87]\n\t" \
  "ds_read_b128 a[152:155], v222 offset:4096\n\t" \
  "v_cvt_pk_bf16_f32 v109, v50, v51\n\t" \
  "v_mfma_f32_16x16x32_bf16 a[88:91], a[184:187], v[96:99], a[88:91]\n\t" \
  "s_mov_b32 s88, 16384\n\t" \
  "v_exp_f32 v52, v52\n\t" \
  "v_mfma_f32_16x16x32_bf16 a[92:95], a[188:191], v[96:99], a[92:95]\n\t" \
  "buffer_load_dwordx4 a[192:195], v202, s[36:39], s88 offen offset:0 sc1\n\t" \
  "v_mfma_f32_16x16x32_bf16 v[128:131], v[224:227], v[96:99], v[128:131]\n\t" \
  "v_exp_f32 v53, v53\n\t" \
  "v_mfma_f32_16x16x32_bf16 a[96:99], a[160:163], v[100:103], a[96:99]\n\t" \
  "ds_read_b128 a[156:159], v223 offset:4096\n\t" \
  "v_cvt_pk_bf16_f32 v110, v52, v53\n\t" \
  "v_mfma_f32_16x16x32_bf16 a[100:103], a[164:167], v[100:103], a[100:103]\n\t" \
  "v_exp_f32 v54, v54\n\t" \
  "v_mfma_f32_16x16x32_bf16 a[104:107], a[168:171], v[100:103], a[104:107]\n\t" \
  "buffer_load_dwordx4 a[196:199], v202, s[36:39], s88 offen offset:1024 sc1\n\t" \
  "v_mfma_f32_16x16x32_bf16 a[108:111], a[172:175], v[100:103], a[108:111]\n\t" \
  "v_exp_f32 v55, v55\n\t" \
  "v_mfma_f32_16x16x32_bf16 a[112:115], a[176:179], v[100:103], a[112:115]\n\t" \
  "v_cvt_pk_bf16_f32 v111, v54, v55\n\t" \
  "v_mfma_f32_16x16x32_bf16 a[116:119], a[180:183], v[100:103], a[116:119]\n\t" \
  "buffer_load_dwordx4 a[200:203], v202, s[36:39], s88 offen offset:2048 sc1\n\t" \
  "v_mfma_f32_16x16x32_bf16 a[120:123], a[184:187], v[100:103], a[120:123]\n\t" \
  "buffer_load_dwordx4 a[204:207], v202, s[36:39], s88 offen offset:3072 sc1\n\t" \
  "v_mfma_f32_16x16x32_bf16 a[124:127], a[188:191], v[100:103], a[124:127]\n\t" \
  "s_mov_b32 s88, 20480\n\t" \
  "buffer_load_dwordx4 a[208:211], v202, s[36:39], s88 offen offset:0 sc1\n\t" \
  "v_mfma_f32_16x16x32_bf16 v[132:135], v[224:227], v[100:103], v[132:135]\n\t" \
  "buffer_load_dwordx4 a[212:215], v202, s[36:39], s88 offen offset:1024 sc1\n\t" \
  "s_waitcnt lgkmcnt(1)\n\t" \
  "v_mfma_f32_16x16x32_bf16 v[8:11], a[128:131], v[136:139], v[72:75]\n\t" \
  "v_exp_f32 v56, v56\n\t" \
  "v_mfma_f32_16x16x32_bf16 v[8:11], a[132:135], v[140:143], v[8:11]\n\t" \
  "ds_read_b64_tr_b16 a[160:161], v200 offset:8192\n\t" \
  "v_mfma_f32_16x16x32_bf16 v[8:11], a[136:139], v[144:147], v[8:11]\n\t" \
  "v_exp_f32 v57, v57\n\t" \
  "v_mfma_f32_16x16x32_bf16 v[8:11], a[140:143], v[148:151], v[8:11]\n\t" \
  "ds_read_b64_tr_b16 a[162:163], v200 offset:12288\n\t" \
  "v_cvt_pk_bf16_f32 v112, v56, v57\n\t" \
  "v_mfma_f32_16x16x32_bf16 v[12:15], a[144:147], v[136:139], v[72:75]\n\t" \
  "v_exp_f32 v58, v58\n\t" \
  "v_mfma_f32_16x16x32_bf16 v[12:15], a[148:151], v[140:143], v[12:15]\n\t" \
  "ds_read_b64_tr_b16 a[164:165], v201 offset:8192\n\t" \
  "v_mfma_f32_16x16x32_bf16 v[12:15], a[152:155], v[144:147], v[12:15]\n\t" \
  "v_exp_f32 v59, v59\n\t" \
  "s_waitcnt lgkmcnt(3)\n\t" \
  "v_mfma_f32_16x16x32_bf16 v[12:15], a[156:159], v[148:151], v[12:15]\n\t" \
  "ds_read_b64_tr_b16 a[166:167], v201 offset:12288\n\t" \
  "v_cvt_pk_bf16_f32 v113, v58, v59\n\t" \
  "v_mfma_f32_16x16x32_bf16 v[16:19], a[128:131], v[152:155], v[76:79]\n\t" \
  "v_exp_f32 v60, v60\n\t" \
  "v_mfma_f32_16x16x32_bf16 v[16:19], a[132:135], v[156:159], v[16:19]\n\t" \
  "ds_read_b64_tr_b16 a[168:169], v200 offset:8704\n\t" \
  "v_mfma_f32_16x16x32_bf16 v[16:19], a[136:139], v[160:163], v[16:19]\n\t" \
  "v_exp_f32 v61, v61\n\t" \
  "v_mfma_f32_16x16x32_bf16 v[16:19], a[140:143], v[164:167], v[16:19]\n\t" \
  "ds_read_b64_tr_b16 a[170:171], v200 offset:12800\n\t" \
  "v_cvt_pk_bf16_f32 v114, v60, v61\n\t" \
  "v_mfma_f32_16x16x32_bf16 v[20:23], a[144:147], v[152:155], v[76:79]\n\t" \
  "v_exp_f32 v62, v62\n\t" \
  "v_mfma_f32_16x16x32_bf16 v[20:23], a[148:151], v[156:159], v[20:23]\n\t" \
  "ds_read_b64_tr_b16 a[172:173], v201 offset:8704\n\t" \
  "v_mfma_f32_16x16x32_bf16 v[20:23], a[152:155], v[160:163], v[20:23]\n\t" \
  "v_exp_f32 v63, v63\n\t" \
  "v_mfma_f32_16x16x32_bf16 v[20:23], a[156:159], v[164:167], v[20:23]\n\t" \
  "ds_read_b64_tr_b16 a[174:175], v201 offset:12800\n\t" \
  "v_cvt_pk_bf16_f32 v115, v62, v63\n\t" \
  "v_mfma_f32_16x16x32_bf16 v[24:27], a[128:131], v[168:171], v[80:83]\n\t" \
  "v_exp_f32 v64, v64\n\t" \
  "v_mfma_f32_16x16x32_bf16 v[24:27], a[132:135], v[172:175], v[24:27]\n\t" \
  "ds_read_b64_tr_b16 a[176:177], v200 offset:9216\n\t" \
  "v_mfma_f32_16x16x32_bf16 v[24:27], a[136:139], v[176:179], v[24:27]\n\t" \
  "v_exp_f32 v65, v65\n\t" \
  "v_mfma_f32_16x16x32_bf16 v[24:27], a[140:143], v[180:183], v[24:27]\n\t" \
  "ds_read_b64_tr_b16 a[178:179], v200 offset:13312\n\t" \
  "v_cvt_pk_bf16_f32 v116, v64, v65\n\t" \
  "v_mfma_f32_16x16x32_bf16 v[28:31], a[144:147], v[168:171], v[80:83]\n\t" \
  "v_exp_f32 v66, v66\n\t" \
  "v_mfma_f32_16x16x32_bf16 v[28:31], a[148:151], v[172:175], v[28:31]\n\t" \
  "ds_read_b64_tr_b16 a[180:181], v201 offset:9216\n\t" \
  "v_mfma_f32_16x16x32_bf16 v[28:31], a[152:155], v[176:179], v[28:31]\n\t" \
  "v_exp_f32 v67, v67\n\t" \
  "v_mfma_f32_16x16x32_bf16 v[28:31], a[156:159], v[180:183], v[28:31]\n\t" \
  "ds_read_b64_tr_b16 a[182:183], v201 offset:13312\n\t" \
  "v_cvt_pk_bf16_f32 v117, v66, v67\n\t" \
  "v_mfma_f32_16x16x32_bf16 v[32:35], a[128:131], v[184:187], v[84:87]\n\t" \
  "v_exp_f32 v68, v68\n\t" \
  "v_mfma_f32_16x16x32_bf16 v[32:35], a[132:135], v[188:191], v[32:35]\n\t" \
  "ds_read_b64_tr_b16 a[184:185], v200 offset:9728\n\t" \
  "v_mfma_f32_16x16x32_bf16 v[32:35], a[136:139], v[192:195], v[32:35]\n\t" \
  "v_exp_f32 v69, v69\n\t" \
  "v_mfma_f32_16x16x32_bf16 v[32:35], a[140:143], v[196:199], v[32:35]\n\t" \
  "ds_read_b64_tr_b16 a[186:187], v200 offset:13824\n\t" \
  "v_cvt_pk_bf16_f32 v118, v68, v69\n\t" \
  "v_mfma_f32_16x16x32_bf16 v[36:39], a[144:147], v[184:187], v[84:87]\n\t" \
  "v_exp_f32 v70, v70\n\t" \
  "v_mfma_f32_16x16x32_bf16 v[36:39], a[148:151], v[188:191], v[36:39]\n\t" \
  "ds_read_b64_tr_b16 a[188:189], v201 offset:9728\n\t" \
  "v_mfma_f32_16x16x32_bf16 v[36:39], a[152:155], v[192:195], v[36:39]\n\t" \
  "v_exp_f32 v71, v71\n\t" \
  "v_mfma_f32_16x16x32_bf16 v[36:39], a[156:159], v[196:199], v[36:39]\n\t" \
  "ds_read_b64_tr_b16 a[190:191], v201 offset:13824\n\t" \
  "v_cvt_pk_bf16_f32 v119, v70, v71\n\t" \
  "s_waitcnt lgkmcnt(5)\n\t" \
  "v_mfma_f32_16x16x32_bf16 a[0:3], a[160:163], v[104:107], a[0:3]\n\t" \
  "v_exp_f32 v8, v8\n\t" \
  "v_mfma_f32_16x16x32_bf16 a[4:7], a[164:167], v[104:107], a[4:7]\n\t" \
  "ds_read_b128 a[128:131], v220 offset:8192\n\t" \
  "v_mfma_f32_16x16x32_bf16 a[8:11], a[168:171], v[104:107], a[8:11]\n\t" \
  "v_exp_f32 v9, v9\n\t" \
  "v_mfma_f32_16x16x32_bf16 a[12:15], a[172:175], v[104:107], a[12:15]\n\t" \
  "ds_read_b128 a[132:135], v221 offset:8192\n\t" \
  "v_cvt_pk_bf16_f32 v88, v8, v9\n\t" \
  "v_mfma_f32_16x16x32_bf16 a[16:19], a[176:179], v[104:107], a[16:19]\n\t" \
  "v_exp_f32 v10, v10\n\t" \
  "s_waitcnt lgkmcnt(4)\n\t" \
  "v_mfma_f32_16x16x32_bf16 a[20:23], a[180:183], v[104:107], a[20:23]\n\t" \
  "buffer_load_dwordx4 a[216:219], v202, s[36:39], s88 offen offset:2048 sc1\n\t" \
  "v_mfma_f32_16x16x32_bf16 a[24:27], a[184:187], v[104:107], a[24:27]\n\t" \
  "v_exp_f32 v11, v11\n\t" \
  "s_waitcnt lgkmcnt(2)\n\t" \
  "v_mfma_f32_16x16x32_bf16 a[28:31], a[188:191], v[104:107], a[28:31]\n\t" \
  "ds_read_b128 a[136:139], v222 offset:8192\n\t" \
  "v_cvt_pk_bf16_f32 v89, v10, v11\n\t" \
  "v_mfma_f32_16x16x32_bf16 v[120:123], v[224:227], v[104:107], v[120:123]\n\t" \
  "v_exp_f32 v12, v12\n\t" \
  "v_mfma_f32_16x16x32_bf16 a[32:35], a[160:163], v[108:111], a[32:35]\n\t" \
  "buffer_load_dwordx4 a[220:223], v202, s[36:39], s88 offen offset:3072 sc1\n\t" \
  "v_add_u32 v200, 0x8000, v200\n\t" \
  "v_mfma_f32_16x16x32_bf16 a[36:39], a[164:167], v[108:111], a[36:39]\n\t" \
  "v_exp_f32 v13, v13\n\t" \
  "v_mfma_f32_16x16x32_bf16 a[40:43], a[168:171], v[108:111], a[40:43]\n\t" \
  "ds_read_b128 a[140:143], v223 offset:8192\n\t" \
  "v_cvt_pk_bf16_f32 v90, v12, v13\n\t" \
  "v_mfma_f32_16x16x32_bf16 a[44:47], a[172:175], v[108:111], a[44:47]\n\t" \
  "s_mov_b32 s88, 24576\n\t" \
  "v_exp_f32 v14, v14\n\t" \
  "v_mfma_f32_16x16x32_bf16 a[48:51], a[176:179], v[108:111], a[48:51]\n\t" \
  "buffer_load_dwordx4 a[224:227], v202, s[36:39], s88 offen offset:0 sc1\n\t" \
  "v_mfma_f32_16x16x32_bf16 a[52:55], a[180:183], v[108:111], a[52:55]\n\t" \
  "v_exp_f32 v15, v15\n\t" \
  "v_mfma_f32_16x16x32_bf16 a[56:59], a[184:187], v[108:111], a[56:59]\n\t" \
  "ds_read_b128 a[144:147], v220 offset:12288\n\t" \
  "v_cvt_pk_bf16_f32 v91, v14, v15\n\t" \
  "v_mfma_f32_16x16x32_bf16 a[60:63], a[188:191], v[108:111], a[60:63]\n\t" \
  "v_exp_f32 v16, v16\n\t" \
  "v_mfma_f32_16x16x32_bf16 v[124:127], v[224:227], v[108:111], v[124:127]\n\t" \
  "buffer_load_dwordx4 a[228:231], v202, s[36:39], s88 offen offset:1024 sc1\n\t" \
  "v_and_b32 v200, 0x1ffff, v200\n\t" \
  "v_mfma_f32_16x16x32_bf16 a[64:67], a[160:163], v[112:115], a[64:67]\n\t" \
  "v_exp_f32 v17, v17\n\t" \
  "v_mfma_f32_16x16x32_bf16 a[68:71], a[164:167], v[112:115], a[68:71]\n\t" \
  "ds_read_b128 a[148:151], v221 offset:12288\n\t" \
  "v_cvt_pk_bf16_f32 v92, v16, v17\n\t" \
  "v_mfma_f32_16x16x32_bf16 a[72:75], a[168:171], v[112:115], a[72:75]\n\t" \
  "v_exp_f32 v18, v18\n\t" \
  "v_mfma_f32_16x16x32_bf16 a[76:79], a[172:175], v[112:115], a[76:79]\n\t" \
  "buffer_load_dwordx4 a[232:235], v202, s[36:39], s88 offen offset:2048 sc1\n\t" \
  "v_mfma_f32_16x16x32_bf16 a[80:83], a[176:179], v[112:115], a[80:83]\n\t" \
  "v_exp_f32 v19, v19\n\t" \
  "v_mfma_f32_16x16x32_bf16 a[84:87], a[180:183], v[112:115], a[84:87]\n\t" \
  "ds_read_b128 a[152:155], v222 offset:12288\n\t" \
  "v_cvt_pk_bf16_f32 v93, v18, v19\n\t" \
  "v_mfma_f32_16x16x32_bf16 a[88:91], a[184:187], v[112:115], a[88:91]\n\t" \
  "v_exp_f32 v20, v20\n\t" \
  "v_mfma_f32_16x16x32_bf16 a[92:95], a[188:191], v[112:115], a[92:95]\n\t" \
  "buffer_load_dwordx4 a[236:239], v202, s[36:39], s88 offen offset:3072 sc1\n\t" \
  "v_xor_b32 v201, 32, v200\n\t" \
  "v_mfma_f32_16x16x32_bf16 v[128:131], v[224:227], v[112:115], v[128:131]\n\t" \
  "v_exp_f32 v21, v21\n\t" \
  "v_mfma_f32_16x16x32_bf16 a[96:99], a[160:163], v[116:119], a[96:99]\n\t" \
  "ds_read_b128 a[156:159], v223 offset:12288\n\t" \
  "v_cvt_pk_bf16_f32 v94, v20, v21\n\t" \
  "v_mfma_f32_16x16x32_bf16 a[100:103], a[164:167], v[116:119], a[100:103]\n\t" \
  "s_mov_b32 s88, 28672\n\t" \
  "v_exp_f32 v22, v22\n\t" \
  "v_mfma_f32_16x16x32_bf16 a[104:107], a[168:171], v[116:119], a[104:107]\n\t" \
  "buffer_load_dwordx4 a[240:243], v202, s[36:39], s88 offen offset:0 sc1\n\t" \
  "v_mfma_f32_16x16x32_bf16 a[108:111], a[172:175], v[116:119], a[108:111]\n\t" \
  "v_exp_f32 v23, v23\n\t" \
  "v_mfma_f32_16x16x32_bf16 a[112:115], a[176:179], v[116:119], a[112:115]\n\t" \
  "v_cvt_pk_bf16_f32 v95, v22, v23\n\t" \
  "v_mfma_f32_16x16x32_bf16 a[116:119], a[180:183], v[116:119], a[116:119]\n\t" \
  "buffer_load_dwordx4 a[244:247], v202, s[36:39], s88 offen offset:1024 sc1\n\t" \
  "v_mfma_f32_16x16x32_bf16 a[120:123], a[184:187], v[116:119], a[120:123]\n\t" \
  "buffer_load_dwordx4 a[248:251], v202, s[36:39], s88 offen offset:2048 sc1\n\t" \
  "v_mfma_f32_16x16x32_bf16 a[124:127], a[188:191], v[116:119], a[124:127]\n\t" \
  "buffer_load_dwordx4 a[252:255], v202, s[36:39], s88 offen offset:3072 sc1\n\t" \
  "v_mfma_f32_16x16x32_bf16 v[132:135], v[224:227], v[116:119], v[132:135]\n\t" \
  "s_waitcnt lgkmcnt(1)\n\t" \
  "v_mfma_f32_16x16x32_bf16 v[40:43], a[128:131], v[136:139], v[72:75]\n\t" \
  "s_barrier\n\t" \
  "v_exp_f32 v24, v24\n\t" \
  "v_mfma_f32_16x16x32_bf16 v[40:43], a[132:135], v[140:143], v[40:43]\n\t" \
  "ds_read_b64_tr_b16 a[160:161], v200 offset:0\n\t" \
  "v_mfma_f32_16x16x32_bf16 v[40:43], a[136:139], v[144:147], v[40:43]\n\t" \
  "s_add_u32 s84, s83, 0x8000\n\t" \
  "v_exp_f32 v25, v25\n\t" \
  "v_mfma_f32_16x16x32_bf16 v[40:43], a[140:143], v[148:151], v[40:43]\n\t" \
  "s_mov_b32 m0, 162816\n\t" \
  "ds_read_b64_tr_b16 a[162:163], v200 offset:4096\n\t" \
  "v_cvt_pk_bf16_f32 v96, v24, v25\n\t" \
  "v_mfma_f32_16x16x32_bf16 v[44:47], a[144:147], v[136:139], v[72:75]\n\t" \
  "s_and_b32 s84, s84, 0x1ffff\n\t" \
  "global_load_lds_dword v252, %[wg]\n\t" \
  "v_exp_f32 v26, v26\n\t" \
  "v_mfma_f32_16x16x32_bf16 v[44:47], a[148:151], v[140:143], v[44:47]\n\t" \
  "ds_read_b64_tr_b16 a[164:165], v201 offset:0\n\t" \
  "v_mfma_f32_16x16x32_bf16 v[44:47], a[152:155], v[144:147], v[44:47]\n\t" \
  "s_sub_u32 s82, s84, s83\n\t" \
  "v_exp_f32 v27, v27\n\t" \
  "s_waitcnt lgkmcnt(3)\n\t" \
  "v_mfma_f32_16x16x32_bf16 v[44:47], a[156:159], v[148:151], v[44:47]\n\t" \
  "s_mov_b32 s83, s84\n\t" \
  "ds_read_b64_tr_b16 a[166:167], v201 offset:4096\n\t" \
  "v_cvt_pk_bf16_f32 v97, v26, v27\n\t" \
  "v_mfma_f32_16x16x32_bf16 v[48:51], a[128:131], v[152:155], v[76:79]\n\t" \
  "global_load_lds_dword v252, %[wu]\n\t" \
  "v_exp_f32 v28, v28\n\t" \
  "v_mfma_f32_16x16x32_bf16 v[48:51], a[132:135], v[156:159], v[48:51]\n\t" \
  "ds_read_b64_tr_b16 a[168:169], v200 offset:512\n\t" \
  "v_mfma_f32_16x16x32_bf16 v[48:51], a[136:139], v[160:163], v[48:51]\n\t" \
  "v_exp_f32 v29, v29\n\t" \
  "v_mfma_f32_16x16x32_bf16 v[48:51], a[140:143], v[164:167], v[48:51]\n\t" \
  "ds_read_b64_tr_b16 a[170:171], v200 offset:4608\n\t" \
  "v_cvt_pk_bf16_f32 v98, v28, v29\n\t" \
  "v_mfma_f32_16x16x32_bf16 v[52:55], a[144:147], v[152:155], v[76:79]\n\t" \
  "global_load_lds_dword v252, %[wd]\n\t" \
  "v_exp_f32 v30, v30\n\t" \
  "v_mfma_f32_16x16x32_bf16 v[52:55], a[148:151], v[156:159], v[52:55]\n\t" \
  "ds_read_b64_tr_b16 a[172:173], v201 offset:512\n\t" \
  "v_add_u32 v220, s82, v220\n\t" \
  "v_mfma_f32_16x16x32_bf16 v[52:55], a[152:155], v[160:163], v[52:55]\n\t" \
  "v_exp_f32 v31, v31\n\t" \
  "v_mfma_f32_16x16x32_bf16 v[52:55], a[156:159], v[164:167], v[52:55]\n\t" \
  "ds_read_b64_tr_b16 a[174:175], v201 offset:4608\n\t" \
  "v_cvt_pk_bf16_f32 v99, v30, v31\n\t" \
  "v_mfma_f32_16x16x32_bf16 v[56:59], a[128:131], v[168:171], v[80:83]\n\t" \
  "v_exp_f32 v32, v32\n\t" \
  "v_mfma_f32_16x16x32_bf16 v[56:59], a[132:135], v[172:175], v[56:59]\n\t" \
  "ds_read_b64_tr_b16 a[176:177], v200 offset:1024\n\t" \
  "v_add_u32 v221, s82, v221\n\t" \
  "v_mfma_f32_16x16x32_bf16 v[56:59], a[136:139], v[176:179], v[56:59]\n\t" \
  "v_exp_f32 v33, v33\n\t" \
  "v_mfma_f32_16x16x32_bf16 v[56:59], a[140:143], v[180:183], v[56:59]\n\t" \
  "ds_read_b64_tr_b16 a[178:179], v200 offset:5120\n\t" \
  "v_cvt_pk_bf16_f32 v100, v32, v33\n\t" \
  "v_mfma_f32_16x16x32_bf16 v[60:63], a[144:147], v[168:171], v[80:83]\n\t" \
  "v_exp_f32 v34, v34\n\t" \
  "v_mfma_f32_16x16x32_bf16 v[60:63], a[148:151], v[172:175], v[60:63]\n\t" \
  "ds_read_b64_tr_b16 a[180:181], v201 offset:1024\n\t" \
  "v_add_u32 v222, s82, v222\n\t" \
  "v_mfma_f32_16x16x32_bf16 v[60:63], a[152:155], v[176:179], v[60:63]\n\t" \
  "v_exp_f32 v35, v35\n\t" \
  "v_mfma_f32_16x16x32_bf16 v[60:63], a[156:159], v[180:183], v[60:63]\n\t" \
  "ds_read_b64_tr_b16 a[182:183], v201 offset:5120\n\t" \
  "v_cvt_pk_bf16_f32 v101, v34, v35\n\t" \
  "v_mfma_f32_16x16x32_bf16 v[64:67], a[128:131], v[184:187], v[84:87]\n\t" \
  "v_exp_f32 v36, v36\n\t" \
  "v_mfma_f32_16x16x32_bf16 v[64:67], a[132:135], v[188:191], v[64:67]\n\t" \
  "ds_read_b64_tr_b16 a[184:185], v200 offset:1536\n\t" \
  "v_add_u32 v223, s82, v223\n\t" \
  "v_mfma_f32_16x16x32_bf16 v[64:67], a[136:139], v[192:195], v[64:67]\n\t" \
  "v_exp_f32 v37, v37\n\t" \
  "v_mfma_f32_16x16x32_bf16 v[64:67], a[140:143], v[196:199], v[64:67]\n\t" \
  "ds_read_b64_tr_b16 a[186:187], v200 offset:5632\n\t" \
  "v_cvt_pk_bf16_f32 v102, v36, v37\n\t" \
  "v_mfma_f32_16x16x32_bf16 v[68:71], a[144:147], v[184:187], v[84:87]\n\t" \
  "v_exp_f32 v38, v38\n\t" \
  "v_mfma_f32_16x16x32_bf16 v[68:71], a[148:151], v[188:191], v[68:71]\n\t" \
  "ds_read_b64_tr_b16 a[188:189], v201 offset:1536\n\t" \
  "v_mfma_f32_16x16x32_bf16 v[68:71], a[152:155], v[192:195], v[68:71]\n\t" \
  "v_exp_f32 v39, v39\n\t" \
  "v_mfma_f32_16x16x32_bf16 v[68:71], a[156:159], v[196:199], v[68:71]\n\t" \
  "ds_read_b64_tr_b16 a[190:191], v201 offset:5632\n\t" \
  "v_cvt_pk_bf16_f32 v103, v38, v39\n\t" \
  "s_waitcnt lgkmcnt(4)\n\t" \
  "v_mfma_f32_16x16x32_bf16 a[0:3], a[160:163], v[88:91], a[0:3]\n\t" \
  "v_exp_f32 v40, v40\n\t" \
  "v_mfma_f32_16x16x32_bf16 a[4:7], a[164:167], v[88:91], a[4:7]\n\t" \
  "ds_read_b128 a[128:131], v220 offset:0\n\t" \
  "v_mfma_f32_16x16x32_bf16 a[8:11], a[168:171], v[88:91], a[8:11]\n\t" \
  "v_exp_f32 v41, v41\n\t" \
  "v_mfma_f32_16x16x32_bf16 a[12:15], a[172:175], v[88:91], a[12:15]\n\t" \
  "ds_read_b128 a[132:135], v221 offset:0\n\t" \
  "v_cvt_pk_bf16_f32 v104, v40, v41\n\t" \
  "v_mfma_f32_16x16x32_bf16 a[16:19], a[176:179], v[88:91], a[16:19]\n\t" \
  "v_exp_f32 v42, v42\n\t" \
  "v_mfma_f32_16x16x32_bf16 a[20:23], a[180:183], v[88:91], a[20:23]\n\t" \
  "s_waitcnt vmcnt(18)\n\t" \
  "ds_write_b128 v254, a[192:195] offset:0\n\t" \
  "s_waitcnt lgkmcnt(5)\n\t" \
  "v_mfma_f32_16x16x32_bf16 a[24:27], a[184:187], v[88:91], a[24:27]\n\t" \
  "v_exp_f32 v43, v43\n\t" \
  "s_waitcnt lgkmcnt(3)\n\t" \
  "v_mfma_f32_16x16x32_bf16 a[28:31], a[188:191], v[88:91], a[28:31]\n\t" \
  "ds_read_b128 a[136:139], v222 offset:0\n\t" \
  "v_cvt_pk_bf16_f32 v105, v42, v43\n\t" \
  "v_mfma_f32_16x16x32_bf16 v[120:123], v[224:227], v[88:91], v[120:123]\n\t" \
  "v_exp_f32 v44, v44\n\t" \
  "v_mfma_f32_16x16x32_bf16 a[32:35], a[160:163], v[92:95], a[32:35]\n\t" \
  "s_waitcnt vmcnt(17)\n\t" \
  "ds_write_b128 v254, a[196:199] offset:1024\n\t" \
  "v_mfma_f32_16x16x32_bf16 a[36:39], a[164:167], v[92:95], a[36:39]\n\t" \
  "v_exp_f32 v45, v45\n\t" \
  "v_mfma_f32_16x16x32_bf16 a[40:43], a[168:171], v[92:95], a[40:43]\n\t" \
  "ds_read_b128 a[140:143], v223 offset:0\n\t" \
  "v_cvt_pk_bf16_f32 v106, v44, v45\n\t" \
  "v_mfma_f32_16x16x32_bf16 a[44:47], a[172:175], v[92:95], a[44:47]\n\t" \
  "v_exp_f32 v46, v46\n\t" \
  "v_mfma_f32_16x16x32_bf16 a[48:51], a[176:179], v[92:95], a[48:51]\n\t" \
  "s_waitcnt vmcnt(16)\n\t" \
  "ds_write_b128 v254, a[200:203] offset:2048\n\t" \
  "v_mfma_f32_16x16x32_bf16 a[52:55], a[180:183], v[92:95], a[52:55]\n\t" \
  "v_exp_f32 v47, v47\n\t" \
  "v_mfma_f32_16x16x32_bf16 a[56:59], a[184:187], v[92:95], a[56:59]\n\t" \
  "ds_read_b128 a[144:147], v220 offset:4096\n\t" \
  "v_cvt_pk_bf16_f32 v107, v46, v47\n\t" \
  "v_mfma_f32_16x16x32_bf16 a[60:63], a[188:191], v[92:95], a[60:63]\n\t" \
  "v_exp_f32 v48, v48\n\t" \
  "v_mfma_f32_16x16x32_bf16 v[124:127], v[224:227], v[92:95], v[124:127]\n\t" \
  "s_waitcnt vmcnt(15)\n\t" \
  "ds_write_b128 v254, a[204:207] offset:3072\n\t" \
  "v_mfma_f32_16x16x32_bf16 a[64:67], a[160:163], v[96:99], a[64:67]\n\t" \
  "v_exp_f32 v49, v49\n\t" \
  "v_mfma_f32_16x16x32_bf16 a[68:71], a[164:167], v[96:99], a[68:71]\n\t" \
  "ds_read_b128 a[148:151], v221 offset:4096\n\t" \
  "v_cvt_pk_bf16_f32 v108, v48, v49\n\t" \
  "v_mfma_f32_16x16x32_bf16 a[72:75], a[168:171], v[96:99], a[72:75]\n\t" \
  "v_exp_f32 v50, v50\n\t" \
  "v_mfma_f32_16x16x32_bf16 a[76:79], a[172:175], v[96:99], a[76:79]\n\t" \
  "s_waitcnt vmcnt(14)\n\t" \
  "ds_write_b128 v254, a[208:211] offset:16384\n\t" \
  "v_mfma_f32_16x16x32_bf16 a[80:83], a[176:179], v[96:99], a[80:83]\n\t" \
  "v_exp_f32 v51, v51\n\t" \
  "v_mfma_f32_16x16x32_bf16 a[84:87], a[180:183], v[96:99], a[84:87]\n\t" \
  "ds_read_b128 a[152:155], v222 offset:4096\n\t" \
  "v_cvt_pk_bf16_f32 v109, v50, v51\n\t" \
  "v_mfma_f32_16x16x32_bf16 a[88:91], a[184:187], v[96:99], a[88:91]\n\t" \
  "v_exp_f32 v52, v52\n\t" \
  "v_mfma_f32_16x16x32_bf16 a[92:95], a[188:191], v[96:99], a[92:95]\n\t" \
  "s_waitcnt vmcnt(13)\n\t" \
  "ds_write_b128 v254, a[212:215] offset:17408\n\t" \
  "v_mfma_f32_16x16x32_bf16 v[128:131], v[224:227], v[96:99], v[128:131]\n\t" \
  "v_exp_f32 v53, v53\n\t" \
  "v_mfma_f32_16x16x32_bf16 a[96:99], a[160:163], v[100:103], a[96:99]\n\t" \
  "ds_read_b128 a[156:159], v223 offset:4096\n\t" \
  "v_cvt_pk_bf16_f32 v110, v52, v53\n\t" \
  "v_mfma_f32_16x16x32_bf16 a[100:103], a[164:167], v[100:103], a[100:103]\n\t" \
  "v_exp_f32 v54, v54\n\t" \
  "v_mfma_f32_16x16x32_bf16 a[104:107], a[168:171], v[100:103], a[104:107]\n\t" \
  "s_waitcnt vmcnt(12)\n\t" \
  "ds_write_b128 v254, a[216:219] offset:18432\n\t" \
  "v_mfma_f32_16x16x32_bf16 a[108:111], a[172:175], v[100:103], a[108:111]\n\t" \
  "v_exp_f32 v55, v55\n\t" \
  "v_mfma_f32_16x16x32_bf16 a[112:115], a[176:179], v[100:103], a[112:115]\n\t" \
  "v_cvt_pk_bf16_f32 v111, v54, v55\n\t" \
  "v_mfma_f32_16x16x32_bf16 a[116:119], a[180:183], v[100:103], a[116:119]\n\t" \
  "s_waitcnt vmcnt(11)\n\t" \
  "ds_write_b128 v254, a[220:223] offset:19456\n\t" \
  "v_mfma_f32_16x16x32_bf16 a[120:123], a[184:187], v[100:103], a[120:123]\n\t" \
  "s_waitcnt vmcnt(10)\n\t" \
  "ds_write_b128 v254, a[224:227] offset:32768\n\t" \
  "v_mfma_f32_16x16x32_bf16 a[124:127], a[188:191], v[100:103], a[124:127]\n\t" \
  "s_waitcnt vmcnt(9)\n\t" \
  "ds_write_b128 v254, a[228:231] offset:33792\n\t" \
  "v_mfma_f32_16x16x32_bf16 v[132:135], v[224:227], v[100:103], v[132:135]\n\t" \
  "s_waitcnt vmcnt(8)\n\t" \
  "ds_write_b128 v254, a[232:235] offset:34816\n\t" \
  "s_waitcnt lgkmcnt(6)\n\t" \
  "v_mfma_f32_16x16x32_bf16 v[8:11], a[128:131], v[136:139], v[72:75]\n\t" \
  "v_exp_f32 v56, v56\n\t" \
  "v_mfma_f32_16x16x32_bf16 v[8:11], a[132:135], v[140:143], v[8:11]\n\t" \
  "ds_read_b64_tr_b16 a[160:161], v200 offset:8192\n\t" \
  "v_mfma_f32_16x16x32_bf16 v[8:11], a[136:139], v[144:147], v[8:11]\n\t" \
  "s_waitcnt vmcnt(7)\n\t" \
  "ds_write_b128 v254, a[236:239] offset:35840\n\t" \
  "v_exp_f32 v57, v57\n\t" \
  "v_mfma_f32_16x16x32_bf16 v[8:11], a[140:143], v[148:151], v[8:11]\n\t" \
  "ds_read_b64_tr_b16 a[162:163], v200 offset:12288\n\t" \
  "v_cvt_pk_bf16_f32 v112, v56, v57\n\t" \
  "v_mfma_f32_16x16x32_bf16 v[12:15], a[144:147], v[136:139], v[72:75]\n\t" \
  "s_mov_b32 s88, 0\n\t" \
  "v_exp_f32 v58, v58\n\t" \
  "v_mfma_f32_16x16x32_bf16 v[12:15], a[148:151], v[140:143], v[12:15]\n\t" \
  "ds_read_b64_tr_b16 a[164:165], v201 offset:8192\n\t" \
  "buffer_load_dwordx4 a[192:195], v202, s[36:39], s88 offen offset:0 sc1\n\t" \
  "v_mfma_f32_16x16x32_bf16 v[12:15], a[152:155], v[144:147], v[12:15]\n\t" \
  "v_exp_f32 v59, v59\n\t" \
  "s_waitcnt lgkmcnt(5)\n\t" \
  "v_mfma_f32_16x16x32_bf16 v[12:15], a[156:159], v[148:151], v[12:15]\n\t" \
  "ds_read_b64_tr_b16 a[166:167], v201 offset:12288\n\t" \
  "v_cvt_pk_bf16_f32 v113, v58, v59\n\t" \
  "v_mfma_f32_16x16x32_bf16 v[16:19], a[128:131], v[152:155], v[76:79]\n\t" \
  "s_waitcnt vmcnt(7)\n\t" \
  "ds_write_b128 v254, a[240:243] offset:49152\n\t" \
  "v_exp_f32 v60, v60\n\t" \
  "v_mfma_f32_16x16x32_bf16 v[16:19], a[132:135], v[156:159], v[16:19]\n\t" \
  "ds_read_b64_tr_b16 a[168:169], v200 offset:8704\n\t" \
  "v_mfma_f32_16x16x32_bf16 v[16:19], a[136:139], v[160:163], v[16:19]\n\t" \
  "v_exp_f32 v61, v61\n\t" \
  "v_mfma_f32_16x16x32_bf16 v[16:19], a[140:143], v[164:167], v[16:19]\n\t" \
  "ds_read_b64_tr_b16 a[170:171], v200 offset:12800\n\t" \
  "buffer_load_dwordx4 a[196:199], v202, s[36:39], s88 offen offset:1024 sc1\n\t" \
  "v_cvt_pk_bf16_f32 v114, v60, v61\n\t" \
  "v_mfma_f32_16x16x32_bf16 v[20:23], a[144:147], v[152:155], v[76:79]\n\t" \
  "v_exp_f32 v62, v62\n\t" \
  "v_mfma_f32_16x16x32_bf16 v[20:23], a[148:151], v[156:159], v[20:23]\n\t" \
  "ds_read_b64_tr_b16 a[172:173], v201 offset:8704\n\t" \
  "v_mfma_f32_16x16x32_bf16 v[20:23], a[152:155], v[160:163], v[20:23]\n\t" \
  "s_waitcnt vmcnt(7)\n\t" \
  "ds_write_b128 v254, a[244:247] offset:50176\n\t" \
  "v_exp_f32 v63, v63\n\t" \
  "v_mfma_f32_16x16x32_bf16 v[20:23], a[156:159], v[164:167], v[20:23]\n\t" \
  "ds_read_b64_tr_b16 a[174:175], v201 offset:12800\n\t" \
  "v_cvt_pk_bf16_f32 v115, v62, v63\n\t" \
  "v_mfma_f32_16x16x32_bf16 v[24:27], a[128:131], v[168:171], v[80:83]\n\t" \
  "v_exp_f32 v64, v64\n\t" \
  "v_mfma_f32_16x16x32_bf16 v[24:27], a[132:135], v[172:175], v[24:27]\n\t" \
  "ds_read_b64_tr_b16 a[176:177], v200 offset:9216\n\t" \
  "buffer_load_dwordx4 a[200:203], v202, s[36:39], s88 offen offset:2048 sc1\n\t" \
  "v_mfma_f32_16x16x32_bf16 v[24:27], a[136:139], v[176:179], v[24:27]\n\t" \
  "v_exp_f32 v65, v65\n\t" \
  "v_mfma_f32_16x16x32_bf16 v[24:27], a[140:143], v[180:183], v[24:27]\n\t" \
  "ds_read_b64_tr_b16 a[178:179], v200 offset:13312\n\t" \
  "v_cvt_pk_bf16_f32 v116, v64, v65\n\t" \
  "v_mfma_f32_16x16x32_bf16 v[28:31], a[144:147], v[168:171], v[80:83]\n\t" \
  "s_waitcnt vmcnt(7)\n\t" \
  "ds_write_b128 v254, a[248:251] offset:51200\n\t" \
  "v_exp_f32 v66, v66\n\t" \
  "v_mfma_f32_16x16x32_bf16 v[28:31], a[148:151], v[172:175], v[28:31]\n\t" \
  "ds_read_b64_tr_b16 a[180:181], v201 offset:9216\n\t" \
  "v_mfma_f32_16x16x32_bf16 v[28:31], a[152:155], v[176:179], v[28:31]\n\t" \
  "v_exp_f32 v67, v67\n\t" \
  "v_mfma_f32_16x16x32_bf16 v[28:31], a[156:159], v[180:183], v[28:31]\n\t" \
  "ds_read_b64_tr_b16 a[182:183], v201 offset:13312\n\t" \
  "buffer_load_dwordx4 a[204:207], v202, s[36:39], s88 offen offset:3072 sc1\n\t" \
  "v_cvt_pk_bf16_f32 v117, v66, v67\n\t" \
  "v_mfma_f32_16x16x32_bf16 v[32:35], a[128:131], v[184:187], v[84:87]\n\t" \
  "v_exp_f32 v68, v68\n\t" \
  "v_mfma_f32_16x16x32_bf16 v[32:35], a[132:135], v[188:191], v[32:35]\n\t" \
  "ds_read_b64_tr_b16 a[184:185], v200 offset:9728\n\t" \
  "v_mfma_f32_16x16x32_bf16 v[32:35], a[136:139], v[192:195], v[32:35]\n\t" \
  "s_waitcnt vmcnt(7)\n\t" \
  "ds_write_b128 v254, a[252:255] offset:52224\n\t" \
  "v_exp_f32 v69, v69\n\t" \
  "v_mfma_f32_16x16x32_bf16 v[32:35], a[140:143], v[196:199], v[32:35]\n\t" \
  "ds_read_b64_tr_b16 a[186:187], v200 offset:13824\n\t" \
  "v_cvt_pk_bf16_f32 v118, v68, v69\n\t" \
  "v_mfma_f32_16x16x32_bf16 v[36:39], a[144:147], v[184:187], v[84:87]\n\t" \
  "s_mov_b32 s88, 4096\n\t" \
  "v_exp_f32 v70, v70\n\t" \
  "v_mfma_f32_16x16x32_bf16 v[36:39], a[148:151], v[188:191], v[36:39]\n\t" \
  "ds_read_b64_tr_b16 a[188:189], v201 offset:9728\n\t" \
  "buffer_load_dwordx4 a[208:211], v202, s[36:39], s88 offen offset:0 sc1\n\t" \
  "v_mfma_f32_16x16x32_bf16 v[36:39], a[152:155], v[192:195], v[36:39]\n\t" \
  "v_exp_f32 v71, v71\n\t" \
  "v_mfma_f32_16x16x32_bf16 v[36:39], a[156:159], v[196:199], v[36:39]\n\t" \
  "ds_read_b64_tr_b16 a[190:191], v201 offset:13824\n\t" \
  "v_cvt_pk_bf16_f32 v119, v70, v71\n\t" \
  "s_waitcnt lgkmcnt(5)\n\t" \
  "v_mfma_f32_16x16x32_bf16 a[0:3], a[160:163], v[104:107], a[0:3]\n\t" \
  "v_exp_f32 v8, v8\n\t" \
  "v_mfma_f32_16x16x32_bf16 a[4:7], a[164:167], v[104:107], a[4:7]\n\t" \
  "ds_read_b128 a[128:131], v220 offset:8192\n\t" \
  "v_mfma_f32_16x16x32_bf16 a[8:11], a[168:171], v[104:107], a[8:11]\n\t" \
  "v_exp_f32 v9, v9\n\t" \
  "v_mfma_f32_16x16x32_bf16 a[12:15], a[172:175], v[104:107], a[12:15]\n\t" \
  "ds_read_b128 a[132:135], v221 offset:8192\n\t" \
  "v_cvt_pk_bf16_f32 v88, v8, v9\n\t" \
  "v_mfma_f32_16x16x32_bf16 a[16:19], a[176:179], v[104:107], a[16:19]\n\t" \
  "v_exp_f32 v10, v10\n\t" \
  "v_mfma_f32_16x16x32_bf16 a[20:23], a[180:183], v[104:107], a[20:23]\n\t" \
  "buffer_load_dwordx4 a[212:215], v202, s[36:39], s88 offen offset:1024 sc1\n\t" \
  "s_waitcnt lgkmcnt(4)\n\t" \
  "v_mfma_f32_16x16x32_bf16 a[24:27], a[184:187], v[104:107], a[24:27]\n\t" \
  "v_exp_f32 v11, v11\n\t" \
  "s_waitcnt lgkmcnt(2)\n\t" \
  "v_mfma_f32_16x16x32_bf16 a[28:31], a[188:191], v[104:107], a[28:31]\n\t" \
  "ds_read_b128 a[136:139], v222 offset:8192\n\t" \
  "v_cvt_pk_bf16_f32 v89, v10, v11\n\t" \
  "v_mfma_f32_16x16x32_bf16 v[120:123], v[224:227], v[104:107], v[120:123]\n\t" \
  "v_exp_f32 v12, v12\n\t" \
  "v_mfma_f32_16x16x32_bf16 a[32:35], a[160:163], v[108:111], a[32:35]\n\t" \
  "buffer_load_dwordx4 a[216:219], v202, s[36:39], s88 offen offset:2048 sc1\n\t" \
  "v_add_u32 v200, 0x8000, v200\n\t" \
  "v_mfma_f32_16x16x32_bf16 a[36:39], a[164:167], v[108:111], a[36:39]\n\t" \
  "v_exp_f32 v13, v13\n\t" \
  "v_mfma_f32_16x16x32_bf16 a[40:43], a[168:171], v[108:111], a[40:43]\n\t" \
  "ds_read_b128 a[140:143], v223 offset:8192\n\t" \
  "v_cvt_pk_bf16_f32 v90, v12, v13\n\t" \
  "v_mfma_f32_16x16x32_bf16 a[44:47], a[172:175], v[108:111], a[44:47]\n\t" \
  "v_exp_f32 v14, v14\n\t" \
  "v_mfma_f32_16x16x32_bf16 a[48:51], a[176:179], v[108:111], a[48:51]\n\t" \
  "buffer_load_dwordx4 a[220:223], v202, s[36:39], s88 offen offset:3072 sc1\n\t" \
  "v_mfma_f32_16x16x32_bf16 a[52:55], a[180:183], v[108:111], a[52:55]\n\t" \
  "v_exp_f32 v15, v15\n\t" \
  "v_mfma_f32_16x16x32_bf16 a[56:59], a[184:187], v[108:111], a[56:59]\n\t" \
  "ds_read_b128 a[144:147], v220 offset:12288\n\t" \
  "v_cvt_pk_bf16_f32 v91, v14, v15\n\t" \
  "v_mfma_f32_16x16x32_bf16 a[60:63], a[188:191], v[108:111], a[60:63]\n\t" \
  "s_mov_b32 s88, 8192\n\t" \
  "v_exp_f32 v16, v16\n\t" \
  "v_mfma_f32_16x16x32_bf16 v[124:127], v[224:227], v[108:111], v[124:127]\n\t" \
  "buffer_load_dwordx4 a[224:227], v202, s[36:39], s88 offen offset:0 sc1\n\t" \
  "v_and_b32 v200, 0x1ffff, v200\n\t" \
  "v_mfma_f32_16x16x32_bf16 a[64:67], a[160:163], v[112:115], a[64:67]\n\t" \
  "v_exp_f32 v17, v17\n\t" \
  "v_mfma_f32_16x16x32_bf16 a[68:71], a[164:167], v[112:115], a[68:71]\n\t" \
  "ds_read_b128 a[148:151], v221 offset:12288\n\t" \
  "v_cvt_pk_bf16_f32 v92, v16, v17\n\t" \
  "v_mfma_f32_16x16x32_bf16 a[72:75], a[168:171], v[112:115], a[72:75]\n\t" \
  "v_exp_f32 v18, v18\n\t" \
  "v_mfma_f32_16x16x32_bf16 a[76:79], a[172:175], v[112:115], a[76:79]\n\t" \
  "buffer_load_dwordx4 a[228:231], v202, s[36:39], s88 offen offset:1024 sc1\n\t" \
  "v_mfma_f32_16x16x32_bf16 a[80:83], a[176:179], v[112:115], a[80:83]\n\t" \
  "v_exp_f32 v19, v19\n\t" \
  "v_mfma_f32_16x16x32_bf16 a[84:87], a[180:183], v[112:115], a[84:87]\n\t" \
  "ds_read_b128 a[152:155], v222 offset:12288\n\t" \
  "v_cvt_pk_bf16_f32 v93, v18, v19\n\t" \
  "v_mfma_f32_16x16x32_bf16 a[88:91], a[184:187], v[112:115], a[88:91]\n\t" \
  "v_exp_f32 v20, v20\n\t" \
  "v_mfma_f32_16x16x32_bf16 a[92:95], a[188:191], v[112:115], a[92:95]\n\t" \
  "buffer_load_dwordx4 a[232:235], v202, s[36:39], s88 offen offset:2048 sc1\n\t" \
  "v_xor_b32 v201, 32, v200\n\t" \
  "v_mfma_f32_16x16x32_bf16 v[128:131], v[224:227], v[112:115], v[128:131]\n\t" \
  "v_exp_f32 v21, v21\n\t" \
  "v_mfma_f32_16x16x32_bf16 a[96:99], a[160:163], v[116:119], a[96:99]\n\t" \
  "ds_read_b128 a[156:159], v223 offset:12288\n\t" \
  "v_cvt_pk_bf16_f32 v94, v20, v21\n\t" \
  "v_mfma_f32_16x16x32_bf16 a[100:103], a[164:167], v[116:119], a[100:103]\n\t" \
  "v_exp_f32 v22, v22\n\t" \
  "v_mfma_f32_16x16x32_bf16 a[104:107], a[168:171], v[116:119], a[104:107]\n\t" \
  "buffer_load_dwordx4 a[236:239], v202, s[36:39], s88 offen offset:3072 sc1\n\t" \
  "v_mfma_f32_16x16x32_bf16 a[108:111], a[172:175], v[116:119], a[108:111]\n\t" \
  "v_exp_f32 v23, v23\n\t" \
  "v_mfma_f32_16x16x32_bf16 a[112:115], a[176:179], v[116:119], a[112:115]\n\t" \
  "s_mov_b32 s88, 12288\n\t" \
  "v_cvt_pk_bf16_f32 v95, v22, v23\n\t" \
  "v_mfma_f32_16x16x32_bf16 a[116:119], a[180:183], v[116:119], a[116:119]\n\t" \
  "buffer_load_dwordx4 a[240:243], v202, s[36:39], s88 offen offset:0 sc1\n\t" \
  "v_mfma_f32_16x16x32_bf16 a[120:123], a[184:187], v[116:119], a[120:123]\n\t" \
  "buffer_load_dwordx4 a[244:247], v202, s[36:39], s88 offen offset:1024 sc1\n\t" \
  "v_mfma_f32_16x16x32_bf16 a[124:127], a[188:191], v[116:119], a[124:127]\n\t" \
  "buffer_load_dwordx4 a[248:251], v202, s[36:39], s88 offen offset:2048 sc1\n\t" \
  "v_mfma_f32_16x16x32_bf16 v[132:135], v[224:227], v[116:119], v[132:135]\n\t" \
  "buffer_load_dwordx4 a[252:255], v202, s[36:39], s88 offen offset:3072 sc1\n\t" \
  "s_waitcnt lgkmcnt(1)\n\t" \
  "v_mfma_f32_16x16x32_bf16 v[40:43], a[128:131], v[136:139], v[72:75]\n\t" \
  "s_barrier\n\t" \
  "v_exp_f32 v24, v24\n\t" \
  "v_mfma_f32_16x16x32_bf16 v[40:43], a[132:135], v[140:143], v[40:43]\n\t" \
  "ds_read_b64_tr_b16 a[160:161], v200 offset:0\n\t" \
  "v_mfma_f32_16x16x32_bf16 v[40:43], a[136:139], v[144:147], v[40:43]\n\t" \
  "v_exp_f32 v25, v25\n\t" \
  "v_mfma_f32_16x16x32_bf16 v[40:43], a[140:143], v[148:151], v[40:43]\n\t" \
  "ds_read_b64_tr_b16 a[162:163], v200 offset:4096\n\t" \
  "v_cvt_pk_bf16_f32 v96, v24, v25\n\t" \
  "v_mfma_f32_16x16x32_bf16 v[44:47], a[144:147], v[136:139], v[72:75]\n\t" \
  "v_exp_f32 v26, v26\n\t" \
  "v_mfma_f32_16x16x32_bf16 v[44:47], a[148:151], v[140:143], v[44:47]\n\t" \
  "ds_read_b64_tr_b16 a[164:165], v201 offset:0\n\t" \
  "v_mfma_f32_16x16x32_bf16 v[44:47], a[152:155], v[144:147], v[44:47]\n\t" \
  "v_exp_f32 v27, v27\n\t" \
  "s_waitcnt lgkmcnt(3)\n\t" \
  "v_mfma_f32_16x16x32_bf16 v[44:47], a[156:159], v[148:151], v[44:47]\n\t" \
  "ds_read_b64_tr_b16 a[166:167], v201 offset:4096\n\t" \
  "v_cvt_pk_bf16_f32 v97, v26, v27\n\t" \
  "v_mfma_f32_16x16x32_bf16 v[48:51], a[128:131], v[152:155], v[76:79]\n\t" \
  "v_exp_f32 v28, v28\n\t" \
  "v_mfma_f32_16x16x32_bf16 v[48:51], a[132:135], v[156:159], v[48:51]\n\t" \
  "ds_read_b64_tr_b16 a[168:169], v200 offset:512\n\t" \
  "v_mfma_f32_16x16x32_bf16 v[48:51], a[136:139], v[160:163], v[48:51]\n\t" \
  "v_exp_f32 v29, v29\n\t" \
  "v_mfma_f32_16x16x32_bf16 v[48:51], a[140:143], v[164:167], v[48:51]\n\t" \
  "ds_read_b64_tr_b16 a[170:171], v200 offset:4608\n\t" \
  "v_cvt_pk_bf16_f32 v98, v28, v29\n\t" \
  "v_mfma_f32_16x16x32_bf16 v[52:55], a[144:147], v[152:155], v[76:79]\n\t" \
  "v_exp_f32 v30, v30\n\t" \
  "v_mfma_f32_16x16x32_bf16 v[52:55], a[148:151], v[156:159], v[52:55]\n\t" \
  "ds_read_b64_tr_b16 a[172:173], v201 offset:512\n\t" \
  "v_mfma_f32_16x16x32_bf16 v[52:55], a[152:155], v[160:163], v[52:55]\n\t" \
  "v_exp_f32 v31, v31\n\t" \
  "v_mfma_f32_16x16x32_bf16 v[52:55], a[156:159], v[164:167], v[52:55]\n\t" \
  "ds_read_b64_tr_b16 a[174:175], v201 offset:4608\n\t" \
  "v_cvt_pk_bf16_f32 v99, v30, v31\n\t" \
  "v_mfma_f32_16x16x32_bf16 v[56:59], a[128:131], v[168:171], v[80:83]\n\t" \
  "v_exp_f32 v32, v32\n\t" \
  "v_mfma_f32_16x16x32_bf16 v[56:59], a[132:135], v[172:175], v[56:59]\n\t" \
  "ds_read_b64_tr_b16 a[176:177], v200 offset:1024\n\t" \
  "v_mfma_f32_16x16x32_bf16 v[56:59], a[136:139], v[176:179], v[56:59]\n\t" \
  "v_exp_f32 v33, v33\n\t" \
  "v_mfma_f32_16x16x32_bf16 v[56:59], a[140:143], v[180:183], v[56:59]\n\t" \
  "ds_read_b64_tr_b16 a[178:179], v200 offset:5120\n\t" \
  "v_cvt_pk_bf16_f32 v100, v32, v33\n\t" \
  "v_mfma_f32_16x16x32_bf16 v[60:63], a[144:147], v[168:171], v[80:83]\n\t" \
  "v_exp_f32 v34, v34\n\t" \
  "v_mfma_f32_16x16x32_bf16 v[60:63], a[148:151], v[172:175], v[60:63]\n\t" \
  "ds_read_b64_tr_b16 a[180:181], v201 offset:1024\n\t" \
  "v_mfma_f32_16x16x32_bf16 v[60:63], a[152:155], v[176:179], v[60:63]\n\t" \
  "v_exp_f32 v35, v35\n\t" \
  "v_mfma_f32_16x16x32_bf16 v[60:63], a[156:159], v[180:183], v[60:63]\n\t" \
  "ds_read_b64_tr_b16 a[182:183], v201 offset:5120\n\t" \
  "v_cvt_pk_bf16_f32 v101, v34, v35\n\t" \
  "v_mfma_f32_16x16x32_bf16 v[64:67], a[128:131], v[184:187], v[84:87]\n\t" \
  "v_exp_f32 v36, v36\n\t" \
  "v_mfma_f32_16x16x32_bf16 v[64:67], a[132:135], v[188:191], v[64:67]\n\t" \
  "ds_read_b64_tr_b16 a[184:185], v200 offset:1536\n\t" \
  "v_mfma_f32_16x16x32_bf16 v[64:67], a[136:139], v[192:195], v[64:67]\n\t" \
  "v_exp_f32 v37, v37\n\t" \
  "v_mfma_f32_16x16x32_bf16 v[64:67], a[140:143], v[196:199], v[64:67]\n\t" \
  "ds_read_b64_tr_b16 a[186:187], v200 offset:5632\n\t" \
  "v_cvt_pk_bf16_f32 v102, v36, v37\n\t" \
  "v_mfma_f32_16x16x32_bf16 v[68:71], a[144:147], v[184:187], v[84:87]\n\t" \
  "v_exp_f32 v38, v38\n\t" \
  "v_mfma_f32_16x16x32_bf16 v[68:71], a[148:151], v[188:191], v[68:71]\n\t" \
  "ds_read_b64_tr_b16 a[188:189], v201 offset:1536\n\t" \
  "v_mfma_f32_16x16x32_bf16 v[68:71], a[152:155], v[192:195], v[68:71]\n\t" \
  "v_exp_f32 v39, v39\n\t" \
  "v_mfma_f32_16x16x32_bf16 v[68:71], a[156:159], v[196:199], v[68:71]\n\t" \
  "ds_read_b64_tr_b16 a[190:191], v201 offset:5632\n\t" \
  "v_cvt_pk_bf16_f32 v103, v38, v39\n\t" \
  "s_waitcnt lgkmcnt(5)\n\t" \
  "v_mfma_f32_16x16x32_bf16 a[0:3], a[160:163], v[88:91], a[0:3]\n\t" \
  "v_exp_f32 v40, v40\n\t" \
  "v_mfma_f32_16x16x32_bf16 a[4:7], a[164:167], v[88:91], a[4:7]\n\t" \
  "ds_read_b64_tr_b16 a[128:129], v200 offset:8192\n\t" \
  "v_mfma_f32_16x16x32_bf16 a[8:11], a[168:171], v[88:91], a[8:11]\n\t" \
  "v_exp_f32 v41, v41\n\t" \
  "v_mfma_f32_16x16x32_bf16 a[12:15], a[172:175], v[88:91], a[12:15]\n\t" \
  "ds_read_b64_tr_b16 a[130:131], v200 offset:12288\n\t" \
  "v_cvt_pk_bf16_f32 v104, v40, v41\n\t" \
  "v_mfma_f32_16x16x32_bf16 a[16:19], a[176:179], v[88:91], a[16:19]\n\t" \
  "v_exp_f32 v42, v42\n\t" \
  "s_waitcnt lgkmcnt(4)\n\t" \
  "v_mfma_f32_16x16x32_bf16 a[20:23], a[180:183], v[88:91], a[20:23]\n\t" \
  "ds_read_b64_tr_b16 a[132:133], v201 offset:8192\n\t" \
  "v_mfma_f32_16x16x32_bf16 a[24:27], a[184:187], v[88:91], a[24:27]\n\t" \
  "v_exp_f32 v43, v43\n\t" \
  "s_waitcnt lgkmcnt(3)\n\t" \
  "v_mfma_f32_16x16x32_bf16 a[28:31], a[188:191], v[88:91], a[28:31]\n\t" \
  "ds_read_b64_tr_b16 a[134:135], v201 offset:12288\n\t" \
  "v_cvt_pk_bf16_f32 v105, v42, v43\n\t" \
  "v_mfma_f32_16x16x32_bf16 v[120:123], v[224:227], v[88:91], v[120:123]\n\t" \
  "v_exp_f32 v44, v44\n\t" \
  "v_mfma_f32_16x16x32_bf16 a[32:35], a[160:163], v[92:95], a[32:35]\n\t" \
  "ds_read_b64_tr_b16 a[136:137], v200 offset:8704\n\t" \
  "v_mfma_f32_16x16x32_bf16 a[36:39], a[164:167], v[92:95], a[36:39]\n\t" \
  "v_exp_f32 v45, v45\n\t" \
  "v_mfma_f32_16x16x32_bf16 a[40:43], a[168:171], v[92:95], a[40:43]\n\t" \
  "ds_read_b64_tr_b16 a[138:139], v200 offset:12800\n\t" \
  "v_cvt_pk_bf16_f32 v106, v44, v45\n\t" \
  "v_mfma_f32_16x16x32_bf16 a[44:47], a[172:175], v[92:95], a[44:47]\n\t" \
  "v_exp_f32 v46, v46\n\t" \
  "v_mfma_f32_16x16x32_bf16 a[48:51], a[176:179], v[92:95], a[48:51]\n\t" \
  "ds_read_b64_tr_b16 a[140:141], v201 offset:8704\n\t" \
  "v_mfma_f32_16x16x32_bf16 a[52:55], a[180:183], v[92:95], a[52:55]\n\t" \
  "v_exp_f32 v47, v47\n\t" \
  "v_mfma_f32_16x16x32_bf16 a[56:59], a[184:187], v[92:95], a[56:59]\n\t" \
  "ds_read_b64_tr_b16 a[142:143], v201 offset:12800\n\t" \
  "v_cvt_pk_bf16_f32 v107, v46, v47\n\t" \
  "v_mfma_f32_16x16x32_bf16 a[60:63], a[188:191], v[92:95], a[60:63]\n\t" \
  "v_exp_f32 v48, v48\n\t" \
  "v_mfma_f32_16x16x32_bf16 v[124:127], v[224:227], v[92:95], v[124:127]\n\t" \
  "ds_read_b64_tr_b16 a[144:145], v200 offset:9216\n\t" \
  "v_mfma_f32_16x16x32_bf16 a[64:67], a[160:163], v[96:99], a[64:67]\n\t" \
  "v_exp_f32 v49, v49\n\t" \
  "v_mfma_f32_16x16x32_bf16 a[68:71], a[164:167], v[96:99], a[68:71]\n\t" \
  "ds_read_b64_tr_b16 a[146:147], v200 offset:13312\n\t" \
  "v_cvt_pk_bf16_f32 v108, v48, v49\n\t" \
  "v_mfma_f32_16x16x32_bf16 a[72:75], a[168:171], v[96:99], a[72:75]\n\t" \
  "v_exp_f32 v50, v50\n\t" \
  "v_mfma_f32_16x16x32_bf16 a[76:79], a[172:175], v[96:99], a[76:79]\n\t" \
  "ds_read_b64_tr_b16 a[148:149], v201 offset:9216\n\t" \
  "v_mfma_f32_16x16x32_bf16 a[80:83], a[176:179], v[96:99], a[80:83]\n\t" \
  "v_exp_f32 v51, v51\n\t" \
  "v_mfma_f32_16x16x32_bf16 a[84:87], a[180:183], v[96:99], a[84:87]\n\t" \
  "ds_read_b64_tr_b16 a[150:151], v201 offset:13312\n\t" \
  "v_cvt_pk_bf16_f32 v109, v50, v51\n\t" \
  "v_mfma_f32_16x16x32_bf16 a[88:91], a[184:187], v[96:99], a[88:91]\n\t" \
  "v_exp_f32 v52, v52\n\t" \
  "v_mfma_f32_16x16x32_bf16 a[92:95], a[188:191], v[96:99], a[92:95]\n\t" \
  "ds_read_b64_tr_b16 a[152:153], v200 offset:9728\n\t" \
  "v_mfma_f32_16x16x32_bf16 v[128:131], v[224:227], v[96:99], v[128:131]\n\t" \
  "v_exp_f32 v53, v53\n\t" \
  "v_mfma_f32_16x16x32_bf16 a[96:99], a[160:163], v[100:103], a[96:99]\n\t" \
  "ds_read_b64_tr_b16 a[154:155], v200 offset:13824\n\t" \
  "v_cvt_pk_bf16_f32 v110, v52, v53\n\t" \
  "v_mfma_f32_16x16x32_bf16 a[100:103], a[164:167], v[100:103], a[100:103]\n\t" \
  "v_exp_f32 v54, v54\n\t" \
  "v_mfma_f32_16x16x32_bf16 a[104:107], a[168:171], v[100:103], a[104:107]\n\t" \
  "ds_read_b64_tr_b16 a[156:157], v201 offset:9728\n\t" \
  "v_mfma_f32_16x16x32_bf16 a[108:111], a[172:175], v[100:103], a[108:111]\n\t" \
  "v_exp_f32 v55, v55\n\t" \
  "v_mfma_f32_16x16x32_bf16 a[112:115], a[176:179], v[100:103], a[112:115]\n\t" \
  "ds_read_b64_tr_b16 a[158:159], v201 offset:13824\n\t" \
  "v_cvt_pk_bf16_f32 v111, v54, v55\n\t" \
  "v_mfma_f32_16x16x32_bf16 a[116:119], a[180:183], v[100:103], a[116:119]\n\t" \
  "v_mfma_f32_16x16x32_bf16 a[120:123], a[184:187], v[100:103], a[120:123]\n\t" \
  "v_mfma_f32_16x16x32_bf16 a[124:127], a[188:191], v[100:103], a[124:127]\n\t" \
  "v_mfma_f32_16x16x32_bf16 v[132:135], v[224:227], v[100:103], v[132:135]\n\t" \
  ".Ltailc%=:\n\t" \
  "s_waitcnt lgkmcnt(4)\n\t" \
  "v_mfma_f32_16x16x32_bf16 a[0:3], a[128:131], v[104:107], a[0:3]\n\t" \
  "v_exp_f32 v56, v56\n\t" \
  "v_exp_f32 v57, v57\n\t" \
  "v_mfma_f32_16x16x32_bf16 a[4:7], a[132:135], v[104:107], a[4:7]\n\t" \
  "v_exp_f32 v58, v58\n\t" \
  "v_mfma_f32_16x16x32_bf16 a[8:11], a[136:139], v[104:107], a[8:11]\n\t" \
  "v_cvt_pk_bf16_f32 v112, v56, v57\n\t" \
  "v_exp_f32 v59, v59\n\t" \
  "v_mfma_f32_16x16x32_bf16 a[12:15], a[140:143], v[104:107], a[12:15]\n\t" \
  "v_exp_f32 v60, v60\n\t" \
  "v_mfma_f32_16x16x32_bf16 a[16:19], a[144:147], v[104:107], a[16:19]\n\t" \
  "v_cvt_pk_bf16_f32 v113, v58, v59\n\t" \
  "v_exp_f32 v61, v61\n\t" \
  "v_mfma_f32_16x16x32_bf16 a[20:23], a[148:151], v[104:107], a[20:23]\n\t" \
  "v_exp_f32 v62, v62\n\t" \
  "s_waitcnt lgkmcnt(0)\n\t" \
  "v_mfma_f32_16x16x32_bf16 a[24:27], a[152:155], v[104:107], a[24:27]\n\t" \
  "v_cvt_pk_bf16_f32 v114, v60, v61\n\t" \
  "v_exp_f32 v63, v63\n\t" \
  "v_mfma_f32_16x16x32_bf16 a[28:31], a[156:159], v[104:107], a[28:31]\n\t" \
  "v_exp_f32 v64, v64\n\t" \
  "v_mfma_f32_16x16x32_bf16 v[120:123], v[224:227], v[104:107], v[120:123]\n\t" \
  "v_cvt_pk_bf16_f32 v115, v62, v63\n\t" \
  "v_exp_f32 v65, v65\n\t" \
  "v_mfma_f32_16x16x32_bf16 a[32:35], a[128:131], v[108:111], a[32:35]\n\t" \
  "v_exp_f32 v66, v66\n\t" \
  "v_mfma_f32_16x16x32_bf16 a[36:39], a[132:135], v[108:111], a[36:39]\n\t" \
  "v_cvt_pk_bf16_f32 v116, v64, v65\n\t" \
  "v_exp_f32 v67, v67\n\t" \
  "v_mfma_f32_16x16x32_bf16 a[40:43], a[136:139], v[108:111], a[40:43]\n\t" \
  "v_exp_f32 v68, v68\n\t" \
  "v_mfma_f32_16x16x32_bf16 a[44:47], a[140:143], v[108:111], a[44:47]\n\t" \
  "v_cvt_pk_bf16_f32 v117, v66, v67\n\t" \
  "v_exp_f32 v69, v69\n\t" \
  "v_mfma_f32_16x16x32_bf16 a[48:51], a[144:147], v[108:111], a[48:51]\n\t" \
  "v_exp_f32 v70, v70\n\t" \
  "v_mfma_f32_16x16x32_bf16 a[52:55], a[148:151], v[108:111], a[52:55]\n\t" \
  "v_cvt_pk_bf16_f32 v118, v68, v69\n\t" \
  "v_exp_f32 v71, v71\n\t" \
  "v_mfma_f32_16x16x32_bf16 a[56:59], a[152:155], v[108:111], a[56:59]\n\t" \
  "v_cvt_pk_bf16_f32 v119, v70, v71\n\t" \
  "v_mfma_f32_16x16x32_bf16 a[60:63], a[156:159], v[108:111], a[60:63]\n\t" \
  "v_mfma_f32_16x16x32_bf16 v[124:127], v[224:227], v[108:111], v[124:127]\n\t" \
  "v_mfma_f32_16x16x32_bf16 a[64:67], a[128:131], v[112:115], a[64:67]\n\t" \
  "v_mfma_f32_16x16x32_bf16 a[68:71], a[132:135], v[112:115], a[68:71]\n\t" \
  "v_mfma_f32_16x16x32_bf16 a[72:75], a[136:139], v[112:115], a[72:75]\n\t" \
  "s_mov_b32 s84, 0x5d800000\n\t" \
  "s_mov_b32 s85, 0x1000\n\t" \
  "s_mov_b32 s86, 0x2000\n\t" \
  "s_mov_b32 s87, 0x3000\n\t" \
  "v_mfma_f32_16x16x32_bf16 a[76:79], a[140:143], v[112:115], a[76:79]\n\t" \
  "v_mfma_f32_16x16x32_bf16 a[80:83], a[144:147], v[112:115], a[80:83]\n\t" \
  "v_rcp_f32 v228, v120\n\t" \
  "v_mfma_f32_16x16x32_bf16 a[84:87], a[148:151], v[112:115], a[84:87]\n\t" \
  "v_mfma_f32_16x16x32_bf16 a[88:91], a[152:155], v[112:115], a[88:91]\n\t" \
  "v_mfma_f32_16x16x32_bf16 a[92:95], a[156:159], v[112:115], a[92:95]\n\t" \
  "v_mfma_f32_16x16x32_bf16 v[128:131], v[224:227], v[112:115], v[128:131]\n\t" \
  "v_rcp_f32 v230, v124\n\t" \
  "v_mfma_f32_16x16x32_bf16 v[132:135], v[224:227], v[116:119], v[132:135]\n\t" \
  "v_mfma_f32_16x16x32_bf16 a[96:99], a[128:131], v[116:119], a[96:99]\n\t" \
  "v_add_f32 v240, v120, v124\n\t" \
  "v_mfma_f32_16x16x32_bf16 a[100:103], a[132:135], v[116:119], a[100:103]\n\t" \
  "v_mfma_f32_16x16x32_bf16 a[104:107], a[136:139], v[116:119], a[104:107]\n\t" \
  "v_mfma_f32_16x16x32_bf16 a[108:111], a[140:143], v[116:119], a[108:111]\n\t" \
  "v_mfma_f32_16x16x32_bf16 a[112:115], a[144:147], v[116:119], a[112:115]\n\t" \
  "v_mfma_f32_16x16x32_bf16 a[116:119], a[148:151], v[116:119], a[116:119]\n\t" \
  "v_mfma_f32_16x16x32_bf16 a[120:123], a[152:155], v[116:119], a[120:123]\n\t" \
  "v_mfma_f32_16x16x32_bf16 a[124:127], a[156:159], v[116:119], a[124:127]\n\t" \
  "v_add_f32 v241, v128, v132\n\t" \
  "v_rcp_f32 v232, v128\n\t" \
  "v_add_f32 v240, v240, v241\n\t" \
  "v_rcp_f32 v234, v132\n\t" \
  "v_cmp_nlt_f32 vcc, v240, s84\n\t" \
  "s_cbranch_vccnz .Labad%=\n\t" \
  "s_lshr_b32 s84, %[head], 4\n\t" \
  "s_xor_b32 s84, s84, %[w]\n\t" \
  "s_and_b32 s84, s84, 1\n\t" \
  "s_lshl_b32 s50, s84, 14\n\t" \
  "v_accvgpr_read_b32 v40, a0\n\t" \
  "v_accvgpr_read_b32 v41, a1\n\t" \
  "v_accvgpr_read_b32 v42, a2\n\t" \
  "v_accvgpr_read_b32 v43, a3\n\t" \
  "v_accvgpr_read_b32 v44, a4\n\t" \
  "v_accvgpr_read_b32 v45, a5\n\t" \
  "v_accvgpr_read_b32 v46, a6\n\t" \
  "v_accvgpr_read_b32 v47, a7\n\t" \
  "v_accvgpr_read_b32 v48, a8\n\t" \
  "v_accvgpr_read_b32 v49, a9\n\t" \
  "v_accvgpr_read_b32 v50, a10\n\t" \
  "v_accvgpr_read_b32 v51, a11\n\t" \
  "v_accvgpr_read_b32 v52, a12\n\t" \
  "v_accvgpr_read_b32 v53, a13\n\t" \
  "v_accvgpr_read_b32 v54, a14\n\t" \
  "v_accvgpr_read_b32 v55, a15\n\t" \
  "v_pk_mul_f32 v[40:41], v[40:41], v[228:229] op_sel_hi:[1,0]\n\t" \
  "v_pk_mul_f32 v[42:43], v[42:43], v[228:229] op_sel_hi:[1,0]\n\t" \
  "v_pk_mul_f32 v[44:45], v[44:45], v[228:229] op_sel_hi:[1,0]\n\t" \
  "v_pk_mul_f32 v[46:47], v[46:47], v[228:229] op_sel_hi:[1,0]\n\t" \
  "v_pk_mul_f32 v[48:49], v[48:49], v[228:229] op_sel_hi:[1,0]\n\t" \
  "v_pk_mul_f32 v[50:51], v[50:51], v[228:229] op_sel_hi:[1,0]\n\t" \
  "v_pk_mul_f32 v[52:53], v[52:53], v[228:229] op_sel_hi:[1,0]\n\t" \
  "v_pk_mul_f32 v[54:55], v[54:55], v[228:229] op_sel_hi:[1,0]\n\t" \
  "v_cvt_pk_bf16_f32 v136, v40, v41\n\t" \
  "v_cvt_pk_bf16_f32 v137, v42, v43\n\t" \
  "v_cvt_pk_bf16_f32 v138, v44, v45\n\t" \
  "v_cvt_pk_bf16_f32 v139, v46, v47\n\t" \
  "v_cvt_pk_bf16_f32 v140, v48, v49\n\t" \
  "v_cvt_pk_bf16_f32 v141, v50, v51\n\t" \
  "v_cvt_pk_bf16_f32 v142, v52, v53\n\t" \
  "v_cvt_pk_bf16_f32 v143, v54, v55\n\t" \
  "buffer_store_dwordx4 v[136:139], v202, s[48:51], 0 offen offset:0 sc1\n\t" \
  "buffer_store_dwordx4 v[140:143], v202, s[48:51], s85 offen offset:0 sc1\n\t" \
  "v_accvgpr_read_b32 v56, a16\n\t" \
  "v_accvgpr_read_b32 v57, a17\n\t" \
  "v_accvgpr_read_b32 v58, a18\n\t" \
  "v_accvgpr_read_b32 v59, a19\n\t" \
  "v_accvgpr_read_b32 v60, a20\n\t" \
  "v_accvgpr_read_b32 v61, a21\n\t" \
  "v_accvgpr_read_b32 v62, a22\n\t" \
  "v_accvgpr_read_b32 v63, a23\n\t" \
  "v_accvgpr_read_b32 v64, a24\n\t" \
  "v_accvgpr_read_b32 v65, a25\n\t" \
  "v_accvgpr_read_b32 v66, a26\n\t" \
  "v_accvgpr_read_b32 v67, a27\n\t" \
  "v_accvgpr_read_b32 v68, a28\n\t" \
  "v_accvgpr_read_b32 v69, a29\n\t" \
  "v_accvgpr_read_b32 v70, a30\n\t" \
  "v_accvgpr_read_b32 v71, a31\n\t" \
  "v_pk_mul_f32 v[56:57], v[56:57], v[228:229] op_sel_hi:[1,0]\n\t" \
  "v_pk_mul_f32 v[58:59], v[58:59], v[228:229] op_sel_hi:[1,0]\n\t" \
  "v_pk_mul_f32 v[60:61], v[60:61], v[228:229] op_sel_hi:[1,0]\n\t" \
  "v_pk_mul_f32 v[62:63], v[62:63], v[228:229] op_sel_hi:[1,0]\n\t" \
  "v_pk_mul_f32 v[64:65], v[64:65], v[228:229] op_sel_hi:[1,0]\n\t" \
  "v_pk_mul_f32 v[66:67], v[66:67], v[228:229] op_sel_hi:[1,0]\n\t" \
  "v_pk_mul_f32 v[68:69], v[68:69], v[228:229] op_sel_hi:[1,0]\n\t" \
  "v_pk_mul_f32 v[70:71], v[70:71], v[228:229] op_sel_hi:[1,0]\n\t" \
  "v_cvt_pk_bf16_f32 v144, v56, v57\n\t" \
  "v_cvt_pk_bf16_f32 v145, v58, v59\n\t" \
  "v_cvt_pk_bf16_f32 v146, v60, v61\n\t" \
  "v_cvt_pk_bf16_f32 v147, v62, v63\n\t" \
  "v_cvt_pk_bf16_f32 v148, v64, v65\n\t" \
  "v_cvt_pk_bf16_f32 v149, v66, v67\n\t" \
  "v_cvt_pk_bf16_f32 v150, v68, v69\n\t" \
  "v_cvt_pk_bf16_f32 v151, v70, v71\n\t" \
  "buffer_store_dwordx4 v[144:147], v202, s[48:51], s86 offen offset:0 sc1\n\t" \
  "buffer_store_dwordx4 v[148:151], v202, s[48:51], s87 offen offset:0 sc1\n\t" \
  "v_accvgpr_read_b32 v72, a32\n\t" \
  "v_accvgpr_read_b32 v73, a33\n\t" \
  "v_accvgpr_read_b32 v74, a34\n\t" \
  "v_accvgpr_read_b32 v75, a35\n\t" \
  "v_accvgpr_read_b32 v76, a36\n\t" \
  "v_accvgpr_read_b32 v77, a37\n\t" \
  "v_accvgpr_read_b32 v78, a38\n\t" \
  "v_accvgpr_read_b32 v79, a39\n\t" \
  "v_accvgpr_read_b32 v80, a40\n\t" \
  "v_accvgpr_read_b32 v81, a41\n\t" \
  "v_accvgpr_read_b32 v82, a42\n\t" \
  "v_accvgpr_read_b32 v83, a43\n\t" \
  "v_accvgpr_read_b32 v84, a44\n\t" \
  "v_accvgpr_read_b32 v85, a45\n\t" \
  "v_accvgpr_read_b32 v86, a46\n\t" \
  "v_accvgpr_read_b32 v87, a47\n\t" \
  "v_pk_mul_f32 v[72:73], v[72:73], v[230:231] op_sel_hi:[1,0]\n\t" \
  "v_pk_mul_f32 v[74:75], v[74:75], v[230:231] op_sel_hi:[1,0]\n\t" \
  "v_pk_mul_f32 v[76:77], v[76:77], v[230:231] op_sel_hi:[1,0]\n\t" \
  "v_pk_mul_f32 v[78:79], v[78:79], v[230:231] op_sel_hi:[1,0]\n\t" \
  "v_pk_mul_f32 v[80:81], v[80:81], v[230:231] op_sel_hi:[1,0]\n\t" \
  "v_pk_mul_f32 v[82:83], v[82:83], v[230:231] op_sel_hi:[1,0]\n\t" \
  "v_pk_mul_f32 v[84:85], v[84:85], v[230:231] op_sel_hi:[1,0]\n\t" \
  "v_pk_mul_f32 v[86:87], v[86:87], v[230:231] op_sel_hi:[1,0]\n\t" \
  "v_cvt_pk_bf16_f32 v152, v72, v73\n\t" \
  "v_cvt_pk_bf16_f32 v153, v74, v75\n\t" \
  "v_cvt_pk_bf16_f32 v154, v76, v77\n\t" \
  "v_cvt_pk_bf16_f32 v155, v78, v79\n\t" \
  "v_cvt_pk_bf16_f32 v156, v80, v81\n\t" \
  "v_cvt_pk_bf16_f32 v157, v82, v83\n\t" \
  "v_cvt_pk_bf16_f32 v158, v84, v85\n\t" \
  "v_cvt_pk_bf16_f32 v159, v86, v87\n\t" \
  "buffer_store_dwordx4 v[152:155], v202, s[48:51], 0 offen offset:1024 sc1\n\t" \
  "buffer_store_dwordx4 v[156:159], v202, s[48:51], s85 offen offset:1024 sc1\n\t" \
  "v_accvgpr_read_b32 v88, a48\n\t" \
  "v_accvgpr_read_b32 v89, a49\n\t" \
  "v_accvgpr_read_b32 v90, a50\n\t" \
  "v_accvgpr_read_b32 v91, a51\n\t" \
  "v_accvgpr_read_b32 v92, a52\n\t" \
  "v_accvgpr_read_b32 v93, a53\n\t" \
  "v_accvgpr_read_b32 v94, a54\n\t" \
  "v_accvgpr_read_b32 v95, a55\n\t" \
  "v_accvgpr_read_b32 v96, a56\n\t" \
  "v_accvgpr_read_b32 v97, a57\n\t" \
  "v_accvgpr_read_b32 v98, a58\n\t" \
  "v_accvgpr_read_b32 v99, a59\n\t" \
  "v_accvgpr_read_b32 v100, a60\n\t" \
  "v_accvgpr_read_b32 v101, a61\n\t" \
  "v_accvgpr_read_b32 v102, a62\n\t" \
  "v_accvgpr_read_b32 v103, a63\n\t" \
  "v_pk_mul_f32 v[88:89], v[88:89], v[230:231] op_sel_hi:[1,0]\n\t" \
  "v_pk_mul_f32 v[90:91], v[90:91], v[230:231] op_sel_hi:[1,0]\n\t" \
  "v_pk_mul_f32 v[92:93], v[92:93], v[230:231] op_sel_hi:[1,0]\n\t" \
  "v_pk_mul_f32 v[94:95], v[94:95], v[230:231] op_sel_hi:[1,0]\n\t" \
  "v_pk_mul_f32 v[96:97], v[96:97], v[230:231] op_sel_hi:[1,0]\n\t" \
  "v_pk_mul_f32 v[98:99], v[98:99], v[230:231] op_sel_hi:[1,0]\n\t" \
  "v_pk_mul_f32 v[100:101], v[100:101], v[230:231] op_sel_hi:[1,0]\n\t" \
  "v_pk_mul_f32 v[102:103], v[102:103], v[230:231] op_sel_hi:[1,0]\n\t" \
  "v_cvt_pk_bf16_f32 v160, v88, v89\n\t" \
  "v_cvt_pk_bf16_f32 v161, v90, v91\n\t" \
  "v_cvt_pk_bf16_f32 v162, v92, v93\n\t" \
  "v_cvt_pk_bf16_f32 v163, v94, v95\n\t" \
  "v_cvt_pk_bf16_f32 v164, v96, v97\n\t" \
  "v_cvt_pk_bf16_f32 v165, v98, v99\n\t" \
  "v_cvt_pk_bf16_f32 v166, v100, v101\n\t" \
  "v_cvt_pk_bf16_f32 v167, v102, v103\n\t" \
  "buffer_store_dwordx4 v[160:163], v202, s[48:51], s86 offen offset:1024 sc1\n\t" \
  "buffer_store_dwordx4 v[164:167], v202, s[48:51], s87 offen offset:1024 sc1\n\t" \
  "v_accvgpr_read_b32 v104, a64\n\t" \
  "v_accvgpr_read_b32 v105, a65\n\t" \
  "v_accvgpr_read_b32 v106, a66\n\t" \
  "v_accvgpr_read_b32 v107, a67\n\t" \
  "v_accvgpr_read_b32 v108, a68\n\t" \
  "v_accvgpr_read_b32 v109, a69\n\t" \
  "v_accvgpr_read_b32 v110, a70\n\t" \
  "v_accvgpr_read_b32 v111, a71\n\t" \
  "v_accvgpr_read_b32 v112, a72\n\t" \
  "v_accvgpr_read_b32 v113, a73\n\t" \
  "v_accvgpr_read_b32 v114, a74\n\t" \
  "v_accvgpr_read_b32 v115, a75\n\t" \
  "v_accvgpr_read_b32 v116, a76\n\t" \
  "v_accvgpr_read_b32 v117, a77\n\t" \
  "v_accvgpr_read_b32 v118, a78\n\t" \
  "v_accvgpr_read_b32 v119, a79\n\t" \
  "v_pk_mul_f32 v[104:105], v[104:105], v[232:233] op_sel_hi:[1,0]\n\t" \
  "v_pk_mul_f32 v[106:107], v[106:107], v[232:233] op_sel_hi:[1,0]\n\t" \
  "v_pk_mul_f32 v[108:109], v[108:109], v[232:233] op_sel_hi:[1,0]\n\t" \
  "v_pk_mul_f32 v[110:111], v[110:111], v[232:233] op_sel_hi:[1,0]\n\t" \
  "v_pk_mul_f32 v[112:113], v[112:113], v[232:233] op_sel_hi:[1,0]\n\t" \
  "v_pk_mul_f32 v[114:115], v[114:115], v[232:233] op_sel_hi:[1,0]\n\t" \
  "v_pk_mul_f32 v[116:117], v[116:117], v[232:233] op_sel_hi:[1,0]\n\t" \
  "v_pk_mul_f32 v[118:119], v[118:119], v[232:233] op_sel_hi:[1,0]\n\t" \
  "v_cvt_pk_bf16_f32 v168, v104, v105\n\t" \
  "v_cvt_pk_bf16_f32 v169, v106, v107\n\t" \
  "v_cvt_pk_bf16_f32 v170, v108, v109\n\t" \
  "v_cvt_pk_bf16_f32 v171, v110, v111\n\t" \
  "v_cvt_pk_bf16_f32 v172, v112, v113\n\t" \
  "v_cvt_pk_bf16_f32 v173, v114, v115\n\t" \
  "v_cvt_pk_bf16_f32 v174, v116, v117\n\t" \
  "v_cvt_pk_bf16_f32 v175, v118, v119\n\t" \
  "buffer_store_dwordx4 v[168:171], v202, s[48:51], 0 offen offset:2048 sc1\n\t" \
  "buffer_store_dwordx4 v[172:175], v202, s[48:51], s85 offen offset:2048 sc1\n\t" \
  "v_accvgpr_read_b32 v40, a80\n\t" \
  "v_accvgpr_read_b32 v41, a81\n\t" \
  "v_accvgpr_read_b32 v42, a82\n\t" \
  "v_accvgpr_read_b32 v43, a83\n\t" \
  "v_accvgpr_read_b32 v44, a84\n\t" \
  "v_accvgpr_read_b32 v45, a85\n\t" \
  "v_accvgpr_read_b32 v46, a86\n\t" \
  "v_accvgpr_read_b32 v47, a87\n\t" \
  "v_accvgpr_read_b32 v48, a88\n\t" \
  "v_accvgpr_read_b32 v49, a89\n\t" \
  "v_accvgpr_read_b32 v50, a90\n\t" \
  "v_accvgpr_read_b32 v51, a91\n\t" \
  "v_accvgpr_read_b32 v52, a92\n\t" \
  "v_accvgpr_read_b32 v53, a93\n\t" \
  "v_accvgpr_read_b32 v54, a94\n\t" \
  "v_accvgpr_read_b32 v55, a95\n\t" \
  "v_pk_mul_f32 v[40:41], v[40:41], v[232:233] op_sel_hi:[1,0]\n\t" \
  "v_pk_mul_f32 v[42:43], v[42:43], v[232:233] op_sel_hi:[1,0]\n\t" \
  "v_pk_mul_f32 v[44:45], v[44:45], v[232:233] op_sel_hi:[1,0]\n\t" \
  "v_pk_mul_f32 v[46:47], v[46:47], v[232:233] op_sel_hi:[1,0]\n\t" \
  "v_pk_mul_f32 v[48:49], v[48:49], v[232:233] op_sel_hi:[1,0]\n\t" \
  "v_pk_mul_f32 v[50:51], v[50:51], v[232:233] op_sel_hi:[1,0]\n\t" \
  "v_pk_mul_f32 v[52:53], v[52:53], v[232:233] op_sel_hi:[1,0]\n\t" \
  "v_pk_mul_f32 v[54:55], v[54:55], v[232:233] op_sel_hi:[1,0]\n\t" \
  "v_cvt_pk_bf16_f32 v176, v40, v41\n\t" \
  "v_cvt_pk_bf16_f32 v177, v42, v43\n\t" \
  "v_cvt_pk_bf16_f32 v178, v44, v45\n\t" \
  "v_cvt_pk_bf16_f32 v179, v46, v47\n\t" \
  "v_cvt_pk_bf16_f32 v180, v48, v49\n\t" \
  "v_cvt_pk_bf16_f32 v181, v50, v51\n\t" \
  "v_cvt_pk_bf16_f32 v182, v52, v53\n\t" \
  "v_cvt_pk_bf16_f32 v183, v54, v55\n\t" \
  "buffer_store_dwordx4 v[176:179], v202, s[48:51], s86 offen offset:2048 sc1\n\t" \
  "buffer_store_dwordx4 v[180:183], v202, s[48:51], s87 offen offset:2048 sc1\n\t" \
  "v_accvgpr_read_b32 v56, a96\n\t" \
  "v_accvgpr_read_b32 v57, a97\n\t" \
  "v_accvgpr_read_b32 v58, a98\n\t" \
  "v_accvgpr_read_b32 v59, a99\n\t" \
  "v_accvgpr_read_b32 v60, a100\n\t" \
  "v_accvgpr_read_b32 v61, a101\n\t" \
  "v_accvgpr_read_b32 v62, a102\n\t" \
  "v_accvgpr_read_b32 v63, a103\n\t" \
  "v_accvgpr_read_b32 v64, a104\n\t" \
  "v_accvgpr_read_b32 v65, a105\n\t" \
  "v_accvgpr_read_b32 v66, a106\n\t" \
  "v_accvgpr_read_b32 v67, a107\n\t" \
  "v_accvgpr_read_b32 v68, a108\n\t" \
  "v_accvgpr_read_b32 v69, a109\n\t" \
  "v_accvgpr_read_b32 v70, a110\n\t" \
  "v_accvgpr_read_b32 v71, a111\n\t" \
  "v_pk_mul_f32 v[56:57], v[56:57], v[234:235] op_sel_hi:[1,0]\n\t" \
  "v_pk_mul_f32 v[58:59], v[58:59], v[234:235] op_sel_hi:[1,0]\n\t" \
  "v_pk_mul_f32 v[60:61], v[60:61], v[234:235] op_sel_hi:[1,0]\n\t" \
  "v_pk_mul_f32 v[62:63], v[62:63], v[234:235] op_sel_hi:[1,0]\n\t" \
  "v_pk_mul_f32 v[64:65], v[64:65], v[234:235] op_sel_hi:[1,0]\n\t" \
  "v_pk_mul_f32 v[66:67], v[66:67], v[234:235] op_sel_hi:[1,0]\n\t" \
  "v_pk_mul_f32 v[68:69], v[68:69], v[234:235] op_sel_hi:[1,0]\n\t" \
  "v_pk_mul_f32 v[70:71], v[70:71], v[234:235] op_sel_hi:[1,0]\n\t" \
  "v_cvt_pk_bf16_f32 v184, v56, v57\n\t" \
  "v_cvt_pk_bf16_f32 v185, v58, v59\n\t" \
  "v_cvt_pk_bf16_f32 v186, v60, v61\n\t" \
  "v_cvt_pk_bf16_f32 v187, v62, v63\n\t" \
  "v_cvt_pk_bf16_f32 v188, v64, v65\n\t" \
  "v_cvt_pk_bf16_f32 v189, v66, v67\n\t" \
  "v_cvt_pk_bf16_f32 v190, v68, v69\n\t" \
  "v_cvt_pk_bf16_f32 v191, v70, v71\n\t" \
  "buffer_store_dwordx4 v[184:187], v202, s[48:51], 0 offen offset:3072 sc1\n\t" \
  "buffer_store_dwordx4 v[188:191], v202, s[48:51], s85 offen offset:3072 sc1\n\t" \
  "v_accvgpr_read_b32 v72, a112\n\t" \
  "v_accvgpr_read_b32 v73, a113\n\t" \
  "v_accvgpr_read_b32 v74, a114\n\t" \
  "v_accvgpr_read_b32 v75, a115\n\t" \
  "v_accvgpr_read_b32 v76, a116\n\t" \
  "v_accvgpr_read_b32 v77, a117\n\t" \
  "v_accvgpr_read_b32 v78, a118\n\t" \
  "v_accvgpr_read_b32 v79, a119\n\t" \
  "v_accvgpr_read_b32 v80, a120\n\t" \
  "v_accvgpr_read_b32 v81, a121\n\t" \
  "v_accvgpr_read_b32 v82, a122\n\t" \
  "v_accvgpr_read_b32 v83, a123\n\t" \
  "v_accvgpr_read_b32 v84, a124\n\t" \
  "v_accvgpr_read_b32 v85, a125\n\t" \
  "v_accvgpr_read_b32 v86, a126\n\t" \
  "v_accvgpr_read_b32 v87, a127\n\t" \
  "v_pk_mul_f32 v[72:73], v[72:73], v[234:235] op_sel_hi:[1,0]\n\t" \
  "v_pk_mul_f32 v[74:75], v[74:75], v[234:235] op_sel_hi:[1,0]\n\t" \
  "v_pk_mul_f32 v[76:77], v[76:77], v[234:235] op_sel_hi:[1,0]\n\t" \
  "v_pk_mul_f32 v[78:79], v[78:79], v[234:235] op_sel_hi:[1,0]\n\t" \
  "v_pk_mul_f32 v[80:81], v[80:81], v[234:235] op_sel_hi:[1,0]\n\t" \
  "v_pk_mul_f32 v[82:83], v[82:83], v[234:235] op_sel_hi:[1,0]\n\t" \
  "v_pk_mul_f32 v[84:85], v[84:85], v[234:235] op_sel_hi:[1,0]\n\t" \
  "v_pk_mul_f32 v[86:87], v[86:87], v[234:235] op_sel_hi:[1,0]\n\t" \
  "v_cvt_pk_bf16_f32 v192, v72, v73\n\t" \
  "v_cvt_pk_bf16_f32 v193, v74, v75\n\t" \
  "v_cvt_pk_bf16_f32 v194, v76, v77\n\t" \
  "v_cvt_pk_bf16_f32 v195, v78, v79\n\t" \
  "v_cvt_pk_bf16_f32 v196, v80, v81\n\t" \
  "v_cvt_pk_bf16_f32 v197, v82, v83\n\t" \
  "v_cvt_pk_bf16_f32 v198, v84, v85\n\t" \
  "v_cvt_pk_bf16_f32 v199, v86, v87\n\t" \
  "buffer_store_dwordx4 v[192:195], v202, s[48:51], s86 offen offset:3072 sc1\n\t" \
  "buffer_store_dwordx4 v[196:199], v202, s[48:51], s87 offen offset:3072 sc1\n\t" \
  ".Lbjoin%=:\n\t" \
  "ds_read_b128 a[160:163], v254 offset:0\n\t" \
  "ds_read_b128 a[164:167], v254 offset:1024\n\t" \
  "ds_read_b128 a[168:171], v254 offset:2048\n\t" \
  "ds_read_b128 a[172:175], v254 offset:3072\n\t" \
  "ds_read_b128 a[176:179], v254 offset:16384\n\t" \
  "ds_read_b128 a[180:183], v254 offset:17408\n\t" \
  "ds_read_b128 a[184:187], v254 offset:18432\n\t" \
  "ds_read_b128 a[188:191], v254 offset:19456\n\t" \
  "ds_read_b128 a[128:131], v254 offset:32768\n\t" \
  "ds_read_b128 a[132:135], v254 offset:33792\n\t" \
  "ds_read_b128 a[136:139], v254 offset:34816\n\t" \
  "ds_read_b128 a[140:143], v254 offset:35840\n\t" \
  "ds_read_b128 a[144:147], v254 offset:49152\n\t" \
  "ds_read_b128 a[148:151], v254 offset:50176\n\t" \
  "ds_read_b128 a[152:155], v254 offset:51200\n\t" \
  "ds_read_b128 a[156:159], v254 offset:52224\n\t" \
  "s_add_u32 s84, %[w], 0\n\t" \
  "s_and_b32 s84, s84, 3\n\t" \
  "s_lshl_b32 s85, s84, 10\n\t" \
  "v_add_u32 v204, s85, v202\n\t" \
  "s_mul_i32 s84, s84, 15360\n\t" \
  "s_add_u32 s84, s84, 65536\n\t" \
  "v_add_u32 v211, s84, v202\n\t" \
  "s_add_u32 s84, %[w], 1\n\t" \
  "s_and_b32 s84, s84, 3\n\t" \
  "s_lshl_b32 s85, s84, 10\n\t" \
  "v_add_u32 v205, s85, v202\n\t" \
  "s_mul_i32 s84, s84, 15360\n\t" \
  "s_add_u32 s84, s84, 65536\n\t" \
  "v_add_u32 v208, s84, v202\n\t" \
  "s_add_u32 s84, %[w], 2\n\t" \
  "s_and_b32 s84, s84, 3\n\t" \
  "s_lshl_b32 s85, s84, 10\n\t" \
  "v_add_u32 v206, s85, v202\n\t" \
  "s_mul_i32 s84, s84, 15360\n\t" \
  "s_add_u32 s84, s84, 70656\n\t" \
  "v_add_u32 v209, s84, v202\n\t" \
  "s_add_u32 s84, %[w], 3\n\t" \
  "s_and_b32 s84, s84, 3\n\t" \
  "s_lshl_b32 s85, s84, 10\n\t" \
  "v_add_u32 v207, s85, v202\n\t" \
  "s_mul_i32 s84, s84, 15360\n\t" \
  "s_add_u32 s84, s84, 75776\n\t" \
  "v_add_u32 v210, s84, v202\n\t" \
  "v_and_b32 v221, 15, v203\n\t" \
  "v_mul_u32_u24 v221, 160, v221\n\t" \
  "v_lshrrev_b32 v222, 4, v203\n\t" \
  "v_lshl_add_u32 v221, v222, 3, v221\n\t" \
  "s_mul_i32 s84, %[w], 2560\n\t" \
  "s_add_u32 s84, s84, 152576\n\t" \
  "v_add_u32 v221, s84, v221\n\t" \
  "v_lshlrev_b32 v222, 4, v222\n\t" \
  "v_add_u32 v222, 151552, v222\n\t" \
  "s_and_b32 s86, %[mt], 1\n\t" \
  "s_sub_u32 s87, %[mt], s86\n\t" \
  "s_mul_i32 s44, s87, 524288\n\t" \
  "s_mul_i32 s72, s86, 262144\n\t" \
  "s_add_u32 s44, s44, s72\n\t" \
  "s_mul_i32 s72, %[w], 65536\n\t" \
  "s_add_u32 s44, s44, s72\n\t" \
  "s_add_u32 s44, s44, 5554176\n\t" \
  "s_mov_b64 s[72:73], %[ws]\n\t" \
  "s_add_u32 s44, s44, s72\n\t" \
  "s_addc_u32 s45, s73, 0\n\t" \
  "s_and_b32 s45, s45, 0xffff\n\t" \
  "s_mov_b32 s46, 0x90000\n\t" \
  "s_mov_b32 s47, 0x20000\n\t" \
  "s_lshl_b32 s83, s86, 19\n\t" \
  "s_xor_b32 s79, s83, 0x80000\n\t" \
  "s_lshr_b32 s86, %[head], 3\n\t" \
  "s_and_b32 s87, %[head], 7\n\t" \
  "s_mul_i32 s68, %[qblk], 4096\n\t" \
  "s_mul_i32 s72, s86, 1024\n\t" \
  "s_add_u32 s68, s68, s72\n\t" \
  "s_mul_i32 s72, s87, 128\n\t" \
  "s_add_u32 s68, s68, s72\n\t" \
  "s_add_u32 s68, s68, 42205184\n\t" \
  "s_mov_b64 s[72:73], %[ws]\n\t" \
  "s_add_u32 s68, s68, s72\n\t" \
  "s_addc_u32 s69, s73, 0\n\t" \
  "s_waitcnt vmcnt(0) lgkmcnt(0)\n\t" \
  "s_barrier\n\t" \
  "s_cmp_lg_u32 %[w], 0\n\t" \
  "s_cbranch_scc1 .Lfo%=\n\t" \
  "s_mov_b64 exec, 0xffffffff\n\t" \
  "v_lshlrev_b32 v200, 2, v203\n\t" \
  "v_mov_b32 v201, %[tag]\n\t" \
  "global_store_dword v200, v201, s[68:69] sc1\n\t" \
  "s_mov_b64 exec, -1\n\t" \
  ".Lfo%=:\n\t" \
  "s_mul_i32 s40, %[qblk], 4096\n\t" \
  "s_mul_i32 s72, s86, 1024\n\t" \
  "s_add_u32 s40, s40, s72\n\t" \
  "s_mul_i32 s72, s87, 128\n\t" \
  "s_add_u32 s40, s40, s72\n\t" \
  "s_add_u32 s40, s40, 42369024\n\t" \
  "s_mov_b64 s[72:73], %[ws]\n\t" \
  "s_add_u32 s40, s40, s72\n\t" \
  "s_addc_u32 s41, s73, 0\n\t" \
  "s_xor_b32 s50, s50, 0x4000\n\t" \
  "s_mov_b32 s85, 0x1000\n\t" \
  "s_mov_b32 s86, 0x2000\n\t" \
  "s_mov_b32 s87, 0x3000\n\t" \
  "buffer_store_dwordx4 v[136:139], v202, s[48:51], 0 offen offset:0 sc1\n\t" \
  "buffer_store_dwordx4 v[140:143], v202, s[48:51], s85 offen offset:0 sc1\n\t" \
  "buffer_store_dwordx4 v[144:147], v202, s[48:51], s86 offen offset:0 sc1\n\t" \
  "buffer_store_dwordx4 v[148:151], v202, s[48:51], s87 offen offset:0 sc1\n\t" \
  "buffer_store_dwordx4 v[152:155], v202, s[48:51], 0 offen offset:1024 sc1\n\t" \
  "buffer_store_dwordx4 v[156:159], v202, s[48:51], s85 offen offset:1024 sc1\n\t" \
  "buffer_store_dwordx4 v[160:163], v202, s[48:51], s86 offen offset:1024 sc1\n\t" \
  "buffer_store_dwordx4 v[164:167], v202, s[48:51], s87 offen offset:1024 sc1\n\t" \
  "buffer_store_dwordx4 v[168:171], v202, s[48:51], 0 offen offset:2048 sc1\n\t" \
  "buffer_store_dwordx4 v[172:175], v202, s[48:51], s85 offen offset:2048 sc1\n\t" \
  "buffer_store_dwordx4 v[176:179], v202, s[48:51], s86 offen offset:2048 sc1\n\t" \
  "buffer_store_dwordx4 v[180:183], v202, s[48:51], s87 offen offset:2048 sc1\n\t" \
  "buffer_store_dwordx4 v[184:187], v202, s[48:51], 0 offen offset:3072 sc1\n\t" \
  "buffer_store_dwordx4 v[188:191], v202, s[48:51], s85 offen offset:3072 sc1\n\t" \
  "buffer_store_dwordx4 v[192:195], v202, s[48:51], s86 offen offset:3072 sc1\n\t" \
  "buffer_store_dwordx4 v[196:199], v202, s[48:51], s87 offen offset:3072 sc1\n\t" \
  "s_mov_b32 s64, 114688\n\t" \
  "s_mov_b64 s[72:73], %[ws]\n\t" \
  "s_add_u32 s64, s64, s72\n\t" \
  "s_addc_u32 s65, s73, 0\n\t" \
  "s_and_b32 s65, s65, 0xffff\n\t" \
  "s_mov_b32 s66, 0xa000\n\t" \
  "s_mov_b32 s67, 0x20000\n\t" \
  "s_mov_b32 s74, 10240\n\t" \
  "s_mov_b32 s75, 20480\n\t" \
  "s_mov_b32 s89, 30720\n\t" \
  "v_min_u32 v252, 59, v203\n\t" \
  "v_lshlrev_b32 v250, 4, v252\n\t" \
  "v_cmp_lt_u32 vcc, 19, v252\n\t" \
  "v_mov_b32 v253, 4800\n\t" \
  "s_nop 0\n\t" \
  "v_cndmask_b32 v253, 0, v253, vcc\n\t" \
  "v_cndmask_b32 v248, 0, 1.0, vcc\n\t" \
  "v_add_u32 v251, v253, v250\n\t" \
  "v_cmp_lt_u32 vcc, 39, v252\n\t" \
  "v_mov_b32 v253, 2240\n\t" \
  "s_nop 0\n\t" \
  "v_cndmask_b32 v253, 0, v253, vcc\n\t" \
  "v_cndmask_b32 v248, v248, 0, vcc\n\t" \
  "v_add_u32 v251, v253, v251\n\t" \
  "s_mul_i32 s84, %[nt], 320\n\t" \
  "v_add_u32 v251, s84, v251\n\t" \
  "buffer_load_dwordx4 v[40:43], v251, s[64:67], 0 offen sc1\n\t" \
  "buffer_load_dwordx4 v[44:47], v251, s[64:67], s74 offen sc1\n\t" \
  "buffer_load_dwordx4 v[48:51], v251, s[64:67], s75 offen sc1\n\t" \
  "buffer_load_dwordx4 v[52:55], v251, s[64:67], s89 offen sc1\n\t" \
  "v_add_u32 v250, 151552, v250\n\t" \
  "s_mov_b32 s88, 32768\n\t" \
  "buffer_load_dwordx4 v[88:91], v202, s[36:39], s88 offen offset:0 sc1\n\t" \
  "buffer_load_dwordx4 v[92:95], v202, s[36:39], s88 offen offset:1024 sc1\n\t" \
  "buffer_load_dwordx4 v[96:99], v202, s[36:39], s88 offen offset:2048 sc1\n\t" \
  "buffer_load_dwordx4 v[100:103], v202, s[36:39], s88 offen offset:3072 sc1\n\t" \
  "s_mov_b32 s88, 36864\n\t" \
  "buffer_load_dwordx4 v[104:107], v202, s[36:39], s88 offen offset:0 sc1\n\t" \
  "buffer_load_dwordx4 v[108:111], v202, s[36:39], s88 offen offset:1024 sc1\n\t" \
  "buffer_load_dwordx4 v[112:115], v202, s[36:39], s88 offen offset:2048 sc1\n\t" \
  "buffer_load_dwordx4 v[116:119], v202, s[36:39], s88 offen offset:3072 sc1\n\t" \
  "s_waitcnt vmcnt(10)\n\t" \
  "v_pk_add_f32 v[40:41], v[40:41], v[44:45]\n\t" \
  "s_waitcnt vmcnt(8)\n\t" \
  "v_pk_add_f32 v[48:49], v[48:49], v[52:53]\n\t" \
  "v_pk_add_f32 v[42:43], v[42:43], v[46:47]\n\t" \
  "v_pk_add_f32 v[50:51], v[50:51], v[54:55]\n\t" \
  "v_pk_add_f32 v[40:41], v[40:41], v[48:49]\n\t" \
  "v_pk_add_f32 v[42:43], v[42:43], v[50:51]\n\t" \
  "v_cvt_pk_bf16_f32 v44, v40, v41\n\t" \
  "v_cvt_pk_bf16_f32 v45, v42, v43\n\t" \
  "v_lshlrev_b32 v40, 16, v44\n\t" \
  "v_and_b32 v41, 0xffff0000, v44\n\t" \
  "v_lshlrev_b32 v42, 16, v45\n\t" \
  "v_and_b32 v43, 0xffff0000, v45\n\t" \
  "v_add_f32 v40, v248, v40\n\t" \
  "v_add_f32 v41, v248, v41\n\t" \
  "v_add_f32 v42, v248, v42\n\t" \
  "v_add_f32 v43, v248, v43\n\t" \
  "v_cvt_pk_bf16_f32 v44, v40, v41\n\t" \
  "v_cvt_pk_bf16_f32 v45, v42, v43\n\t" \
  "v_lshlrev_b32 v40, 16, v44\n\t" \
  "v_and_b32 v41, 0xffff0000, v44\n\t" \
  "v_lshlrev_b32 v42, 16, v45\n\t" \
  "v_and_b32 v43, 0xffff0000, v45\n\t" \
  "ds_write_b128 v250, v[40:43]\n\t" \
  "s_sub_u32 s86, %[ft], 27\n\t" \
  "s_min_u32 s86, s86, %[ft]\n\t" \
  "s_min_u32 s86, s86, 26\n\t" \
  "s_mul_i32 s87, s86, 128\n\t" \
  "s_sub_u32 s87, 2215936, s87\n\t" \
  "s_cmp_lt_u32 %[ft], 54\n\t" \
  "s_cselect_b32 s87, s87, 0\n\t" \
  "s_mul_i32 s56, s86, 128\n\t" \
  "s_mov_b64 s[72:73], %[wg]\n\t" \
  "s_add_u32 s56, s56, s72\n\t" \
  "s_addc_u32 s57, s73, 0\n\t" \
  "s_sub_u32 s56, s56, 4096\n\t" \
  "s_subb_u32 s57, s57, 0\n\t" \
  "s_and_b32 s57, s57, 0xffff\n\t" \
  "s_mov_b32 s58, s87\n\t" \
  "s_mov_b32 s59, 0x20000\n\t" \
  "s_mul_i32 s60, s86, 128\n\t" \
  "s_mov_b64 s[72:73], %[wu]\n\t" \
  "s_add_u32 s60, s60, s72\n\t" \
  "s_addc_u32 s61, s73, 0\n\t" \
  "s_sub_u32 s60, s60, 4096\n\t" \
  "s_subb_u32 s61, s61, 0\n\t" \
  "s_and_b32 s61, s61, 0xffff\n\t" \
  "s_mov_b32 s62, s87\n\t" \
  "s_mov_b32 s63, 0x20000\n\t" \
  "v_bfe_u32 v200, v203, 2, 3\n\t" \
  "s_mul_i32 s84, %[w], 16\n\t" \
  "v_add_u32 v200, s84, v200\n\t" \
  "v_mul_u32_u24 v220, 3456, v200\n\t" \
  "v_lshrrev_b32 v200, 3, v203\n\t" \
  "v_and_b32 v200, 2, v200\n\t" \
  "v_and_b32 v201, 3, v203\n\t" \
  "v_xor_b32 v200, v200, v201\n\t" \
  "v_lshl_add_u32 v220, v200, 4, v220\n\t" \
  "v_lshrrev_b32 v201, 5, v203\n\t" \
  "v_lshl_add_u32 v220, v201, 6, v220\n\t" \
  "v_add_u32 v216, 4096, v220\n\t" \
  "v_add_u32 v217, 3072, v220\n\t" \
  "v_add_u32 v218, 29696, v220\n\t" \
  "v_add_u32 v219, 28672, v220\n\t" \
  "s_mul_i32 s84, %[w], 96\n\t" \
  "v_add_u32 v220, s84, v203\n\t" \
  "v_mul_u32_u24 v220, 3456, v220\n\t" \
  "s_bitcmp1_b32 %[pc], 0\n\t" \
  "s_cselect_b64 s[52:53], s[60:61], s[56:57]\n\t" \
  "s_mov_b64 s[54:55], s[58:59]\n\t" \
  "s_lshl_b32 s80, %[w], 12\n\t" \
  "s_xor_b32 s86, %[mt], 1\n\t" \
  "s_mul_i32 s48, s86, 163840\n\t" \
  "s_mul_i32 s72, %[nt], 20480\n\t" \
  "s_add_u32 s48, s48, s72\n\t" \
  "s_mul_i32 s72, %[w], 5120\n\t" \
  "s_add_u32 s48, s48, s72\n\t" \
  "s_add_u32 s48, s48, 36864000\n\t" \
  "s_mov_b64 s[72:73], %[ws]\n\t" \
  "s_add_u32 s48, s48, s72\n\t" \
  "s_addc_u32 s49, s73, 0\n\t" \
  "s_and_b32 s49, s49, 0xffff\n\t" \
  "s_mov_b32 s50, 0x1400\n\t" \
  "s_mov_b32 s51, 0x20000\n\t" \
  "s_mov_b32 s77, 0x1000\n\t" \
  "s_lshr_b32 s86, %[mt], 2\n\t" \
  "s_and_b32 s87, %[mt], 1\n\t" \
  "s_lshr_b32 s85, %[w], 1\n\t" \
  "s_and_b32 s82, %[w], 1\n\t" \
  "s_mul_i32 s68, s86, 4096\n\t" \
  "s_mul_i32 s72, s87, 2048\n\t" \
  "s_add_u32 s68, s68, s72\n\t" \
  "s_mul_i32 s72, s85, 1024\n\t" \
  "s_add_u32 s68, s68, s72\n\t" \
  "s_mul_i32 s72, s82, 512\n\t" \
  "s_add_u32 s68, s68, s72\n\t" \
  "s_add_u32 s68, s68, 42205184\n\t" \
  "s_mov_b64 s[72:73], %[ws]\n\t" \
  "s_add_u32 s68, s68, s72\n\t" \
  "s_addc_u32 s69, s73, 0\n\t" \
  "v_and_b32 v200, 7, v203\n\t" \
  "v_min_u32 v200, 3, v200\n\t" \
  "v_mul_u32_u24 v200, 128, v200\n\t" \
  "s_mov_b32 s84, 0\n\t" \
  ".Lpo%=:\n\t" \
  "global_load_dword v201, v200, s[68:69] sc1\n\t" \
  "s_waitcnt vmcnt(0)\n\t" \
  "v_cmp_ne_u32 vcc, %[tag], v201\n\t" \
  "s_cbranch_vccz .Lpod%=\n\t" \
  "s_sleep 1\n\t" \
  "s_add_u32 s84, s84, 1\n\t" \
  "s_cmp_lt_u32 s84, 30000\n\t" \
  "s_cbranch_scc1 .Lpo%=\n\t" \
  "v_mov_b32 v201, 0\n\t" \
  "v_mov_b32 v202, 1\n\t" \
  "global_store_dword v201, v202, s[70:71] sc1\n\t" \
  "s_waitcnt vmcnt(0)\n\t" \
  ".Lpod%=:\n\t" \
  "v_mov_b32 v212, v200\n\t" \
  "s_add_u32 s42, s68, 163840\n\t" \
  "s_addc_u32 s43, s69, 0\n\t" \
  "s_mul_i32 s68, %[mt], 1024\n\t" \
  "s_mul_i32 s72, %[w], 256\n\t" \
  "s_add_u32 s68, s68, s72\n\t" \
  "s_add_u32 s68, s68, 42106880\n\t" \
  "s_mov_b64 s[72:73], %[ws]\n\t" \
  "s_add_u32 s68, s68, s72\n\t" \
  "s_addc_u32 s69, s73, 0\n\t" \
  "v_and_b32 v200, 7, v203\n\t" \
  "v_and_b32 v201, 8, v203\n\t" \
  "v_lshlrev_b32 v200, 2, v200\n\t" \
  "v_lshl_or_b32 v200, v201, 4, v200\n\t" \
  "global_load_dword v215, v200, s[68:69] sc1\n\t" \
  "s_add_u32 s76, s79, 0\n\t" \
  "buffer_load_dwordx4 v[152:155], v204, s[44:47], s76 offen sc1\n\t" \
  "buffer_load_dwordx4 v[156:159], v205, s[44:47], s76 offen sc1\n\t" \
  "buffer_load_dwordx4 v[160:163], v206, s[44:47], s76 offen sc1\n\t" \
  "buffer_load_dwordx4 v[164:167], v207, s[44:47], s76 offen sc1\n\t" \
  "s_add_u32 s76, s79, 4096\n\t" \
  "buffer_load_dwordx4 v[168:171], v204, s[44:47], s76 offen sc1\n\t" \
  "buffer_load_dwordx4 v[172:175], v205, s[44:47], s76 offen sc1\n\t" \
  "buffer_load_dwordx4 v[176:179], v206, s[44:47], s76 offen sc1\n\t" \
  "buffer_load_dwordx4 v[180:183], v207, s[44:47], s76 offen sc1\n\t" \
  "s_add_u32 s76, s79, 8192\n\t" \
  "buffer_load_dwordx4 v[184:187], v204, s[44:47], s76 offen sc1\n\t" \
  "buffer_load_dwordx4 v[188:191], v205, s[44:47], s76 offen sc1\n\t" \
  "buffer_load_dwordx4 v[192:195], v206, s[44:47], s76 offen sc1\n\t" \
  "buffer_load_dwordx4 v[196:199], v207, s[44:47], s76 offen sc1\n\t" \
  "s_add_u32 s76, s79, 12288\n\t" \
  "buffer_load_dwordx4 v[224:227], v204, s[44:47], s76 offen sc1\n\t" \
  "buffer_load_dwordx4 v[228:231], v205, s[44:47], s76 offen sc1\n\t" \
  "buffer_load_dwordx4 v[232:235], v206, s[44:47], s76 offen sc1\n\t" \
  "buffer_load_dwordx4 v[236:239], v207, s[44:47], s76 offen sc1\n\t" \
  "s_add_u32 s76, s79, 16384\n\t" \
  "buffer_load_dwordx4 v[240:243], v204, s[44:47], s76 offen sc1\n\t" \
  "buffer_load_dwordx4 v[244:247], v205, s[44:47], s76 offen sc1\n\t" \
  "buffer_load_dwordx4 v[248:251], v206, s[44:47], s76 offen sc1\n\t" \
  "buffer_load_dwordx4 v[252:255], v207, s[44:47], s76 offen sc1\n\t" \
  "s_mov_b32 s88, 40960\n\t" \
  "buffer_load_dwordx4 v[120:123], v202, s[36:39], s88 offen offset:0 sc1\n\t" \
  "buffer_load_dwordx4 v[124:127], v202, s[36:39], s88 offen offset:1024 sc1\n\t" \
  "buffer_load_dwordx4 v[128:131], v202, s[36:39], s88 offen offset:2048 sc1\n\t" \
  "buffer_load_dwordx4 v[132:135], v202, s[36:39], s88 offen offset:3072 sc1\n\t" \
  "s_waitcnt vmcnt(23)\n\t" \
  "v_mfma_f32_16x16x32_bf16 v[8:11], a[192:195], v[152:155], 0\n\t" \
  "s_mov_b32 s88, 45056\n\t" \
  "buffer_load_dwordx4 v[136:139], v202, s[36:39], s88 offen offset:0 sc1\n\t" \
  "s_waitcnt vmcnt(23)\n\t" \
  "v_mfma_f32_16x16x32_bf16 v[28:31], a[192:195], v[156:159], 0\n\t" \
  "s_waitcnt vmcnt(22)\n\t" \
  "v_mfma_f32_16x16x32_bf16 v[48:51], a[192:195], v[160:163], 0\n\t" \
  "buffer_load_dwordx4 v[140:143], v202, s[36:39], s88 offen offset:1024 sc1\n\t" \
  "s_waitcnt vmcnt(22)\n\t" \
  "v_mfma_f32_16x16x32_bf16 v[68:71], a[192:195], v[164:167], 0\n\t" \
  "v_mfma_f32_16x16x32_bf16 v[12:15], a[196:199], v[152:155], 0\n\t" \
  "buffer_load_dwordx4 v[144:147], v202, s[36:39], s88 offen offset:2048 sc1\n\t" \
  "v_mfma_f32_16x16x32_bf16 v[32:35], a[196:199], v[156:159], 0\n\t" \
  "v_mfma_f32_16x16x32_bf16 v[52:55], a[196:199], v[160:163], 0\n\t" \
  "buffer_load_dwordx4 v[148:151], v202, s[36:39], s88 offen offset:3072 sc1\n\t" \
  "v_mfma_f32_16x16x32_bf16 v[72:75], a[196:199], v[164:167], 0\n\t" \
  "s_mov_b32 s88, 49152\n\t" \
  "v_mfma_f32_16x16x32_bf16 v[16:19], a[200:203], v[152:155], 0\n\t" \
  "buffer_load_dwordx4 a[0:3], v202, s[36:39], s88 offen offset:0 sc1\n\t" \
  "v_mfma_f32_16x16x32_bf16 v[36:39], a[200:203], v[156:159], 0\n\t" \
  "v_mfma_f32_16x16x32_bf16 v[56:59], a[200:203], v[160:163], 0\n\t" \
  "buffer_load_dwordx4 a[4:7], v202, s[36:39], s88 offen offset:1024 sc1\n\t" \
  "v_mfma_f32_16x16x32_bf16 v[76:79], a[200:203], v[164:167], 0\n\t" \
  "v_mfma_f32_16x16x32_bf16 v[20:23], a[204:207], v[152:155], 0\n\t" \
  "buffer_load_dwordx4 a[8:11], v202, s[36:39], s88 offen offset:2048 sc1\n\t" \
  "v_mfma_f32_16x16x32_bf16 v[40:43], a[204:207], v[156:159], 0\n\t" \
  "v_mfma_f32_16x16x32_bf16 v[60:63], a[204:207], v[160:163], 0\n\t" \
  "v_mfma_f32_16x16x32_bf16 v[80:83], a[204:207], v[164:167], 0\n\t" \
  "v_mfma_f32_16x16x32_bf16 v[24:27], a[208:211], v[152:155], 0\n\t" \
  "v_mfma_f32_16x16x32_bf16 v[44:47], a[208:211], v[156:159], 0\n\t" \
  "v_mfma_f32_16x16x32_bf16 v[64:67], a[208:211], v[160:163], 0\n\t" \
  "v_mfma_f32_16x16x32_bf16 v[84:87], a[208:211], v[164:167], 0\n\t" \
  "s_barrier\n\t" \
  "s_cmp_lg_u32 %[w], 0\n\t" \
  "s_cbranch_scc1 .Lf2%=\n\t" \
  "s_mov_b64 exec, 0xffffffff\n\t" \
  "v_lshlrev_b32 v200, 2, v203\n\t" \
  "v_mov_b32 v201, %[tag]\n\t" \
  "global_store_dword v200, v201, s[40:41] sc1\n\t" \
  "s_mov_b64 exec, -1\n\t" \
  ".Lf2%=:\n\t" \
  "s_waitcnt vmcnt(26)\n\t" \
  "v_mfma_f32_16x16x32_bf16 v[8:11], a[212:215], v[168:171], v[8:11]\n\t" \
  "s_add_u32 s76, s79, 20480\n\t" \
  "buffer_load_dwordx4 v[152:155], v204, s[44:47], s76 offen sc1\n\t" \
  "s_waitcnt vmcnt(26)\n\t" \
  "v_mfma_f32_16x16x32_bf16 v[28:31], a[212:215], v[172:175], v[28:31]\n\t" \
  "s_waitcnt vmcnt(25)\n\t" \
  "v_mfma_f32_16x16x32_bf16 v[48:51], a[212:215], v[176:179], v[48:51]\n\t" \
  "buffer_load_dwordx4 v[156:159], v205, s[44:47], s76 offen sc1\n\t" \
  "s_waitcnt vmcnt(25)\n\t" \
  "v_mfma_f32_16x16x32_bf16 v[68:71], a[212:215], v[180:183], v[68:71]\n\t" \
  "v_mfma_f32_16x16x32_bf16 v[12:15], a[216:219], v[168:171], v[12:15]\n\t" \
  "buffer_load_dwordx4 v[160:163], v206, s[44:47], s76 offen sc1\n\t" \
  "v_mfma_f32_16x16x32_bf16 v[32:35], a[216:219], v[172:175], v[32:35]\n\t" \
  "v_mfma_f32_16x16x32_bf16 v[52:55], a[216:219], v[176:179], v[52:55]\n\t" \
  "buffer_load_dwordx4 v[164:167], v207, s[44:47], s76 offen sc1\n\t" \
  "v_mfma_f32_16x16x32_bf16 v[72:75], a[216:219], v[180:183], v[72:75]\n\t" \
  "v_mfma_f32_16x16x32_bf16 v[16:19], a[220:223], v[168:171], v[16:19]\n\t" \
  "buffer_load_dwordx4 a[12:15], v202, s[36:39], s88 offen offset:3072 sc1\n\t" \
  "v_mfma_f32_16x16x32_bf16 v[36:39], a[220:223], v[172:175], v[36:39]\n\t" \
  "s_mov_b32 s88, 53248\n\t" \
  "v_mfma_f32_16x16x32_bf16 v[56:59], a[220:223], v[176:179], v[56:59]\n\t" \
  "buffer_load_dwordx4 a[16:19], v202, s[36:39], s88 offen offset:0 sc1\n\t" \
  "v_mfma_f32_16x16x32_bf16 v[76:79], a[220:223], v[180:183], v[76:79]\n\t" \
  "v_mfma_f32_16x16x32_bf16 v[20:23], a[224:227], v[168:171], v[20:23]\n\t" \
  "buffer_load_dwordx4 a[20:23], v202, s[36:39], s88 offen offset:1024 sc1\n\t" \
  "v_mfma_f32_16x16x32_bf16 v[40:43], a[224:227], v[172:175], v[40:43]\n\t" \
  "v_mfma_f32_16x16x32_bf16 v[60:63], a[224:227], v[176:179], v[60:63]\n\t" \
  "v_mfma_f32_16x16x32_bf16 v[80:83], a[224:227], v[180:183], v[80:83]\n\t" \
  "v_mfma_f32_16x16x32_bf16 v[24:27], a[228:231], v[168:171], v[24:27]\n\t" \
  "v_mfma_f32_16x16x32_bf16 v[44:47], a[228:231], v[172:175], v[44:47]\n\t" \
  "v_mfma_f32_16x16x32_bf16 v[64:67], a[228:231], v[176:179], v[64:67]\n\t" \
  "v_mfma_f32_16x16x32_bf16 v[84:87], a[228:231], v[180:183], v[84:87]\n\t" \
  "s_waitcnt vmcnt(29)\n\t" \
  "v_mfma_f32_16x16x32_bf16 v[8:11], a[232:235], v[184:187], v[8:11]\n\t" \
  "s_add_u32 s76, s79, 24576\n\t" \
  "buffer_load_dwordx4 v[168:171], v204, s[44:47], s76 offen sc1\n\t" \
  "s_waitcnt vmcnt(29)\n\t" \
  "v_mfma_f32_16x16x32_bf16 v[28:31], a[232:235], v[188:191], v[28:31]\n\t" \
  "s_waitcnt vmcnt(28)\n\t" \
  "v_mfma_f32_16x16x32_bf16 v[48:51], a[232:235], v[192:195], v[48:51]\n\t" \
  "buffer_load_dwordx4 v[172:175], v205, s[44:47], s76 offen sc1\n\t" \
  "s_waitcnt vmcnt(28)\n\t" \
  "v_mfma_f32_16x16x32_bf16 v[68:71], a[232:235], v[196:199], v[68:71]\n\t" \
  "v_mfma_f32_16x16x32_bf16 v[12:15], a[236:239], v[184:187], v[12:15]\n\t" \
  "buffer_load_dwordx4 v[176:179], v206, s[44:47], s76 offen sc1\n\t" \
  "v_mfma_f32_16x16x32_bf16 v[32:35], a[236:239], v[188:191], v[32:35]\n\t" \
  "v_mfma_f32_16x16x32_bf16 v[52:55], a[236:239], v[192:195], v[52:55]\n\t" \
  "buffer_load_dwordx4 v[180:183], v207, s[44:47], s76 offen sc1\n\t" \
  "v_mfma_f32_16x16x32_bf16 v[72:75], a[236:239], v[196:199], v[72:75]\n\t" \
  "v_mfma_f32_16x16x32_bf16 v[16:19], a[240:243], v[184:187], v[16:19]\n\t" \
  "buffer_load_dwordx4 a[24:27], v202, s[36:39], s88 offen offset:2048 sc1\n\t" \
  "v_mfma_f32_16x16x32_bf16 v[36:39], a[240:243], v[188:191], v[36:39]\n\t" \
  "v_mfma_f32_16x16x32_bf16 v[56:59], a[240:243], v[192:195], v[56:59]\n\t" \
  "buffer_load_dwordx4 a[28:31], v202, s[36:39], s88 offen offset:3072 sc1\n\t" \
  "v_mfma_f32_16x16x32_bf16 v[76:79], a[240:243], v[196:199], v[76:79]\n\t" \
  "s_mov_b32 s88, 57344\n\t" \
  "v_mfma_f32_16x16x32_bf16 v[20:23], a[244:247], v[184:187], v[20:23]\n\t" \
  "buffer_load_dwordx4 a[32:35], v202, s[36:39], s88 offen offset:0 sc1\n\t" \
  "v_mfma_f32_16x16x32_bf16 v[40:43], a[244:247], v[188:191], v[40:43]\n\t" \
  "v_mfma_f32_16x16x32_bf16 v[60:63], a[244:247], v[192:195], v[60:63]\n\t" \
  "v_mfma_f32_16x16x32_bf16 v[80:83], a[244:247], v[196:199], v[80:83]\n\t" \
  "v_mfma_f32_16x16x32_bf16 v[24:27], a[248:251], v[184:187], v[24:27]\n\t" \
  "v_mfma_f32_16x16x32_bf16 v[44:47], a[248:251], v[188:191], v[44:47]\n\t" \
  "v_mfma_f32_16x16x32_bf16 v[64:67], a[248:251], v[192:195], v[64:67]\n\t" \
  "v_mfma_f32_16x16x32_bf16 v[84:87], a[248:251], v[196:199], v[84:87]\n\t" \
  "s_waitcnt vmcnt(32)\n\t" \
  "v_mfma_f32_16x16x32_bf16 v[8:11], a[252:255], v[224:227], v[8:11]\n\t" \
  "s_add_u32 s76, s79, 28672\n\t" \
  "buffer_load_dwordx4 v[184:187], v204, s[44:47], s76 offen sc1\n\t" \
  "s_waitcnt vmcnt(32)\n\t" \
  "v_mfma_f32_16x16x32_bf16 v[28:31], a[252:255], v[228:231], v[28:31]\n\t" \
  "s_waitcnt vmcnt(31)\n\t" \
  "v_mfma_f32_16x16x32_bf16 v[48:51], a[252:255], v[232:235], v[48:51]\n\t" \
  "buffer_load_dwordx4 v[188:191], v205, s[44:47], s76 offen sc1\n\t" \
  "s_waitcnt vmcnt(31)\n\t" \
  "v_mfma_f32_16x16x32_bf16 v[68:71], a[252:255], v[236:239], v[68:71]\n\t" \
  "v_mfma_f32_16x16x32_bf16 v[12:15], a[160:163], v[224:227], v[12:15]\n\t" \
  "buffer_load_dwordx4 v[192:195], v206, s[44:47], s76 offen sc1\n\t" \
  "v_mfma_f32_16x16x32_bf16 v[32:35], a[160:163], v[228:231], v[32:35]\n\t" \
  "v_mfma_f32_16x16x32_bf16 v[52:55], a[160:163], v[232:235], v[52:55]\n\t" \
  "buffer_load_dwordx4 v[196:199], v207, s[44:47], s76 offen sc1\n\t" \
  "v_mfma_f32_16x16x32_bf16 v[72:75], a[160:163], v[236:239], v[72:75]\n\t" \
  "v_mfma_f32_16x16x32_bf16 v[16:19], a[164:167], v[224:227], v[16:19]\n\t" \
  "buffer_load_dwordx4 a[36:39], v202, s[36:39], s88 offen offset:1024 sc1\n\t" \
  "v_mfma_f32_16x16x32_bf16 v[36:39], a[164:167], v[228:231], v[36:39]\n\t" \
  "v_mfma_f32_16x16x32_bf16 v[56:59], a[164:167], v[232:235], v[56:59]\n\t" \
  "buffer_load_dwordx4 a[40:43], v202, s[36:39], s88 offen offset:2048 sc1\n\t" \
  "v_mfma_f32_16x16x32_bf16 v[76:79], a[164:167], v[236:239], v[76:79]\n\t" \
  "v_mfma_f32_16x16x32_bf16 v[20:23], a[168:171], v[224:227], v[20:23]\n\t" \
  "buffer_load_dwordx4 a[44:47], v202, s[36:39], s88 offen offset:3072 sc1\n\t" \
  "v_mfma_f32_16x16x32_bf16 v[40:43], a[168:171], v[228:231], v[40:43]\n\t" \
  "v_mfma_f32_16x16x32_bf16 v[60:63], a[168:171], v[232:235], v[60:63]\n\t" \
  "v_mfma_f32_16x16x32_bf16 v[80:83], a[168:171], v[236:239], v[80:83]\n\t" \
  "v_mfma_f32_16x16x32_bf16 v[24:27], a[172:175], v[224:227], v[24:27]\n\t" \
  "v_mfma_f32_16x16x32_bf16 v[44:47], a[172:175], v[228:231], v[44:47]\n\t" \
  "v_mfma_f32_16x16x32_bf16 v[64:67], a[172:175], v[232:235], v[64:67]\n\t" \
  "v_mfma_f32_16x16x32_bf16 v[84:87], a[172:175], v[236:239], v[84:87]\n\t" \
  "s_waitcnt vmcnt(35)\n\t" \
  "v_mfma_f32_16x16x32_bf16 v[8:11], a[176:179], v[240:243], v[8:11]\n\t" \
  "s_add_u32 s76, s79, 32768\n\t" \
  "buffer_load_dwordx4 v[224:227], v204, s[44:47], s76 offen sc1\n\t" \
  "s_waitcnt vmcnt(35)\n\t" \
  "v_mfma_f32_16x16x32_bf16 v[28:31], a[176:179], v[244:247], v[28:31]\n\t" \
  "s_waitcnt vmcnt(34)\n\t" \
  "v_mfma_f32_16x16x32_bf16 v[48:51], a[176:179], v[248:251], v[48:51]\n\t" \
  "buffer_load_dwordx4 v[228:231], v205, s[44:47], s76 offen sc1\n\t" \
  "s_waitcnt vmcnt(34)\n\t" \
  "v_mfma_f32_16x16x32_bf16 v[68:71], a[176:179], v[252:255], v[68:71]\n\t" \
  "v_mfma_f32_16x16x32_bf16 v[12:15], a[180:183], v[240:243], v[12:15]\n\t" \
  "buffer_load_dwordx4 v[232:235], v206, s[44:47], s76 offen sc1\n\t" \
  "v_mfma_f32_16x16x32_bf16 v[32:35], a[180:183], v[244:247], v[32:35]\n\t" \
  "v_mfma_f32_16x16x32_bf16 v[52:55], a[180:183], v[248:251], v[52:55]\n\t" \
  "buffer_load_dwordx4 v[236:239], v207, s[44:47], s76 offen sc1\n\t" \
  "v_mfma_f32_16x16x32_bf16 v[72:75], a[180:183], v[252:255], v[72:75]\n\t" \
  "s_mov_b32 s88, 61440\n\t" \
  "v_mfma_f32_16x16x32_bf16 v[16:19], a[184:187], v[240:243], v[16:19]\n\t" \
  "buffer_load_dwordx4 a[48:51], v202, s[36:39], s88 offen offset:0 sc1\n\t" \
  "v_mfma_f32_16x16x32_bf16 v[36:39], a[184:187], v[244:247], v[36:39]\n\t" \
  "v_mfma_f32_16x16x32_bf16 v[56:59], a[184:187], v[248:251], v[56:59]\n\t" \
  "buffer_load_dwordx4 a[52:55], v202, s[36:39], s88 offen offset:1024 sc1\n\t" \
  "v_mfma_f32_16x16x32_bf16 v[76:79], a[184:187], v[252:255], v[76:79]\n\t" \
  "v_mfma_f32_16x16x32_bf16 v[20:23], a[188:191], v[240:243], v[20:23]\n\t" \
  "buffer_load_dwordx4 a[56:59], v202, s[36:39], s88 offen offset:2048 sc1\n\t" \
  "v_mfma_f32_16x16x32_bf16 v[40:43], a[188:191], v[244:247], v[40:43]\n\t" \
  "v_mfma_f32_16x16x32_bf16 v[60:63], a[188:191], v[248:251], v[60:63]\n\t" \
  "v_mfma_f32_16x16x32_bf16 v[80:83], a[188:191], v[252:255], v[80:83]\n\t" \
  "v_mfma_f32_16x16x32_bf16 v[24:27], a[128:131], v[240:243], v[24:27]\n\t" \
  "v_mfma_f32_16x16x32_bf16 v[44:47], a[128:131], v[244:247], v[44:47]\n\t" \
  "v_mfma_f32_16x16x32_bf16 v[64:67], a[128:131], v[248:251], v[64:67]\n\t" \
  "v_mfma_f32_16x16x32_bf16 v[84:87], a[128:131], v[252:255], v[84:87]\n\t" \
  "s_waitcnt vmcnt(27)\n\t" \
  "v_mfma_f32_16x16x32_bf16 v[8:11], a[132:135], v[152:155], v[8:11]\n\t" \
  "s_add_u32 s76, s79, 36864\n\t" \
  "buffer_load_dwordx4 v[240:243], v204, s[44:47], s76 offen sc1\n\t" \
  "s_waitcnt vmcnt(27)\n\t" \
  "v_mfma_f32_16x16x32_bf16 v[28:31], a[132:135], v[156:159], v[28:31]\n\t" \
  "s_waitcnt vmcnt(26)\n\t" \
  "v_mfma_f32_16x16x32_bf16 v[48:51], a[132:135], v[160:163], v[48:51]\n\t" \
  "buffer_load_dwordx4 v[244:247], v205, s[44:47], s76 offen sc1\n\t" \
  "s_waitcnt vmcnt(26)\n\t" \
  "v_mfma_f32_16x16x32_bf16 v[68:71], a[132:135], v[164:167], v[68:71]\n\t" \
  "v_mfma_f32_16x16x32_bf16 v[12:15], a[136:139], v[152:155], v[12:15]\n\t" \
  "buffer_load_dwordx4 v[248:251], v206, s[44:47], s76 offen sc1\n\t" \
  "v_mfma_f32_16x16x32_bf16 v[32:35], a[136:139], v[156:159], v[32:35]\n\t" \
  "v_mfma_f32_16x16x32_bf16 v[52:55], a[136:139], v[160:163], v[52:55]\n\t" \
  "buffer_load_dwordx4 v[252:255], v207, s[44:47], s76 offen sc1\n\t" \
  "v_mfma_f32_16x16x32_bf16 v[72:75], a[136:139], v[164:167], v[72:75]\n\t" \
  "v_mfma_f32_16x16x32_bf16 v[16:19], a[140:143], v[152:155], v[16:19]\n\t" \
  "buffer_load_dwordx4 a[60:63], v202, s[36:39], s88 offen offset:3072 sc1\n\t" \
  "v_mfma_f32_16x16x32_bf16 v[36:39], a[140:143], v[156:159], v[36:39]\n\t" \
  "s_mov_b32 s88, 65536\n\t" \
  "v_mfma_f32_16x16x32_bf16 v[56:59], a[140:143], v[160:163], v[56:59]\n\t" \
  "buffer_load_dwordx4 a[64:67], v202, s[36:39], s88 offen offset:0 sc1\n\t" \
  "v_mfma_f32_16x16x32_bf16 v[76:79], a[140:143], v[164:167], v[76:79]\n\t" \
  "v_mfma_f32_16x16x32_bf16 v[20:23], a[144:147], v[152:155], v[20:23]\n\t" \
  "buffer_load_dwordx4 a[68:71], v202, s[36:39], s88 offen offset:1024 sc1\n\t" \
  "v_mfma_f32_16x16x32_bf16 v[40:43], a[144:147], v[156:159], v[40:43]\n\t" \
  "v_mfma_f32_16x16x32_bf16 v[60:63], a[144:147], v[160:163], v[60:63]\n\t" \
  "v_mfma_f32_16x16x32_bf16 v[80:83], a[144:147], v[164:167], v[80:83]\n\t" \
  "v_mfma_f32_16x16x32_bf16 v[24:27], a[148:151], v[152:155], v[24:27]\n\t" \
  "v_mfma_f32_16x16x32_bf16 v[44:47], a[148:151], v[156:159], v[44:47]\n\t" \
  "v_mfma_f32_16x16x32_bf16 v[64:67], a[148:151], v[160:163], v[64:67]\n\t" \
  "v_mfma_f32_16x16x32_bf16 v[84:87], a[148:151], v[164:167], v[84:87]\n\t" \
  "s_waitcnt vmcnt(27)\n\t" \
  "v_mfma_f32_16x16x32_bf16 v[8:11], a[152:155], v[168:171], v[8:11]\n\t" \
  "s_add_u32 s76, s79, 40960\n\t" \
  "buffer_load_dwordx4 v[152:155], v204, s[44:47], s76 offen sc1\n\t" \
  "s_waitcnt vmcnt(27)\n\t" \
  "v_mfma_f32_16x16x32_bf16 v[28:31], a[152:155], v[172:175], v[28:31]\n\t" \
  "s_waitcnt vmcnt(26)\n\t" \
  "v_mfma_f32_16x16x32_bf16 v[48:51], a[152:155], v[176:179], v[48:51]\n\t" \
  "buffer_load_dwordx4 v[156:159], v205, s[44:47], s76 offen sc1\n\t" \
  "s_waitcnt vmcnt(26)\n\t" \
  "v_mfma_f32_16x16x32_bf16 v[68:71], a[152:155], v[180:183], v[68:71]\n\t" \
  "v_mfma_f32_16x16x32_bf16 v[12:15], a[156:159], v[168:171], v[12:15]\n\t" \
  "buffer_load_dwordx4 v[160:163], v206, s[44:47], s76 offen sc1\n\t" \
  "v_mfma_f32_16x16x32_bf16 v[32:35], a[156:159], v[172:175], v[32:35]\n\t" \
  "v_mfma_f32_16x16x32_bf16 v[52:55], a[156:159], v[176:179], v[52:55]\n\t" \
  "buffer_load_dwordx4 v[164:167], v207, s[44:47], s76 offen sc1\n\t" \
  "v_mfma_f32_16x16x32_bf16 v[72:75], a[156:159], v[180:183], v[72:75]\n\t" \
  "v_mfma_f32_16x16x32_bf16 v[16:19], v[88:91], v[168:171], v[16:19]\n\t" \
  "buffer_load_dwordx4 a[72:75], v202, s[36:39], s88 offen offset:2048 sc1\n\t" \
  "v_mfma_f32_16x16x32_bf16 v[36:39], v[88:91], v[172:175], v[36:39]\n\t" \
  "v_mfma_f32_16x16x32_bf16 v[56:59], v[88:91], v[176:179], v[56:59]\n\t" \
  "buffer_load_dwordx4 a[76:79], v202, s[36:39], s88 offen offset:3072 sc1\n\t" \
  "v_mfma_f32_16x16x32_bf16 v[76:79], v[88:91], v[180:183], v[76:79]\n\t" \
  "s_mov_b32 s88, 69632\n\t" \
  "v_mfma_f32_16x16x32_bf16 v[20:23], v[92:95], v[168:171], v[20:23]\n\t" \
  "buffer_load_dwordx4 a[80:83], v202, s[36:39], s88 offen offset:0 sc1\n\t" \
  "v_mfma_f32_16x16x32_bf16 v[40:43], v[92:95], v[172:175], v[40:43]\n\t" \
  "v_mfma_f32_16x16x32_bf16 v[60:63], v[92:95], v[176:179], v[60:63]\n\t" \
  "v_mfma_f32_16x16x32_bf16 v[80:83], v[92:95], v[180:183], v[80:83]\n\t" \
  "v_mfma_f32_16x16x32_bf16 v[24:27], v[96:99], v[168:171], v[24:27]\n\t" \
  "v_mfma_f32_16x16x32_bf16 v[44:47], v[96:99], v[172:175], v[44:47]\n\t" \
  "v_mfma_f32_16x16x32_bf16 v[64:67], v[96:99], v[176:179], v[64:67]\n\t" \
  "v_mfma_f32_16x16x32_bf16 v[84:87], v[96:99], v[180:183], v[84:87]\n\t" \
  "s_waitcnt vmcnt(27)\n\t" \
  "v_mfma_f32_16x16x32_bf16 v[8:11], v[100:103], v[184:187], v[8:11]\n\t" \
  "global_load_dword v213, v212, s[42:43] sc1\n\t" \
  "s_waitcnt vmcnt(27)\n\t" \
  "v_mfma_f32_16x16x32_bf16 v[28:31], v[100:103], v[188:191], v[28:31]\n\t" \
  "s_add_u32 s76, s79, 45056\n\t" \
  "s_waitcnt vmcnt(26)\n\t" \
  "v_mfma_f32_16x16x32_bf16 v[48:51], v[100:103], v[192:195], v[48:51]\n\t" \
  "buffer_load_dwordx4 v[168:171], v204, s[44:47], s76 offen sc1\n\t" \
  "s_waitcnt vmcnt(26)\n\t" \
  "v_mfma_f32_16x16x32_bf16 v[68:71], v[100:103], v[196:199], v[68:71]\n\t" \
  "v_mfma_f32_16x16x32_bf16 v[12:15], v[104:107], v[184:187], v[12:15]\n\t" \
  "buffer_load_dwordx4 v[172:175], v205, s[44:47], s76 offen sc1\n\t" \
  "v_mfma_f32_16x16x32_bf16 v[32:35], v[104:107], v[188:191], v[32:35]\n\t" \
  "v_mfma_f32_16x16x32_bf16 v[52:55], v[104:107], v[192:195], v[52:55]\n\t" \
  "buffer_load_dwordx4 v[176:179], v206, s[44:47], s76 offen sc1\n\t" \
  "v_mfma_f32_16x16x32_bf16 v[72:75], v[104:107], v[196:199], v[72:75]\n\t" \
  "v_mfma_f32_16x16x32_bf16 v[16:19], v[108:111], v[184:187], v[16:19]\n\t" \
  "buffer_load_dwordx4 v[180:183], v207, s[44:47], s76 offen sc1\n\t" \
  "v_mfma_f32_16x16x32_bf16 v[36:39], v[108:111], v[188:191], v[36:39]\n\t" \
  "v_mfma_f32_16x16x32_bf16 v[56:59], v[108:111], v[192:195], v[56:59]\n\t" \
  "buffer_load_dwordx4 a[84:87], v202, s[36:39], s88 offen offset:1024 sc1\n\t" \
  "v_mfma_f32_16x16x32_bf16 v[76:79], v[108:111], v[196:199], v[76:79]\n\t" \
  "v_mfma_f32_16x16x32_bf16 v[20:23], v[112:115], v[184:187], v[20:23]\n\t" \
  "buffer_load_dwordx4 a[88:91], v202, s[36:39], s88 offen offset:2048 sc1\n\t" \
  "v_mfma_f32_16x16x32_bf16 v[40:43], v[112:115], v[188:191], v[40:43]\n\t" \
  "v_mfma_f32_16x16x32_bf16 v[60:63], v[112:115], v[192:195], v[60:63]\n\t" \
  "buffer_load_dwordx4 a[92:95], v202, s[36:39], s88 offen offset:3072 sc1\n\t" \
  "v_mfma_f32_16x16x32_bf16 v[80:83], v[112:115], v[196:199], v[80:83]\n\t" \
  "v_mfma_f32_16x16x32_bf16 v[24:27], v[116:119], v[184:187], v[24:27]\n\t" \
  "v_mfma_f32_16x16x32_bf16 v[44:47], v[116:119], v[188:191], v[44:47]\n\t" \
  "v_mfma_f32_16x16x32_bf16 v[64:67], v[116:119], v[192:195], v[64:67]\n\t" \
  "v_mfma_f32_16x16x32_bf16 v[84:87], v[116:119], v[196:199], v[84:87]\n\t" \
  "s_waitcnt vmcnt(28)\n\t" \
  "v_mfma_f32_16x16x32_bf16 v[8:11], v[120:123], v[224:227], v[8:11]\n\t" \
  "s_add_u32 s76, s79, 49152\n\t" \
  "buffer_load_dwordx4 v[184:187], v204, s[44:47], s76 offen sc1\n\t" \
  "s_waitcnt vmcnt(28)\n\t" \
  "v_mfma_f32_16x16x32_bf16 v[28:31], v[120:123], v[228:231], v[28:31]\n\t" \
  "s_waitcnt vmcnt(27)\n\t" \
  "v_mfma_f32_16x16x32_bf16 v[48:51], v[120:123], v[232:235], v[48:51]\n\t" \
  "buffer_load_dwordx4 v[188:191], v205, s[44:47], s76 offen sc1\n\t" \
  "s_waitcnt vmcnt(27)\n\t" \
  "v_mfma_f32_16x16x32_bf16 v[68:71], v[120:123], v[236:239], v[68:71]\n\t" \
  "v_mfma_f32_16x16x32_bf16 v[12:15], v[124:127], v[224:227], v[12:15]\n\t" \
  "buffer_load_dwordx4 v[192:195], v206, s[44:47], s76 offen sc1\n\t" \
  "v_mfma_f32_16x16x32_bf16 v[32:35], v[124:127], v[228:231], v[32:35]\n\t" \
  "v_mfma_f32_16x16x32_bf16 v[52:55], v[124:127], v[232:235], v[52:55]\n\t" \
  "buffer_load_dwordx4 v[196:199], v207, s[44:47], s76 offen sc1\n\t" \
  "v_mfma_f32_16x16x32_bf16 v[72:75], v[124:127], v[236:239], v[72:75]\n\t" \
  "s_mov_b32 s88, 73728\n\t" \
  "v_mfma_f32_16x16x32_bf16 v[16:19], v[128:131], v[224:227], v[16:19]\n\t" \
  "buffer_load_dwordx4 a[96:99], v202, s[36:39], s88 offen offset:0 sc1\n\t" \
  "v_mfma_f32_16x16x32_bf16 v[36:39], v[128:131], v[228:231], v[36:39]\n\t" \
  "v_mfma_f32_16x16x32_bf16 v[56:59], v[128:131], v[232:235], v[56:59]\n\t" \
  "buffer_load_dwordx4 a[100:103], v202, s[36:39], s88 offen offset:1024 sc1\n\t" \
  "v_mfma_f32_16x16x32_bf16 v[76:79], v[128:131], v[236:239], v[76:79]\n\t" \
  "v_mfma_f32_16x16x32_bf16 v[20:23], v[132:135], v[224:227], v[20:23]\n\t" \
  "buffer_load_dwordx4 a[104:107], v202, s[36:39], s88 offen offset:2048 sc1\n\t" \
  "v_mfma_f32_16x16x32_bf16 v[40:43], v[132:135], v[228:231], v[40:43]\n\t" \
  "v_mfma_f32_16x16x32_bf16 v[60:63], v[132:135], v[232:235], v[60:63]\n\t" \
  "v_mfma_f32_16x16x32_bf16 v[80:83], v[132:135], v[236:239], v[80:83]\n\t" \
  "v_mfma_f32_16x16x32_bf16 v[24:27], v[136:139], v[224:227], v[24:27]\n\t" \
  "v_mfma_f32_16x16x32_bf16 v[44:47], v[136:139], v[228:231], v[44:47]\n\t" \
  "v_mfma_f32_16x16x32_bf16 v[64:67], v[136:139], v[232:235], v[64:67]\n\t" \
  "v_mfma_f32_16x16x32_bf16 v[84:87], v[136:139], v[236:239], v[84:87]\n\t" \
  "s_waitcnt vmcnt(14)\n\t" \
  "v_cmp_ne_u32 vcc, %[tag], v213\n\t" \
  "s_cbranch_vccnz .Lp2spin%=\n\t" \
  ".Lp2back%=:\n\t" \
  "v_mfma_f32_16x16x32_bf16 v[8:11], v[140:143], v[240:243], v[8:11]\n\t" \
  "s_add_u32 s76, s79, 53248\n\t" \
  "buffer_load_dwordx4 v[224:227], v204, s[44:47], s76 offen sc1\n\t" \
  "v_mfma_f32_16x16x32_bf16 v[28:31], v[140:143], v[244:247], v[28:31]\n\t" \
  "v_mfma_f32_16x16x32_bf16 v[48:51], v[140:143], v[248:251], v[48:51]\n\t" \
  "buffer_load_dwordx4 v[228:231], v205, s[44:47], s76 offen sc1\n\t" \
  "v_mfma_f32_16x16x32_bf16 v[68:71], v[140:143], v[252:255], v[68:71]\n\t" \
  "v_mfma_f32_16x16x32_bf16 v[12:15], v[144:147], v[240:243], v[12:15]\n\t" \
  "buffer_load_dwordx4 v[232:235], v206, s[44:47], s76 offen sc1\n\t" \
  "v_mfma_f32_16x16x32_bf16 v[32:35], v[144:147], v[244:247], v[32:35]\n\t" \
  "v_mfma_f32_16x16x32_bf16 v[52:55], v[144:147], v[248:251], v[52:55]\n\t" \
  "buffer_load_dwordx4 v[236:239], v207, s[44:47], s76 offen sc1\n\t" \
  "v_mfma_f32_16x16x32_bf16 v[72:75], v[144:147], v[252:255], v[72:75]\n\t" \
  "v_mfma_f32_16x16x32_bf16 v[16:19], v[148:151], v[240:243], v[16:19]\n\t" \
  "buffer_load_dwordx4 a[108:111], v202, s[36:39], s88 offen offset:3072 sc1\n\t" \
  "v_mfma_f32_16x16x32_bf16 v[36:39], v[148:151], v[244:247], v[36:39]\n\t" \
  "s_mov_b32 s88, 77824\n\t" \
  "v_mfma_f32_16x16x32_bf16 v[56:59], v[148:151], v[248:251], v[56:59]\n\t" \
  "buffer_load_dwordx4 a[112:115], v202, s[36:39], s88 offen offset:0 sc1\n\t" \
  "v_mfma_f32_16x16x32_bf16 v[76:79], v[148:151], v[252:255], v[76:79]\n\t" \
  "v_mfma_f32_16x16x32_bf16 v[20:23], a[0:3], v[240:243], v[20:23]\n\t" \
  "buffer_load_dwordx4 a[116:119], v202, s[36:39], s88 offen offset:1024 sc1\n\t" \
  "v_mfma_f32_16x16x32_bf16 v[40:43], a[0:3], v[244:247], v[40:43]\n\t" \
  "v_mfma_f32_16x16x32_bf16 v[60:63], a[0:3], v[248:251], v[60:63]\n\t" \
  "v_mfma_f32_16x16x32_bf16 v[80:83], a[0:3], v[252:255], v[80:83]\n\t" \
  "v_mfma_f32_16x16x32_bf16 v[24:27], a[4:7], v[240:243], v[24:27]\n\t" \
  "v_mfma_f32_16x16x32_bf16 v[44:47], a[4:7], v[244:247], v[44:47]\n\t" \
  "v_mfma_f32_16x16x32_bf16 v[64:67], a[4:7], v[248:251], v[64:67]\n\t" \
  "v_mfma_f32_16x16x32_bf16 v[84:87], a[4:7], v[252:255], v[84:87]\n\t" \
  "v_mfma_f32_16x16x32_bf16 v[8:11], a[8:11], v[152:155], v[8:11]\n\t" \
  "s_add_u32 s76, s79, 57344\n\t" \
  "buffer_load_dwordx4 v[240:243], v204, s[44:47], s76 offen sc1\n\t" \
  "v_mfma_f32_16x16x32_bf16 v[28:31], a[8:11], v[156:159], v[28:31]\n\t" \
  "v_mfma_f32_16x16x32_bf16 v[48:51], a[8:11], v[160:163], v[48:51]\n\t" \
  "buffer_load_dwordx4 v[244:247], v205, s[44:47], s76 offen sc1\n\t" \
  "v_mfma_f32_16x16x32_bf16 v[68:71], a[8:11], v[164:167], v[68:71]\n\t" \
  "v_mfma_f32_16x16x32_bf16 v[12:15], a[12:15], v[152:155], v[12:15]\n\t" \
  "buffer_load_dwordx4 v[248:251], v206, s[44:47], s76 offen sc1\n\t" \
  "v_mfma_f32_16x16x32_bf16 v[32:35], a[12:15], v[156:159], v[32:35]\n\t" \
  "v_mfma_f32_16x16x32_bf16 v[52:55], a[12:15], v[160:163], v[52:55]\n\t" \
  "buffer_load_dwordx4 v[252:255], v207, s[44:47], s76 offen sc1\n\t" \
  "v_mfma_f32_16x16x32_bf16 v[72:75], a[12:15], v[164:167], v[72:75]\n\t" \
  "v_mfma_f32_16x16x32_bf16 v[16:19], a[16:19], v[152:155], v[16:19]\n\t" \
  "buffer_load_dwordx4 a[120:123], v202, s[36:39], s88 offen offset:2048 sc1\n\t" \
  "v_mfma_f32_16x16x32_bf16 v[36:39], a[16:19], v[156:159], v[36:39]\n\t" \
  "v_mfma_f32_16x16x32_bf16 v[56:59], a[16:19], v[160:163], v[56:59]\n\t" \
  "buffer_load_dwordx4 a[124:127], v202, s[36:39], s88 offen offset:3072 sc1\n\t" \
  "v_mfma_f32_16x16x32_bf16 v[76:79], a[16:19], v[164:167], v[76:79]\n\t" \
  "v_mfma_f32_16x16x32_bf16 v[20:23], a[20:23], v[152:155], v[20:23]\n\t" \
  "v_mfma_f32_16x16x32_bf16 v[40:43], a[20:23], v[156:159], v[40:43]\n\t" \
  "v_mfma_f32_16x16x32_bf16 v[60:63], a[20:23], v[160:163], v[60:63]\n\t" \
  "v_mfma_f32_16x16x32_bf16 v[80:83], a[20:23], v[164:167], v[80:83]\n\t" \
  "v_mfma_f32_16x16x32_bf16 v[24:27], a[24:27], v[152:155], v[24:27]\n\t" \
  "s_add_u32 s76, s79, 61440\n\t" \
  "v_mfma_f32_16x16x32_bf16 v[44:47], a[24:27], v[156:159], v[44:47]\n\t" \
  "buffer_load_dwordx4 v[152:155], v204, s[44:47], s76 offen sc1\n\t" \
  "v_mfma_f32_16x16x32_bf16 v[64:67], a[24:27], v[160:163], v[64:67]\n\t" \
  "v_mfma_f32_16x16x32_bf16 v[84:87], a[24:27], v[164:167], v[84:87]\n\t" \
  "s_waitcnt vmcnt(27)\n\t" \
  "v_mfma_f32_16x16x32_bf16 v[8:11], a[28:31], v[168:171], v[8:11]\n\t" \
  "buffer_load_dwordx4 v[156:159], v205, s[44:47], s76 offen sc1\n\t" \
  "s_waitcnt vmcnt(27)\n\t" \
  "v_mfma_f32_16x16x32_bf16 v[28:31], a[28:31], v[172:175], v[28:31]\n\t" \
  "s_waitcnt vmcnt(26)\n\t" \
  "v_mfma_f32_16x16x32_bf16 v[48:51], a[28:31], v[176:179], v[48:51]\n\t" \
  "buffer_load_dwordx4 v[160:163], v206, s[44:47], s76 offen sc1\n\t" \
  "s_waitcnt vmcnt(26)\n\t" \
  "v_mfma_f32_16x16x32_bf16 v[68:71], a[28:31], v[180:183], v[68:71]\n\t" \
  "v_mfma_f32_16x16x32_bf16 v[12:15], a[32:35], v[168:171], v[12:15]\n\t" \
  "buffer_load_dwordx4 v[164:167], v207, s[44:47], s76 offen sc1\n\t" \
  "v_mfma_f32_16x16x32_bf16 v[32:35], a[32:35], v[172:175], v[32:35]\n\t" \
  "v_mfma_f32_16x16x32_bf16 v[52:55], a[32:35], v[176:179], v[52:55]\n\t" \
  "v_mfma_f32_16x16x32_bf16 v[72:75], a[32:35], v[180:183], v[72:75]\n\t" \
  "v_mfma_f32_16x16x32_bf16 v[16:19], a[36:39], v[168:171], v[16:19]\n\t" \
  "v_mfma_f32_16x16x32_bf16 v[36:39], a[36:39], v[172:175], v[36:39]\n\t" \
  "v_mfma_f32_16x16x32_bf16 v[56:59], a[36:39], v[176:179], v[56:59]\n\t" \
  "v_mfma_f32_16x16x32_bf16 v[76:79], a[36:39], v[180:183], v[76:79]\n\t" \
  "v_mfma_f32_16x16x32_bf16 v[20:23], a[40:43], v[168:171], v[20:23]\n\t" \
  "v_mfma_f32_16x16x32_bf16 v[40:43], a[40:43], v[172:175], v[40:43]\n\t" \
  "v_mfma_f32_16x16x32_bf16 v[60:63], a[40:43], v[176:179], v[60:63]\n\t" \
  "v_mfma_f32_16x16x32_bf16 v[80:83], a[40:43], v[180:183], v[80:83]\n\t" \
  "v_mfma_f32_16x16x32_bf16 v[24:27], a[44:47], v[168:171], v[24:27]\n\t" \
  "s_add_u32 s76, s83, 0\n\t" \
  "v_mfma_f32_16x16x32_bf16 v[44:47], a[44:47], v[172:175], v[44:47]\n\t" \
  "buffer_load_dwordx4 v[168:171], v204, s[44:47], s76 offen sc1\n\t" \
  "v_mfma_f32_16x16x32_bf16 v[64:67], a[44:47], v[176:179], v[64:67]\n\t" \
  "v_mfma_f32_16x16x32_bf16 v[84:87], a[44:47], v[180:183], v[84:87]\n\t" \
  "buffer_load_dwordx4 v[172:175], v205, s[44:47], s76 offen sc1\n\t" \
  "s_waitcnt vmcnt(25)\n\t" \
  "v_mfma_f32_16x16x32_bf16 v[8:11], a[48:51], v[184:187], v[8:11]\n\t" \
  "buffer_load_dwordx4 v[176:179], v206, s[44:47], s76 offen sc1\n\t" \
  "s_waitcnt vmcnt(25)\n\t" \
  "v_mfma_f32_16x16x32_bf16 v[28:31], a[48:51], v[188:191], v[28:31]\n\t" \
  "s_waitcnt vmcnt(24)\n\t" \
  "v_mfma_f32_16x16x32_bf16 v[48:51], a[48:51], v[192:195], v[48:51]\n\t" \
  "buffer_load_dwordx4 v[180:183], v207, s[44:47], s76 offen sc1\n\t" \
  "s_waitcnt vmcnt(24)\n\t" \
  "v_mfma_f32_16x16x32_bf16 v[68:71], a[48:51], v[196:199], v[68:71]\n\t" \
  "v_mfma_f32_16x16x32_bf16 v[12:15], a[52:55], v[184:187], v[12:15]\n\t" \
  "v_mfma_f32_16x16x32_bf16 v[32:35], a[52:55], v[188:191], v[32:35]\n\t" \
  "v_mfma_f32_16x16x32_bf16 v[52:55], a[52:55], v[192:195], v[52:55]\n\t" \
  "v_mfma_f32_16x16x32_bf16 v[72:75], a[52:55], v[196:199], v[72:75]\n\t" \
  "v_mfma_f32_16x16x32_bf16 v[16:19], a[56:59], v[184:187], v[16:19]\n\t" \
  "v_mfma_f32_16x16x32_bf16 v[36:39], a[56:59], v[188:191], v[36:39]\n\t" \
  "v_mfma_f32_16x16x32_bf16 v[56:59], a[56:59], v[192:195], v[56:59]\n\t" \
  "v_mfma_f32_16x16x32_bf16 v[76:79], a[56:59], v[196:199], v[76:79]\n\t" \
  "v_mfma_f32_16x16x32_bf16 v[20:23], a[60:63], v[184:187], v[20:23]\n\t" \
  "v_mfma_f32_16x16x32_bf16 v[40:43], a[60:63], v[188:191], v[40:43]\n\t" \
  "v_mfma_f32_16x16x32_bf16 v[60:63], a[60:63], v[192:195], v[60:63]\n\t" \
  "v_mfma_f32_16x16x32_bf16 v[80:83], a[60:63], v[196:199], v[80:83]\n\t" \
  "v_mfma_f32_16x16x32_bf16 v[24:27], a[64:67], v[184:187], v[24:27]\n\t" \
  "s_add_u32 s76, s83, 4096\n\t" \
  "v_mfma_f32_16x16x32_bf16 v[44:47], a[64:67], v[188:191], v[44:47]\n\t" \
  "buffer_load_dwordx4 v[184:187], v204, s[44:47], s76 offen sc1\n\t" \
  "v_mfma_f32_16x16x32_bf16 v[64:67], a[64:67], v[192:195], v[64:67]\n\t" \
  "v_mfma_f32_16x16x32_bf16 v[84:87], a[64:67], v[196:199], v[84:87]\n\t" \
  "buffer_load_dwordx4 v[188:191], v205, s[44:47], s76 offen sc1\n\t" \
  "s_waitcnt vmcnt(22)\n\t" \
  "v_mfma_f32_16x16x32_bf16 v[8:11], a[68:71], v[224:227], v[8:11]\n\t" \
  "buffer_load_dwordx4 v[192:195], v206, s[44:47], s76 offen sc1\n\t" \
  "s_waitcnt vmcnt(22)\n\t" \
  "v_mfma_f32_16x16x32_bf16 v[28:31], a[68:71], v[228:231], v[28:31]\n\t" \
  "s_waitcnt vmcnt(21)\n\t" \
  "v_mfma_f32_16x16x32_bf16 v[48:51], a[68:71], v[232:235], v[48:51]\n\t" \
  "buffer_load_dwordx4 v[196:199], v207, s[44:47], s76 offen sc1\n\t" \
  "s_waitcnt vmcnt(21)\n\t" \
  "v_mfma_f32_16x16x32_bf16 v[68:71], a[68:71], v[236:239], v[68:71]\n\t" \
  "v_mfma_f32_16x16x32_bf16 v[12:15], a[72:75], v[224:227], v[12:15]\n\t" \
  "v_mfma_f32_16x16x32_bf16 v[32:35], a[72:75], v[228:231], v[32:35]\n\t" \
  "v_mfma_f32_16x16x32_bf16 v[52:55], a[72:75], v[232:235], v[52:55]\n\t" \
  "v_mfma_f32_16x16x32_bf16 v[72:75], a[72:75], v[236:239], v[72:75]\n\t" \
  "v_mfma_f32_16x16x32_bf16 v[16:19], a[76:79], v[224:227], v[16:19]\n\t" \
  "v_mfma_f32_16x16x32_bf16 v[36:39], a[76:79], v[228:231], v[36:39]\n\t" \
  "v_mfma_f32_16x16x32_bf16 v[56:59], a[76:79], v[232:235], v[56:59]\n\t" \
  "v_mfma_f32_16x16x32_bf16 v[76:79], a[76:79], v[236:239], v[76:79]\n\t" \
  "v_mfma_f32_16x16x32_bf16 v[20:23], a[80:83], v[224:227], v[20:23]\n\t" \
  "v_mfma_f32_16x16x32_bf16 v[40:43], a[80:83], v[228:231], v[40:43]\n\t" \
  "v_mfma_f32_16x16x32_bf16 v[60:63], a[80:83], v[232:235], v[60:63]\n\t" \
  "v_mfma_f32_16x16x32_bf16 v[80:83], a[80:83], v[236:239], v[80:83]\n\t" \
  "v_mfma_f32_16x16x32_bf16 v[24:27], a[84:87], v[224:227], v[24:27]\n\t" \
  "s_add_u32 s76, s83, 8192\n\t" \
  "v_mfma_f32_16x16x32_bf16 v[44:47], a[84:87], v[228:231], v[44:47]\n\t" \
  "buffer_load_dwordx4 v[224:227], v204, s[44:47], s76 offen sc1\n\t" \
  "v_mfma_f32_16x16x32_bf16 v[64:67], a[84:87], v[232:235], v[64:67]\n\t" \
  "v_mfma_f32_16x16x32_bf16 v[84:87], a[84:87], v[236:239], v[84:87]\n\t" \
  "buffer_load_dwordx4 v[228:231], v205, s[44:47], s76 offen sc1\n\t" \
  "s_waitcnt vmcnt(19)\n\t" \
  "v_mfma_f32_16x16x32_bf16 v[8:11], a[88:91], v[240:243], v[8:11]\n\t" \
  "buffer_load_dwordx4 v[232:235], v206, s[44:47], s76 offen sc1\n\t" \
  "s_waitcnt vmcnt(19)\n\t" \
  "v_mfma_f32_16x16x32_bf16 v[28:31], a[88:91], v[244:247], v[28:31]\n\t" \
  "s_waitcnt vmcnt(18)\n\t" \
  "v_mfma_f32_16x16x32_bf16 v[48:51], a[88:91], v[248:251], v[48:51]\n\t" \
  "buffer_load_dwordx4 v[236:239], v207, s[44:47], s76 offen sc1\n\t" \
  "s_waitcnt vmcnt(18)\n\t" \
  "v_mfma_f32_16x16x32_bf16 v[68:71], a[88:91], v[252:255], v[68:71]\n\t" \
  "v_mfma_f32_16x16x32_bf16 v[12:15], a[92:95], v[240:243], v[12:15]\n\t" \
  "v_mfma_f32_16x16x32_bf16 v[32:35], a[92:95], v[244:247], v[32:35]\n\t" \
  "v_mfma_f32_16x16x32_bf16 v[52:55], a[92:95], v[248:251], v[52:55]\n\t" \
  "v_mfma_f32_16x16x32_bf16 v[72:75], a[92:95], v[252:255], v[72:75]\n\t" \
  "v_mfma_f32_16x16x32_bf16 v[16:19], a[96:99], v[240:243], v[16:19]\n\t" \
  "v_mfma_f32_16x16x32_bf16 v[36:39], a[96:99], v[244:247], v[36:39]\n\t" \
  "v_mfma_f32_16x16x32_bf16 v[56:59], a[96:99], v[248:251], v[56:59]\n\t" \
  "v_mfma_f32_16x16x32_bf16 v[76:79], a[96:99], v[252:255], v[76:79]\n\t" \
  "v_mfma_f32_16x16x32_bf16 v[20:23], a[100:103], v[240:243], v[20:23]\n\t" \
  "v_mfma_f32_16x16x32_bf16 v[40:43], a[100:103], v[244:247], v[40:43]\n\t" \
  "v_mfma_f32_16x16x32_bf16 v[60:63], a[100:103], v[248:251], v[60:63]\n\t" \
  "v_mfma_f32_16x16x32_bf16 v[80:83], a[100:103], v[252:255], v[80:83]\n\t" \
  "v_mfma_f32_16x16x32_bf16 v[24:27], a[104:107], v[240:243], v[24:27]\n\t" \
  "s_add_u32 s76, s83, 12288\n\t" \
  "v_mfma_f32_16x16x32_bf16 v[44:47], a[104:107], v[244:247], v[44:47]\n\t" \
  "buffer_load_dwordx4 v[240:243], v204, s[44:47], s76 offen sc1\n\t" \
  "v_mfma_f32_16x16x32_bf16 v[64:67], a[104:107], v[248:251], v[64:67]\n\t" \
  "v_mfma_f32_16x16x32_bf16 v[84:87], a[104:107], v[252:255], v[84:87]\n\t" \
  "buffer_load_dwordx4 v[244:247], v205, s[44:47], s76 offen sc1\n\t" \
  "s_waitcnt vmcnt(17)\n\t" \
  "v_mfma_f32_16x16x32_bf16 v[8:11], a[108:111], v[152:155], v[8:11]\n\t" \
  "buffer_load_dwordx4 v[248:251], v206, s[44:47], s76 offen sc1\n\t" \
  "s_waitcnt vmcnt(17)\n\t" \
  "v_mfma_f32_16x16x32_bf16 v[28:31], a[108:111], v[156:159], v[28:31]\n\t" \
  "s_waitcnt vmcnt(16)\n\t" \
  "v_mfma_f32_16x16x32_bf16 v[48:51], a[108:111], v[160:163], v[48:51]\n\t" \
  "buffer_load_dwordx4 v[252:255], v207, s[44:47], s76 offen sc1\n\t" \
  "s_waitcnt vmcnt(16)\n\t" \
  "v_mfma_f32_16x16x32_bf16 v[68:71], a[108:111], v[164:167], v[68:71]\n\t" \
  "v_mfma_f32_16x16x32_bf16 v[12:15], a[112:115], v[152:155], v[12:15]\n\t" \
  "v_mfma_f32_16x16x32_bf16 v[32:35], a[112:115], v[156:159], v[32:35]\n\t" \
  "v_mfma_f32_16x16x32_bf16 v[52:55], a[112:115], v[160:163], v[52:55]\n\t" \
  "v_mfma_f32_16x16x32_bf16 v[72:75], a[112:115], v[164:167], v[72:75]\n\t" \
  "v_mfma_f32_16x16x32_bf16 v[16:19], a[116:119], v[152:155], v[16:19]\n\t" \
  "v_mfma_f32_16x16x32_bf16 v[36:39], a[116:119], v[156:159], v[36:39]\n\t" \
  "ds_write_b128 v208, v[28:31] offset:0\n\t" \
  "v_mfma_f32_16x16x32_bf16 v[56:59], a[116:119], v[160:163], v[56:59]\n\t" \
  "ds_write_b128 v209, v[48:51] offset:0\n\t" \
  "v_mfma_f32_16x16x32_bf16 v[76:79], a[116:119], v[164:167], v[76:79]\n\t" \
  "ds_write_b128 v210, v[68:71] offset:0\n\t" \
  "v_mfma_f32_16x16x32_bf16 v[20:23], a[120:123], v[152:155], v[20:23]\n\t" \
  "v_mfma_f32_16x16x32_bf16 v[40:43], a[120:123], v[156:159], v[40:43]\n\t" \
  "ds_write_b128 v208, v[32:35] offset:1024\n\t" \
  "v_mfma_f32_16x16x32_bf16 v[60:63], a[120:123], v[160:163], v[60:63]\n\t" \
  "ds_write_b128 v209, v[52:55] offset:1024\n\t" \
  "v_mfma_f32_16x16x32_bf16 v[80:83], a[120:123], v[164:167], v[80:83]\n\t" \
  "ds_write_b128 v210, v[72:75] offset:1024\n\t" \
  "v_mfma_f32_16x16x32_bf16 v[24:27], a[124:127], v[152:155], v[24:27]\n\t" \
  "s_add_u32 s76, s83, 16384\n\t" \
  "v_mfma_f32_16x16x32_bf16 v[44:47], a[124:127], v[156:159], v[44:47]\n\t" \
  "buffer_load_dwordx4 v[152:155], v204, s[44:47], s76 offen sc1\n\t" \
  "ds_write_b128 v208, v[36:39] offset:2048\n\t" \
  "v_mfma_f32_16x16x32_bf16 v[64:67], a[124:127], v[160:163], v[64:67]\n\t" \
  "ds_write_b128 v209, v[56:59] offset:2048\n\t" \
  "v_mfma_f32_16x16x32_bf16 v[84:87], a[124:127], v[164:167], v[84:87]\n\t" \
  "buffer_load_dwordx4 v[156:159], v205, s[44:47], s76 offen sc1\n\t" \
  "ds_write_b128 v210, v[76:79] offset:2048\n\t" \
  "ds_write_b128 v208, v[40:43] offset:3072\n\t" \
  "ds_write_b128 v209, v[60:63] offset:3072\n\t" \
  "ds_write_b128 v210, v[80:83] offset:3072\n\t" \
  "ds_write_b128 v208, v[44:47] offset:4096\n\t" \
  "ds_write_b128 v209, v[64:67] offset:4096\n\t" \
  "s_nop 0\n\t" \
  "ds_write_b128 v210, v[84:87] offset:4096\n\t" \
  "s_add_u32 m0, s80, 0\n\t" \
  "s_mov_b32 s74, 0\n\t" \
  "buffer_load_dwordx4 v216, s[56:59], s74 offen offset:0 lds\n\t" \
  "buffer_load_dwordx4 v217, s[60:63], s74 offen offset:1024 lds\n\t" \
  "buffer_load_dwordx4 v218, s[56:59], s74 offen offset:2048 lds\n\t" \
  "buffer_load_dwordx4 v219, s[60:63], s74 offen offset:3072 lds\n\t" \
  "s_add_u32 m0, s80, 16384\n\t" \
  "s_mov_b32 s74, 221184\n\t" \
  "buffer_load_dwordx4 v216, s[56:59], s74 offen offset:0 lds\n\t" \
  "buffer_load_dwordx4 v217, s[60:63], s74 offen offset:1024 lds\n\t" \
  "buffer_load_dwordx4 v218, s[56:59], s74 offen offset:2048 lds\n\t" \
  "buffer_load_dwordx4 v219, s[60:63], s74 offen offset:3072 lds\n\t" \
  "s_waitcnt lgkmcnt(0)\n\t" \
  "s_barrier\n\t" \
  "ds_read_b128 v[28:31], v211 offset:0\n\t" \
  "ds_read_b128 v[32:35], v211 offset:1024\n\t" \
  "ds_read_b128 v[36:39], v211 offset:2048\n\t" \
  "ds_read_b128 v[40:43], v211 offset:3072\n\t" \
  "ds_read_b128 v[44:47], v211 offset:4096\n\t" \
  "ds_read_b128 v[48:51], v211 offset:5120\n\t" \
  "ds_read_b128 v[52:55], v211 offset:6144\n\t" \
  "ds_read_b128 v[56:59], v211 offset:7168\n\t" \
  "ds_read_b128 v[60:63], v211 offset:8192\n\t" \
  "ds_read_b128 v[64:67], v211 offset:9216\n\t" \
  "ds_read_b128 v[68:71], v211 offset:10240\n\t" \
  "ds_read_b128 v[72:75], v211 offset:11264\n\t" \
  "ds_read_b128 v[76:79], v211 offset:12288\n\t" \
  "ds_read_b128 v[80:83], v211 offset:13312\n\t" \
  "ds_read_b128 v[84:87], v211 offset:14336\n\t" \
  "s_waitcnt lgkmcnt(14)\n\t" \
  "v_pk_add_f32 v[8:9], v[8:9], v[28:29]\n\t" \
  "v_pk_add_f32 v[10:11], v[10:11], v[30:31]\n\t" \
  "s_waitcnt lgkmcnt(13)\n\t" \
  "v_pk_add_f32 v[12:13], v[12:13], v[32:33]\n\t" \
  "v_pk_add_f32 v[14:15], v[14:15], v[34:35]\n\t" \
  "s_waitcnt lgkmcnt(12)\n\t" \
  "v_pk_add_f32 v[16:17], v[16:17], v[36:37]\n\t" \
  "v_pk_add_f32 v[18:19], v[18:19], v[38:39]\n\t" \
  "s_waitcnt lgkmcnt(11)\n\t" \
  "v_pk_add_f32 v[20:21], v[20:21], v[40:41]\n\t" \
  "v_pk_add_f32 v[22:23], v[22:23], v[42:43]\n\t" \
  "s_waitcnt lgkmcnt(10)\n\t" \
  "v_pk_add_f32 v[24:25], v[24:25], v[44:45]\n\t" \
  "v_pk_add_f32 v[26:27], v[26:27], v[46:47]\n\t" \
  "s_waitcnt lgkmcnt(4)\n\t" \
  "v_pk_add_f32 v[48:49], v[48:49], v[68:69]\n\t" \
  "v_pk_add_f32 v[50:51], v[50:51], v[70:71]\n\t" \
  "s_waitcnt lgkmcnt(3)\n\t" \
  "v_pk_add_f32 v[52:53], v[52:53], v[72:73]\n\t" \
  "v_pk_add_f32 v[54:55], v[54:55], v[74:75]\n\t" \
  "s_waitcnt lgkmcnt(2)\n\t" \
  "v_pk_add_f32 v[56:57], v[56:57], v[76:77]\n\t" \
  "v_pk_add_f32 v[58:59], v[58:59], v[78:79]\n\t" \
  "s_waitcnt lgkmcnt(1)\n\t" \
  "v_pk_add_f32 v[60:61], v[60:61], v[80:81]\n\t" \
  "v_pk_add_f32 v[62:63], v[62:63], v[82:83]\n\t" \
  "s_waitcnt lgkmcnt(0)\n\t" \
  "v_pk_add_f32 v[64:65], v[64:65], v[84:85]\n\t" \
  "v_pk_add_f32 v[66:67], v[66:67], v[86:87]\n\t" \
  "v_pk_add_f32 v[8:9], v[8:9], v[48:49]\n\t" \
  "v_pk_add_f32 v[10:11], v[10:11], v[50:51]\n\t" \
  "v_pk_add_f32 v[12:13], v[12:13], v[52:53]\n\t" \
  "v_pk_add_f32 v[14:15], v[14:15], v[54:55]\n\t" \
  "v_pk_add_f32 v[16:17], v[16:17], v[56:57]\n\t" \
  "v_pk_add_f32 v[18:19], v[18:19], v[58:59]\n\t" \
  "v_pk_add_f32 v[20:21], v[20:21], v[60:61]\n\t" \
  "v_pk_add_f32 v[22:23], v[22:23], v[62:63]\n\t" \
  "v_pk_add_f32 v[24:25], v[24:25], v[64:65]\n\t" \
  "v_pk_add_f32 v[26:27], v[26:27], v[66:67]\n\t" \
  "buffer_store_dwordx4 v[8:11], v202, s[48:51], 0 offen offset:0 sc1\n\t" \
  "buffer_store_dwordx4 v[12:15], v202, s[48:51], 0 offen offset:1024 sc1\n\t" \
  "buffer_store_dwordx4 v[16:19], v202, s[48:51], 0 offen offset:2048 sc1\n\t" \
  "buffer_store_dwordx4 v[20:23], v202, s[48:51], 0 offen offset:3072 sc1\n\t" \
  "buffer_store_dwordx4 v[24:27], v202, s[48:51], s77 offen offset:0 sc1\n\t" \
  "s_waitcnt vmcnt(30)\n\t" \
  "v_mfma_f32_16x16x32_bf16 v[8:11], a[192:195], v[168:171], 0\n\t" \
  "s_waitcnt vmcnt(29)\n\t" \
  "v_mfma_f32_16x16x32_bf16 v[28:31], a[192:195], v[172:175], 0\n\t" \
  "s_waitcnt vmcnt(28)\n\t" \
  "v_mfma_f32_16x16x32_bf16 v[48:51], a[192:195], v[176:179], 0\n\t" \
  "s_waitcnt vmcnt(27)\n\t" \
  "v_mfma_f32_16x16x32_bf16 v[68:71], a[192:195], v[180:183], 0\n\t" \
  "v_mfma_f32_16x16x32_bf16 v[12:15], a[196:199], v[168:171], 0\n\t" \
  "v_mfma_f32_16x16x32_bf16 v[32:35], a[196:199], v[172:175], 0\n\t" \
  "v_mfma_f32_16x16x32_bf16 v[52:55], a[196:199], v[176:179], 0\n\t" \
  "v_mfma_f32_16x16x32_bf16 v[72:75], a[196:199], v[180:183], 0\n\t" \
  "v_mfma_f32_16x16x32_bf16 v[16:19], a[200:203], v[168:171], 0\n\t" \
  "v_mfma_f32_16x16x32_bf16 v[36:39], a[200:203], v[172:175], 0\n\t" \
  "v_mfma_f32_16x16x32_bf16 v[56:59], a[200:203], v[176:179], 0\n\t" \
  "v_mfma_f32_16x16x32_bf16 v[76:79], a[200:203], v[180:183], 0\n\t" \
  "v_mfma_f32_16x16x32_bf16 v[20:23], a[204:207], v[168:171], 0\n\t" \
  "v_mfma_f32_16x16x32_bf16 v[40:43], a[204:207], v[172:175], 0\n\t" \
  "v_mfma_f32_16x16x32_bf16 v[60:63], a[204:207], v[176:179], 0\n\t" \
  "v_mfma_f32_16x16x32_bf16 v[80:83], a[204:207], v[180:183], 0\n\t" \
  "v_mfma_f32_16x16x32_bf16 v[24:27], a[208:211], v[168:171], 0\n\t" \
  "v_mfma_f32_16x16x32_bf16 v[44:47], a[208:211], v[172:175], 0\n\t" \
  "v_mfma_f32_16x16x32_bf16 v[64:67], a[208:211], v[176:179], 0\n\t" \
  "v_mfma_f32_16x16x32_bf16 v[84:87], a[208:211], v[180:183], 0\n\t" \
  "s_waitcnt vmcnt(26)\n\t" \
  "v_mfma_f32_16x16x32_bf16 v[8:11], a[212:215], v[184:187], v[8:11]\n\t" \
  "s_waitcnt vmcnt(25)\n\t" \
  "v_mfma_f32_16x16x32_bf16 v[28:31], a[212:215], v[188:191], v[28:31]\n\t" \
  "s_waitcnt vmcnt(24)\n\t" \
  "v_mfma_f32_16x16x32_bf16 v[48:51], a[212:215], v[192:195], v[48:51]\n\t" \
  "s_waitcnt vmcnt(23)\n\t" \
  "v_mfma_f32_16x16x32_bf16 v[68:71], a[212:215], v[196:199], v[68:71]\n\t" \
  "v_mfma_f32_16x16x32_bf16 v[12:15], a[216:219], v[184:187], v[12:15]\n\t" \
  "v_mfma_f32_16x16x32_bf16 v[32:35], a[216:219], v[188:191], v[32:35]\n\t" \
  "v_mfma_f32_16x16x32_bf16 v[52:55], a[216:219], v[192:195], v[52:55]\n\t" \
  "v_mfma_f32_16x16x32_bf16 v[72:75], a[216:219], v[196:199], v[72:75]\n\t" \
  "v_mfma_f32_16x16x32_bf16 v[16:19], a[220:223], v[184:187], v[16:19]\n\t" \
  "v_mfma_f32_16x16x32_bf16 v[36:39], a[220:223], v[188:191], v[36:39]\n\t" \
  "v_mfma_f32_16x16x32_bf16 v[56:59], a[220:223], v[192:195], v[56:59]\n\t" \
  "v_mfma_f32_16x16x32_bf16 v[76:79], a[220:223], v[196:199], v[76:79]\n\t" \
  "v_mfma_f32_16x16x32_bf16 v[20:23], a[224:227], v[184:187], v[20:23]\n\t" \
  "v_mfma_f32_16x16x32_bf16 v[40:43], a[224:227], v[188:191], v[40:43]\n\t" \
  "v_mfma_f32_16x16x32_bf16 v[60:63], a[224:227], v[192:195], v[60:63]\n\t" \
  "v_mfma_f32_16x16x32_bf16 v[80:83], a[224:227], v[196:199], v[80:83]\n\t" \
  "v_mfma_f32_16x16x32_bf16 v[24:27], a[228:231], v[184:187], v[24:27]\n\t" \
  "v_mfma_f32_16x16x32_bf16 v[44:47], a[228:231], v[188:191], v[44:47]\n\t" \
  "v_mfma_f32_16x16x32_bf16 v[64:67], a[228:231], v[192:195], v[64:67]\n\t" \
  "v_mfma_f32_16x16x32_bf16 v[84:87], a[228:231], v[196:199], v[84:87]\n\t" \
  "s_waitcnt vmcnt(22)\n\t" \
  "v_mfma_f32_16x16x32_bf16 v[8:11], a[232:235], v[224:227], v[8:11]\n\t" \
  "s_waitcnt vmcnt(21)\n\t" \
  "v_mfma_f32_16x16x32_bf16 v[28:31], a[232:235], v[228:231], v[28:31]\n\t" \
  "s_waitcnt vmcnt(20)\n\t" \
  "v_mfma_f32_16x16x32_bf16 v[48:51], a[232:235], v[232:235], v[48:51]\n\t" \
  "s_waitcnt vmcnt(19)\n\t" \
  "v_mfma_f32_16x16x32_bf16 v[68:71], a[232:235], v[236:239], v[68:71]\n\t" \
  "v_mfma_f32_16x16x32_bf16 v[12:15], a[236:239], v[224:227], v[12:15]\n\t" \
  "v_mfma_f32_16x16x32_bf16 v[32:35], a[236:239], v[228:231], v[32:35]\n\t" \
  "v_mfma_f32_16x16x32_bf16 v[52:55], a[236:239], v[232:235], v[52:55]\n\t" \
  "v_mfma_f32_16x16x32_bf16 v[72:75], a[236:239], v[236:239], v[72:75]\n\t" \
  "v_mfma_f32_16x16x32_bf16 v[16:19], a[240:243], v[224:227], v[16:19]\n\t" \
  "v_mfma_f32_16x16x32_bf16 v[36:39], a[240:243], v[228:231], v[36:39]\n\t" \
  "v_mfma_f32_16x16x32_bf16 v[56:59], a[240:243], v[232:235], v[56:59]\n\t" \
  "v_mfma_f32_16x16x32_bf16 v[76:79], a[240:243], v[236:239], v[76:79]\n\t" \
  "v_mfma_f32_16x16x32_bf16 v[20:23], a[244:247], v[224:227], v[20:23]\n\t" \
  "v_mfma_f32_16x16x32_bf16 v[40:43], a[244:247], v[228:231], v[40:43]\n\t" \
  "v_mfma_f32_16x16x32_bf16 v[60:63], a[244:247], v[232:235], v[60:63]\n\t" \
  "v_mfma_f32_16x16x32_bf16 v[80:83], a[244:247], v[236:239], v[80:83]\n\t" \
  "v_mfma_f32_16x16x32_bf16 v[24:27], a[248:251], v[224:227], v[24:27]\n\t" \
  "v_mfma_f32_16x16x32_bf16 v[44:47], a[248:251], v[228:231], v[44:47]\n\t" \
  "v_mfma_f32_16x16x32_bf16 v[64:67], a[248:251], v[232:235], v[64:67]\n\t" \
  "v_mfma_f32_16x16x32_bf16 v[84:87], a[248:251], v[236:239], v[84:87]\n\t" \
  "s_mul_i32 s68, %[mt], 1024\n\t" \
  "s_mul_i32 s72, %[nt], 128\n\t" \
  "s_add_u32 s68, s68, s72\n\t" \
  "s_add_u32 s68, s68, 42303488\n\t" \
  "s_mov_b64 s[72:73], %[ws]\n\t" \
  "s_add_u32 s68, s68, s72\n\t" \
  "s_addc_u32 s69, s73, 0\n\t" \
  "s_waitcnt vmcnt(0) lgkmcnt(0)\n\t" \
  "s_barrier\n\t" \
  "s_cmp_lg_u32 %[w], 0\n\t" \
  "s_cbranch_scc1 .Lfp%=\n\t" \
  "s_mov_b64 exec, 0xffffffff\n\t" \
  "v_lshlrev_b32 v200, 2, v203\n\t" \
  "v_mov_b32 v201, %[tag]\n\t" \
  "global_store_dword v200, v201, s[68:69] sc1\n\t" \
  "s_mov_b64 exec, -1\n\t" \
  ".Lfp%=:\n\t" \
  "s_xor_b32 s86, %[mt], 1\n\t" \
  "s_mul_i32 s68, s86, 1024\n\t" \
  "s_mul_i32 s72, %[nt], 128\n\t" \
  "s_add_u32 s68, s68, s72\n\t" \
  "s_add_u32 s68, s68, 42303488\n\t" \
  "s_mov_b64 s[72:73], %[ws]\n\t" \
  "s_add_u32 s68, s68, s72\n\t" \
  "s_addc_u32 s69, s73, 0\n\t" \
  "s_mul_i32 s48, %[mt], 163840\n\t" \
  "s_mul_i32 s72, %[nt], 20480\n\t" \
  "s_add_u32 s48, s48, s72\n\t" \
  "s_mul_i32 s72, %[w], 5120\n\t" \
  "s_add_u32 s48, s48, s72\n\t" \
  "s_add_u32 s48, s48, 36864000\n\t" \
  "s_mov_b64 s[72:73], %[ws]\n\t" \
  "s_add_u32 s48, s48, s72\n\t" \
  "s_addc_u32 s49, s73, 0\n\t" \
  "s_and_b32 s49, s49, 0xffff\n\t" \
  "s_mov_b32 s50, 0x1400\n\t" \
  "s_mov_b32 s51, 0x20000\n\t" \
  "v_mov_b32 v212, 0\n\t" \
  "v_mfma_f32_16x16x32_bf16 v[8:11], a[252:255], v[240:243], v[8:11]\n\t" \
  "buffer_load_dwordx4 v[160:163], v206, s[44:47], s76 offen sc1\n\t" \
  "v_mfma_f32_16x16x32_bf16 v[28:31], a[252:255], v[244:247], v[28:31]\n\t" \
  "v_mfma_f32_16x16x32_bf16 v[48:51], a[252:255], v[248:251], v[48:51]\n\t" \
  "buffer_load_dwordx4 v[164:167], v207, s[44:47], s76 offen sc1\n\t" \
  "v_mfma_f32_16x16x32_bf16 v[68:71], a[252:255], v[252:255], v[68:71]\n\t" \
  "s_add_u32 s76, s83, 20480\n\t" \
  "v_mfma_f32_16x16x32_bf16 v[12:15], a[160:163], v[240:243], v[12:15]\n\t" \
  "buffer_load_dwordx4 a[192:195], v204, s[44:47], s76 offen sc1\n\t" \
  "v_mfma_f32_16x16x32_bf16 v[32:35], a[160:163], v[244:247], v[32:35]\n\t" \
  "v_mfma_f32_16x16x32_bf16 v[52:55], a[160:163], v[248:251], v[52:55]\n\t" \
  "buffer_load_dwordx4 a[196:199], v205, s[44:47], s76 offen sc1\n\t" \
  "v_mfma_f32_16x16x32_bf16 v[72:75], a[160:163], v[252:255], v[72:75]\n\t" \
  "v_mfma_f32_16x16x32_bf16 v[16:19], a[164:167], v[240:243], v[16:19]\n\t" \
  "buffer_load_dwordx4 a[200:203], v206, s[44:47], s76 offen sc1\n\t" \
  "v_mfma_f32_16x16x32_bf16 v[36:39], a[164:167], v[244:247], v[36:39]\n\t" \
  "v_mfma_f32_16x16x32_bf16 v[56:59], a[164:167], v[248:251], v[56:59]\n\t" \
  "buffer_load_dwordx4 a[204:207], v207, s[44:47], s76 offen sc1\n\t" \
  "v_mfma_f32_16x16x32_bf16 v[76:79], a[164:167], v[252:255], v[76:79]\n\t" \
  "s_add_u32 s76, s83, 24576\n\t" \
  "v_mfma_f32_16x16x32_bf16 v[20:23], a[168:171], v[240:243], v[20:23]\n\t" \
  "buffer_load_dwordx4 v[168:171], v204, s[44:47], s76 offen sc1\n\t" \
  "v_mfma_f32_16x16x32_bf16 v[40:43], a[168:171], v[244:247], v[40:43]\n\t" \
  "v_mfma_f32_16x16x32_bf16 v[60:63], a[168:171], v[248:251], v[60:63]\n\t" \
  "v_mfma_f32_16x16x32_bf16 v[80:83], a[168:171], v[252:255], v[80:83]\n\t" \
  "v_mfma_f32_16x16x32_bf16 v[24:27], a[172:175], v[240:243], v[24:27]\n\t" \
  "v_mfma_f32_16x16x32_bf16 v[44:47], a[172:175], v[244:247], v[44:47]\n\t" \
  "v_mfma_f32_16x16x32_bf16 v[64:67], a[172:175], v[248:251], v[64:67]\n\t" \
  "v_mfma_f32_16x16x32_bf16 v[84:87], a[172:175], v[252:255], v[84:87]\n\t" \
  "v_mfma_f32_16x16x32_bf16 v[8:11], a[176:179], v[152:155], v[8:11]\n\t" \
  "buffer_load_dwordx4 v[172:175], v205, s[44:47], s76 offen sc1\n\t" \
  "v_mfma_f32_16x16x32_bf16 v[28:31], a[176:179], v[156:159], v[28:31]\n\t" \
  "s_waitcnt vmcnt(7)\n\t" \
  "v_mfma_f32_16x16x32_bf16 v[48:51], a[176:179], v[160:163], v[48:51]\n\t" \
  "buffer_load_dwordx4 v[176:179], v206, s[44:47], s76 offen sc1\n\t" \
  "s_waitcnt vmcnt(7)\n\t" \
  "v_mfma_f32_16x16x32_bf16 v[68:71], a[176:179], v[164:167], v[68:71]\n\t" \
  "v_mfma_f32_16x16x32_bf16 v[12:15], a[180:183], v[152:155], v[12:15]\n\t" \
  "buffer_load_dwordx4 a[208:211], v207, s[44:47], s76 offen sc1\n\t" \
  "v_mfma_f32_16x16x32_bf16 v[32:35], a[180:183], v[156:159], v[32:35]\n\t" \
  "s_add_u32 s76, s83, 28672\n\t" \
  "v_mfma_f32_16x16x32_bf16 v[52:55], a[180:183], v[160:163], v[52:55]\n\t" \
  "buffer_load_dwordx4 v[180:183], v204, s[44:47], s76 offen sc1\n\t" \
  "v_mfma_f32_16x16x32_bf16 v[72:75], a[180:183], v[164:167], v[72:75]\n\t" \
  "v_mfma_f32_16x16x32_bf16 v[16:19], a[184:187], v[152:155], v[16:19]\n\t" \
  "buffer_load_dwordx4 a[212:215], v205, s[44:47], s76 offen sc1\n\t" \
  "v_mfma_f32_16x16x32_bf16 v[36:39], a[184:187], v[156:159], v[36:39]\n\t" \
  "v_mfma_f32_16x16x32_bf16 v[56:59], a[184:187], v[160:163], v[56:59]\n\t" \
  "buffer_load_dwordx4 a[216:219], v206, s[44:47], s76 offen sc1\n\t" \
  "v_mfma_f32_16x16x32_bf16 v[76:79], a[184:187], v[164:167], v[76:79]\n\t" \
  "v_mfma_f32_16x16x32_bf16 v[20:23], a[188:191], v[152:155], v[20:23]\n\t" \
  "buffer_load_dwordx4 a[220:223], v207, s[44:47], s76 offen sc1\n\t" \
  "v_mfma_f32_16x16x32_bf16 v[40:43], a[188:191], v[156:159], v[40:43]\n\t" \
  "v_mfma_f32_16x16x32_bf16 v[60:63], a[188:191], v[160:163], v[60:63]\n\t" \
  "v_mfma_f32_16x16x32_bf16 v[80:83], a[188:191], v[164:167], v[80:83]\n\t" \
  "v_mfma_f32_16x16x32_bf16 v[24:27], a[128:131], v[152:155], v[24:27]\n\t" \
  "v_mfma_f32_16x16x32_bf16 v[44:47], a[128:131], v[156:159], v[44:47]\n\t" \
  "v_mfma_f32_16x16x32_bf16 v[64:67], a[128:131], v[160:163], v[64:67]\n\t" \
  "v_mfma_f32_16x16x32_bf16 v[84:87], a[128:131], v[164:167], v[84:87]\n\t" \
  "s_waitcnt vmcnt(11)\n\t" \
  "v_mfma_f32_16x16x32_bf16 v[8:11], a[132:135], a[192:195], v[8:11]\n\t" \
  "s_add_u32 s76, s83, 32768\n\t" \
  "buffer_load_dwordx4 a[224:227], v204, s[44:47], s76 offen sc1\n\t" \
  "s_waitcnt vmcnt(11)\n\t" \
  "v_mfma_f32_16x16x32_bf16 v[28:31], a[132:135], a[196:199], v[28:31]\n\t" \
  "s_waitcnt vmcnt(10)\n\t" \
  "v_mfma_f32_16x16x32_bf16 v[48:51], a[132:135], a[200:203], v[48:51]\n\t" \
  "buffer_load_dwordx4 v[184:187], v205, s[44:47], s76 offen sc1\n\t" \
  "s_waitcnt vmcnt(10)\n\t" \
  "v_mfma_f32_16x16x32_bf16 v[68:71], a[132:135], a[204:207], v[68:71]\n\t" \
  "v_mfma_f32_16x16x32_bf16 v[12:15], a[136:139], a[192:195], v[12:15]\n\t" \
  "buffer_load_dwordx4 v[188:191], v206, s[44:47], s76 offen sc1\n\t" \
  "v_mfma_f32_16x16x32_bf16 v[32:35], a[136:139], a[196:199], v[32:35]\n\t" \
  "v_mfma_f32_16x16x32_bf16 v[52:55], a[136:139], a[200:203], v[52:55]\n\t" \
  "buffer_load_dwordx4 v[192:195], v207, s[44:47], s76 offen sc1\n\t" \
  "v_mfma_f32_16x16x32_bf16 v[72:75], a[136:139], a[204:207], v[72:75]\n\t" \
  "s_add_u32 s76, s83, 36864\n\t" \
  "v_mfma_f32_16x16x32_bf16 v[16:19], a[140:143], a[192:195], v[16:19]\n\t" \
  "buffer_load_dwordx4 a[228:231], v204, s[44:47], s76 offen sc1\n\t" \
  "v_mfma_f32_16x16x32_bf16 v[36:39], a[140:143], a[196:199], v[36:39]\n\t" \
  "v_mfma_f32_16x16x32_bf16 v[56:59], a[140:143], a[200:203], v[56:59]\n\t" \
  "buffer_load_dwordx4 v[196:199], v205, s[44:47], s76 offen sc1\n\t" \
  "v_mfma_f32_16x16x32_bf16 v[76:79], a[140:143], a[204:207], v[76:79]\n\t" \
  "v_mfma_f32_16x16x32_bf16 v[20:23], a[144:147], a[192:195], v[20:23]\n\t" \
  "buffer_load_dwordx4 a[232:235], v206, s[44:47], s76 offen sc1\n\t" \
  "v_mfma_f32_16x16x32_bf16 v[40:43], a[144:147], a[196:199], v[40:43]\n\t" \
  "v_mfma_f32_16x16x32_bf16 v[60:63], a[144:147], a[200:203], v[60:63]\n\t" \
  "v_mfma_f32_16x16x32_bf16 v[80:83], a[144:147], a[204:207], v[80:83]\n\t" \
  "v_mfma_f32_16x16x32_bf16 v[24:27], a[148:151], a[192:195], v[24:27]\n\t" \
  "v_mfma_f32_16x16x32_bf16 v[44:47], a[148:151], a[196:199], v[44:47]\n\t" \
  "v_mfma_f32_16x16x32_bf16 v[64:67], a[148:151], a[200:203], v[64:67]\n\t" \
  "v_mfma_f32_16x16x32_bf16 v[84:87], a[148:151], a[204:207], v[84:87]\n\t" \
  "s_waitcnt vmcnt(14)\n\t" \
  "v_mfma_f32_16x16x32_bf16 v[8:11], a[152:155], v[168:171], v[8:11]\n\t" \
  "buffer_load_dwordx4 a[236:239], v207, s[44:47], s76 offen sc1\n\t" \
  "s_waitcnt vmcnt(14)\n\t" \
  "v_mfma_f32_16x16x32_bf16 v[28:31], a[152:155], v[172:175], v[28:31]\n\t" \
  "s_add_u32 s76, s83, 40960\n\t" \
  "s_waitcnt vmcnt(13)\n\t" \
  "v_mfma_f32_16x16x32_bf16 v[48:51], a[152:155], v[176:179], v[48:51]\n\t" \
  "buffer_load_dwordx4 a[240:243], v204, s[44:47], s76 offen sc1\n\t" \
  "s_waitcnt vmcnt(13)\n\t" \
  "v_mfma_f32_16x16x32_bf16 v[68:71], a[152:155], a[208:211], v[68:71]\n\t" \
  "v_mfma_f32_16x16x32_bf16 v[12:15], a[156:159], v[168:171], v[12:15]\n\t" \
  "buffer_load_dwordx4 a[244:247], v205, s[44:47], s76 offen sc1\n\t" \
  "v_mfma_f32_16x16x32_bf16 v[32:35], a[156:159], v[172:175], v[32:35]\n\t" \
  "v_mfma_f32_16x16x32_bf16 v[52:55], a[156:159], v[176:179], v[52:55]\n\t" \
  "buffer_load_dwordx4 v[224:227], v206, s[44:47], s76 offen sc1\n\t" \
  "v_mfma_f32_16x16x32_bf16 v[72:75], a[156:159], a[208:211], v[72:75]\n\t" \
  "v_mfma_f32_16x16x32_bf16 v[16:19], v[88:91], v[168:171], v[16:19]\n\t" \
  "buffer_load_dwordx4 v[228:231], v207, s[44:47], s76 offen sc1\n\t" \
  "v_mfma_f32_16x16x32_bf16 v[36:39], v[88:91], v[172:175], v[36:39]\n\t" \
  "s_add_u32 s76, s83, 45056\n\t" \
  "v_mfma_f32_16x16x32_bf16 v[56:59], v[88:91], v[176:179], v[56:59]\n\t" \
  "buffer_load_dwordx4 v[232:235], v204, s[44:47], s76 offen sc1\n\t" \
  "v_mfma_f32_16x16x32_bf16 v[76:79], v[88:91], a[208:211], v[76:79]\n\t" \
  "v_mfma_f32_16x16x32_bf16 v[20:23], v[92:95], v[168:171], v[20:23]\n\t" \
  "buffer_load_dwordx4 a[248:251], v205, s[44:47], s76 offen sc1\n\t" \
  "v_mfma_f32_16x16x32_bf16 v[40:43], v[92:95], v[172:175], v[40:43]\n\t" \
  "v_mfma_f32_16x16x32_bf16 v[60:63], v[92:95], v[176:179], v[60:63]\n\t" \
  "v_mfma_f32_16x16x32_bf16 v[80:83], v[92:95], a[208:211], v[80:83]\n\t" \
  "v_mfma_f32_16x16x32_bf16 v[24:27], v[96:99], v[168:171], v[24:27]\n\t" \
  "v_mfma_f32_16x16x32_bf16 v[44:47], v[96:99], v[172:175], v[44:47]\n\t" \
  "v_mfma_f32_16x16x32_bf16 v[64:67], v[96:99], v[176:179], v[64:67]\n\t" \
  "v_mfma_f32_16x16x32_bf16 v[84:87], v[96:99], a[208:211], v[84:87]\n\t" \
  "s_waitcnt vmcnt(17)\n\t" \
  "v_mfma_f32_16x16x32_bf16 v[8:11], v[100:103], v[180:183], v[8:11]\n\t" \
  "global_load_dword v213, v212, s[68:69] sc1\n\t" \
  "s_waitcnt vmcnt(17)\n\t" \
  "v_mfma_f32_16x16x32_bf16 v[28:31], v[100:103], a[212:215], v[28:31]\n\t" \
  "s_waitcnt vmcnt(16)\n\t" \
  "v_mfma_f32_16x16x32_bf16 v[48:51], v[100:103], a[216:219], v[48:51]\n\t" \
  "buffer_load_dwordx4 v[236:239], v206, s[44:47], s76 offen sc1\n\t" \
  "s_waitcnt vmcnt(16)\n\t" \
  "v_mfma_f32_16x16x32_bf16 v[68:71], v[100:103], a[220:223], v[68:71]\n\t" \
  "v_mfma_f32_16x16x32_bf16 v[12:15], v[104:107], v[180:183], v[12:15]\n\t" \
  "buffer_load_dwordx4 a[252:255], v207, s[44:47], s76 offen sc1\n\t" \
  "v_mfma_f32_16x16x32_bf16 v[32:35], v[104:107], a[212:215], v[32:35]\n\t" \
  "s_add_u32 s76, s83, 49152\n\t" \
  "v_mfma_f32_16x16x32_bf16 v[52:55], v[104:107], a[216:219], v[52:55]\n\t" \
  "buffer_load_dwordx4 a[160:163], v204, s[44:47], s76 offen sc1\n\t" \
  "v_mfma_f32_16x16x32_bf16 v[72:75], v[104:107], a[220:223], v[72:75]\n\t" \
  "v_mfma_f32_16x16x32_bf16 v[16:19], v[108:111], v[180:183], v[16:19]\n\t" \
  "buffer_load_dwordx4 a[164:167], v205, s[44:47], s76 offen sc1\n\t" \
  "v_mfma_f32_16x16x32_bf16 v[36:39], v[108:111], a[212:215], v[36:39]\n\t" \
  "v_mfma_f32_16x16x32_bf16 v[56:59], v[108:111], a[216:219], v[56:59]\n\t" \
  "buffer_load_dwordx4 a[168:171], v206, s[44:47], s76 offen sc1\n\t" \
  "v_mfma_f32_16x16x32_bf16 v[76:79], v[108:111], a[220:223], v[76:79]\n\t" \
  "v_mfma_f32_16x16x32_bf16 v[20:23], v[112:115], v[180:183], v[20:23]\n\t" \
  "buffer_load_dwordx4 v[240:243], v207, s[44:47], s76 offen sc1\n\t" \
  "v_mfma_f32_16x16x32_bf16 v[40:43], v[112:115], a[212:215], v[40:43]\n\t" \
  "s_add_u32 s76, s83, 53248\n\t" \
  "v_mfma_f32_16x16x32_bf16 v[60:63], v[112:115], a[216:219], v[60:63]\n\t" \
  "buffer_load_dwordx4 v[244:247], v204, s[44:47], s76 offen sc1\n\t" \
  "v_mfma_f32_16x16x32_bf16 v[80:83], v[112:115], a[220:223], v[80:83]\n\t" \
  "v_mfma_f32_16x16x32_bf16 v[24:27], v[116:119], v[180:183], v[24:27]\n\t" \
  "v_mfma_f32_16x16x32_bf16 v[44:47], v[116:119], a[212:215], v[44:47]\n\t" \
  "v_mfma_f32_16x16x32_bf16 v[64:67], v[116:119], a[216:219], v[64:67]\n\t" \
  "v_mfma_f32_16x16x32_bf16 v[84:87], v[116:119], a[220:223], v[84:87]\n\t" \
  "s_waitcnt vmcnt(21)\n\t" \
  "v_mfma_f32_16x16x32_bf16 v[8:11], v[120:123], a[224:227], v[8:11]\n\t" \
  "buffer_load_dwordx4 v[248:251], v205, s[44:47], s76 offen sc1\n\t" \
  "s_waitcnt vmcnt(21)\n\t" \
  "v_mfma_f32_16x16x32_bf16 v[28:31], v[120:123], v[184:187], v[28:31]\n\t" \
  "s_waitcnt vmcnt(20)\n\t" \
  "v_mfma_f32_16x16x32_bf16 v[48:51], v[120:123], v[188:191], v[48:51]\n\t" \
  "buffer_load_dwordx4 a[172:175], v206, s[44:47], s76 offen sc1\n\t" \
  "s_waitcnt vmcnt(20)\n\t" \
  "v_mfma_f32_16x16x32_bf16 v[68:71], v[120:123], v[192:195], v[68:71]\n\t" \
  "v_mfma_f32_16x16x32_bf16 v[12:15], v[124:127], a[224:227], v[12:15]\n\t" \
  "buffer_load_dwordx4 v[252:255], v207, s[44:47], s76 offen sc1\n\t" \
  "v_mfma_f32_16x16x32_bf16 v[32:35], v[124:127], v[184:187], v[32:35]\n\t" \
  "s_add_u32 s76, s83, 57344\n\t" \
  "v_mfma_f32_16x16x32_bf16 v[52:55], v[124:127], v[188:191], v[52:55]\n\t" \
  "buffer_load_dwordx4 a[176:179], v204, s[44:47], s76 offen sc1\n\t" \
  "v_mfma_f32_16x16x32_bf16 v[72:75], v[124:127], v[192:195], v[72:75]\n\t" \
  "v_mfma_f32_16x16x32_bf16 v[16:19], v[128:131], a[224:227], v[16:19]\n\t" \
  "buffer_load_dwordx4 a[180:183], v205, s[44:47], s76 offen sc1\n\t" \
  "v_mfma_f32_16x16x32_bf16 v[36:39], v[128:131], v[184:187], v[36:39]\n\t" \
  "v_mfma_f32_16x16x32_bf16 v[56:59], v[128:131], v[188:191], v[56:59]\n\t" \
  "buffer_load_dwordx4 a[184:187], v206, s[44:47], s76 offen sc1\n\t" \
  "v_mfma_f32_16x16x32_bf16 v[76:79], v[128:131], v[192:195], v[76:79]\n\t" \
  "v_mfma_f32_16x16x32_bf16 v[20:23], v[132:135], a[224:227], v[20:23]\n\t" \
  "buffer_load_dwordx4 a[188:191], v207, s[44:47], s76 offen sc1\n\t" \
  "v_mfma_f32_16x16x32_bf16 v[40:43], v[132:135], v[184:187], v[40:43]\n\t" \
  "v_mfma_f32_16x16x32_bf16 v[60:63], v[132:135], v[188:191], v[60:63]\n\t" \
  "v_mfma_f32_16x16x32_bf16 v[80:83], v[132:135], v[192:195], v[80:83]\n\t" \
  "v_mfma_f32_16x16x32_bf16 v[24:27], v[136:139], a[224:227], v[24:27]\n\t" \
  "v_mfma_f32_16x16x32_bf16 v[44:47], v[136:139], v[184:187], v[44:47]\n\t" \
  "v_mfma_f32_16x16x32_bf16 v[64:67], v[136:139], v[188:191], v[64:67]\n\t" \
  "v_mfma_f32_16x16x32_bf16 v[84:87], v[136:139], v[192:195], v[84:87]\n\t" \
  "s_waitcnt vmcnt(24)\n\t" \
  "v_mfma_f32_16x16x32_bf16 v[8:11], v[140:143], a[228:231], v[8:11]\n\t" \
  "s_add_u32 s76, s83, 61440\n\t" \
  "buffer_load_dwordx4 v[152:155], v204, s[44:47], s76 offen sc1\n\t" \
  "s_waitcnt vmcnt(24)\n\t" \
  "v_mfma_f32_16x16x32_bf16 v[28:31], v[140:143], v[196:199], v[28:31]\n\t" \
  "s_waitcnt vmcnt(23)\n\t" \
  "v_mfma_f32_16x16x32_bf16 v[48:51], v[140:143], a[232:235], v[48:51]\n\t" \
  "buffer_load_dwordx4 v[156:159], v205, s[44:47], s76 offen sc1\n\t" \
  "s_waitcnt vmcnt(23)\n\t" \
  "v_mfma_f32_16x16x32_bf16 v[68:71], v[140:143], a[236:239], v[68:71]\n\t" \
  "v_mfma_f32_16x16x32_bf16 v[12:15], v[144:147], a[228:231], v[12:15]\n\t" \
  "buffer_load_dwordx4 v[160:163], v206, s[44:47], s76 offen sc1\n\t" \
  "v_mfma_f32_16x16x32_bf16 v[32:35], v[144:147], v[196:199], v[32:35]\n\t" \
  "v_mfma_f32_16x16x32_bf16 v[52:55], v[144:147], a[232:235], v[52:55]\n\t" \
  "buffer_load_dwordx4 a[128:131], v207, s[44:47], s76 offen sc1\n\t" \
  "v_mfma_f32_16x16x32_bf16 v[72:75], v[144:147], a[236:239], v[72:75]\n\t" \
  "v_mfma_f32_16x16x32_bf16 v[16:19], v[148:151], a[228:231], v[16:19]\n\t" \
  "v_mfma_f32_16x16x32_bf16 v[36:39], v[148:151], v[196:199], v[36:39]\n\t" \
  "v_mfma_f32_16x16x32_bf16 v[56:59], v[148:151], a[232:235], v[56:59]\n\t" \
  "v_mfma_f32_16x16x32_bf16 v[76:79], v[148:151], a[236:239], v[76:79]\n\t" \
  "v_mfma_f32_16x16x32_bf16 v[20:23], a[0:3], a[228:231], v[20:23]\n\t" \
  "v_mfma_f32_16x16x32_bf16 v[40:43], a[0:3], v[196:199], v[40:43]\n\t" \
  "v_mfma_f32_16x16x32_bf16 v[60:63], a[0:3], a[232:235], v[60:63]\n\t" \
  "v_mfma_f32_16x16x32_bf16 v[80:83], a[0:3], a[236:239], v[80:83]\n\t" \
  "v_mfma_f32_16x16x32_bf16 v[24:27], a[4:7], a[228:231], v[24:27]\n\t" \
  "v_mfma_f32_16x16x32_bf16 v[44:47], a[4:7], v[196:199], v[44:47]\n\t" \
  "v_mfma_f32_16x16x32_bf16 v[64:67], a[4:7], a[232:235], v[64:67]\n\t" \
  "v_mfma_f32_16x16x32_bf16 v[84:87], a[4:7], a[236:239], v[84:87]\n\t" \
  "s_waitcnt vmcnt(18)\n\t" \
  "v_cmp_ne_u32 vcc, %[tag], v213\n\t" \
  "s_cbranch_vccnz .Lppspin%=\n\t" \
  ".Lppback%=:\n\t" \
  "v_mfma_f32_16x16x32_bf16 v[8:11], a[8:11], a[240:243], v[8:11]\n\t" \
  "buffer_load_dwordx4 v[164:167], v202, s[48:51], 0 offen offset:0 sc1\n\t" \
  "v_mfma_f32_16x16x32_bf16 v[28:31], a[8:11], a[244:247], v[28:31]\n\t" \
  "v_mfma_f32_16x16x32_bf16 v[48:51], a[8:11], v[224:227], v[48:51]\n\t" \
  "buffer_load_dwordx4 v[168:171], v202, s[48:51], 0 offen offset:1024 sc1\n\t" \
  "v_mfma_f32_16x16x32_bf16 v[68:71], a[8:11], v[228:231], v[68:71]\n\t" \
  "v_mfma_f32_16x16x32_bf16 v[12:15], a[12:15], a[240:243], v[12:15]\n\t" \
  "buffer_load_dwordx4 v[172:175], v202, s[48:51], 0 offen offset:2048 sc1\n\t" \
  "v_mfma_f32_16x16x32_bf16 v[32:35], a[12:15], a[244:247], v[32:35]\n\t" \
  "v_mfma_f32_16x16x32_bf16 v[52:55], a[12:15], v[224:227], v[52:55]\n\t" \
  "buffer_load_dwordx4 v[176:179], v202, s[48:51], 0 offen offset:3072 sc1\n\t" \
  "v_mfma_f32_16x16x32_bf16 v[72:75], a[12:15], v[228:231], v[72:75]\n\t" \
  "v_mfma_f32_16x16x32_bf16 v[16:19], a[16:19], a[240:243], v[16:19]\n\t" \
  "buffer_load_dwordx4 v[180:183], v202, s[48:51], s77 offen offset:0 sc1\n\t" \
  "v_mfma_f32_16x16x32_bf16 v[36:39], a[16:19], a[244:247], v[36:39]\n\t" \
  "v_mfma_f32_16x16x32_bf16 v[56:59], a[16:19], v[224:227], v[56:59]\n\t" \
  "v_mfma_f32_16x16x32_bf16 v[76:79], a[16:19], v[228:231], v[76:79]\n\t" \
  "v_mfma_f32_16x16x32_bf16 v[20:23], a[20:23], a[240:243], v[20:23]\n\t" \
  "v_mfma_f32_16x16x32_bf16 v[40:43], a[20:23], a[244:247], v[40:43]\n\t" \
  "v_mfma_f32_16x16x32_bf16 v[60:63], a[20:23], v[224:227], v[60:63]\n\t" \
  "v_mfma_f32_16x16x32_bf16 v[80:83], a[20:23], v[228:231], v[80:83]\n\t" \
  "v_mfma_f32_16x16x32_bf16 v[24:27], a[24:27], a[240:243], v[24:27]\n\t" \
  "v_mfma_f32_16x16x32_bf16 v[44:47], a[24:27], a[244:247], v[44:47]\n\t" \
  "v_mfma_f32_16x16x32_bf16 v[64:67], a[24:27], v[224:227], v[64:67]\n\t" \
  "v_mfma_f32_16x16x32_bf16 v[84:87], a[24:27], v[228:231], v[84:87]\n\t" \
  "s_mul_i32 s40, %[mt], 81920\n\t" \
  "s_mul_i32 s72, %[w], 1024\n\t" \
  "s_add_u32 s40, s40, s72\n\t" \
  "s_add_u32 s40, s40, 2932736\n\t" \
  "s_mov_b64 s[72:73], %[ws]\n\t" \
  "s_add_u32 s40, s40, s72\n\t" \
  "s_addc_u32 s41, s73, 0\n\t" \
  "s_and_b32 s41, s41, 0xffff\n\t" \
  "s_mov_b32 s42, 0x14000\n\t" \
  "s_mov_b32 s43, 0x20000\n\t" \
  "s_mul_i32 s85, %[nt], 5\n\t" \
  "s_lshr_b32 s85, s85, 1\n\t" \
  "s_lshl_b32 s85, s85, 12\n\t" \
  "s_add_u32 s86, s85, 4096\n\t" \
  "s_add_u32 s87, s85, 8192\n\t" \
  "s_mul_i32 s68, %[mt], 4096\n\t" \
  "s_mul_i32 s72, %[w], 128\n\t" \
  "s_add_u32 s68, s68, s72\n\t" \
  "s_mul_i32 s72, %[nt], 512\n\t" \
  "s_add_u32 s68, s68, s72\n\t" \
  "s_add_u32 s68, s68, 180224\n\t" \
  "s_mov_b64 s[72:73], %[ws]\n\t" \
  "s_add_u32 s68, s68, s72\n\t" \
  "s_addc_u32 s69, s73, 0\n\t" \
  "v_lshlrev_b32 v223, 3, v203\n\t" \
  "v_mfma_f32_16x16x32_bf16 v[8:11], a[28:31], v[232:235], v[8:11]\n\t" \
  "v_mfma_f32_16x16x32_bf16 v[28:31], a[28:31], a[248:251], v[28:31]\n\t" \
  "s_waitcnt vmcnt(22)\n\t" \
  "v_mfma_f32_16x16x32_bf16 v[48:51], a[28:31], v[236:239], v[48:51]\n\t" \
  "s_waitcnt vmcnt(21)\n\t" \
  "v_mfma_f32_16x16x32_bf16 v[68:71], a[28:31], a[252:255], v[68:71]\n\t" \
  "v_mfma_f32_16x16x32_bf16 v[12:15], a[32:35], v[232:235], v[12:15]\n\t" \
  "v_mfma_f32_16x16x32_bf16 v[32:35], a[32:35], a[248:251], v[32:35]\n\t" \
  "v_mfma_f32_16x16x32_bf16 v[52:55], a[32:35], v[236:239], v[52:55]\n\t" \
  "v_mfma_f32_16x16x32_bf16 v[72:75], a[32:35], a[252:255], v[72:75]\n\t" \
  "v_mfma_f32_16x16x32_bf16 v[16:19], a[36:39], v[232:235], v[16:19]\n\t" \
  "v_mfma_f32_16x16x32_bf16 v[36:39], a[36:39], a[248:251], v[36:39]\n\t" \
  "v_mfma_f32_16x16x32_bf16 v[56:59], a[36:39], v[236:239], v[56:59]\n\t" \
  "v_mfma_f32_16x16x32_bf16 v[76:79], a[36:39], a[252:255], v[76:79]\n\t" \
  "v_mfma_f32_16x16x32_bf16 v[20:23], a[40:43], v[232:235], v[20:23]\n\t" \
  "v_mfma_f32_16x16x32_bf16 v[40:43], a[40:43], a[248:251], v[40:43]\n\t" \
  "v_mfma_f32_16x16x32_bf16 v[60:63], a[40:43], v[236:239], v[60:63]\n\t" \
  "v_mfma_f32_16x16x32_bf16 v[80:83], a[40:43], a[252:255], v[80:83]\n\t" \
  "v_mfma_f32_16x16x32_bf16 v[24:27], a[44:47], v[232:235], v[24:27]\n\t" \
  "v_mfma_f32_16x16x32_bf16 v[44:47], a[44:47], a[248:251], v[44:47]\n\t" \
  "v_mfma_f32_16x16x32_bf16 v[64:67], a[44:47], v[236:239], v[64:67]\n\t" \
  "v_mfma_f32_16x16x32_bf16 v[84:87], a[44:47], a[252:255], v[84:87]\n\t" \
  "s_waitcnt vmcnt(20)\n\t" \
  "v_mfma_f32_16x16x32_bf16 v[8:11], a[48:51], a[160:163], v[8:11]\n\t" \
  "s_add_u32 m0, s80, 32768\n\t" \
  "s_mov_b32 s74, 442368\n\t" \
  "ds_read_b64 v[88:89], v221 offset:0\n\t" \
  "s_waitcnt vmcnt(19)\n\t" \
  "v_mfma_f32_16x16x32_bf16 v[28:31], a[48:51], a[164:167], v[28:31]\n\t" \
  "buffer_load_dwordx4 v216, s[56:59], s74 offen offset:0 lds\n\t" \
  "s_waitcnt vmcnt(19)\n\t" \
  "v_mfma_f32_16x16x32_bf16 v[48:51], a[48:51], a[168:171], v[48:51]\n\t" \
  "ds_read_b64 v[90:91], v221 offset:32\n\t" \
  "s_waitcnt vmcnt(18)\n\t" \
  "v_mfma_f32_16x16x32_bf16 v[68:71], a[48:51], v[240:243], v[68:71]\n\t" \
  "ds_read_b64 v[92:93], v221 offset:64\n\t" \
  "v_mfma_f32_16x16x32_bf16 v[12:15], a[52:55], a[160:163], v[12:15]\n\t" \
  "buffer_load_dwordx4 v217, s[60:63], s74 offen offset:1024 lds\n\t" \
  "v_mfma_f32_16x16x32_bf16 v[32:35], a[52:55], a[164:167], v[32:35]\n\t" \
  "ds_read_b64 v[94:95], v221 offset:96\n\t" \
  "v_mfma_f32_16x16x32_bf16 v[52:55], a[52:55], a[168:171], v[52:55]\n\t" \
  "ds_read_b64 v[96:97], v221 offset:128\n\t" \
  "v_mfma_f32_16x16x32_bf16 v[72:75], a[52:55], v[240:243], v[72:75]\n\t" \
  "buffer_load_dwordx4 v218, s[56:59], s74 offen offset:2048 lds\n\t" \
  "v_mfma_f32_16x16x32_bf16 v[16:19], a[56:59], a[160:163], v[16:19]\n\t" \
  "ds_read_b128 v[100:103], v222 offset:0\n\t" \
  "v_mfma_f32_16x16x32_bf16 v[36:39], a[56:59], a[164:167], v[36:39]\n\t" \
  "ds_read_b128 v[104:107], v222 offset:64\n\t" \
  "v_mfma_f32_16x16x32_bf16 v[56:59], a[56:59], a[168:171], v[56:59]\n\t" \
  "buffer_load_dwordx4 v219, s[60:63], s74 offen offset:3072 lds\n\t" \
  "v_mfma_f32_16x16x32_bf16 v[76:79], a[56:59], v[240:243], v[76:79]\n\t" \
  "ds_read_b128 v[108:111], v222 offset:128\n\t" \
  "v_mfma_f32_16x16x32_bf16 v[20:23], a[60:63], a[160:163], v[20:23]\n\t" \
  "s_add_u32 m0, s80, 49152\n\t" \
  "s_mov_b32 s74, 663552\n\t" \
  "v_mfma_f32_16x16x32_bf16 v[40:43], a[60:63], a[164:167], v[40:43]\n\t" \
  "buffer_load_dwordx4 v216, s[56:59], s74 offen offset:0 lds\n\t" \
  "v_mfma_f32_16x16x32_bf16 v[60:63], a[60:63], a[168:171], v[60:63]\n\t" \
  "v_mfma_f32_16x16x32_bf16 v[80:83], a[60:63], v[240:243], v[80:83]\n\t" \
  "v_mfma_f32_16x16x32_bf16 v[24:27], a[64:67], a[160:163], v[24:27]\n\t" \
  "buffer_load_dwordx4 v217, s[60:63], s74 offen offset:1024 lds\n\t" \
  "v_mfma_f32_16x16x32_bf16 v[44:47], a[64:67], a[164:167], v[44:47]\n\t" \
  "v_mfma_f32_16x16x32_bf16 v[64:67], a[64:67], a[168:171], v[64:67]\n\t" \
  "v_mfma_f32_16x16x32_bf16 v[84:87], a[64:67], v[240:243], v[84:87]\n\t" \
  "buffer_load_dwordx4 v218, s[56:59], s74 offen offset:2048 lds\n\t" \
  "s_waitcnt vmcnt(23)\n\t" \
  "v_mfma_f32_16x16x32_bf16 v[8:11], a[68:71], v[244:247], v[8:11]\n\t" \
  "ds_read_b128 v[112:115], v222 offset:192\n\t" \
  "s_waitcnt vmcnt(22)\n\t" \
  "v_mfma_f32_16x16x32_bf16 v[28:31], a[68:71], v[248:251], v[28:31]\n\t" \
  "buffer_load_dwordx4 v219, s[60:63], s74 offen offset:3072 lds\n\t" \
  "s_waitcnt vmcnt(22)\n\t" \
  "v_mfma_f32_16x16x32_bf16 v[48:51], a[68:71], a[172:175], v[48:51]\n\t" \
  "ds_read_b128 v[116:119], v222 offset:256\n\t" \
  "s_waitcnt vmcnt(21)\n\t" \
  "v_mfma_f32_16x16x32_bf16 v[68:71], a[68:71], v[252:255], v[68:71]\n\t" \
  "ds_read_b128 v[120:123], v222 offset:320\n\t" \
  "v_mfma_f32_16x16x32_bf16 v[12:15], a[72:75], v[244:247], v[12:15]\n\t" \
  "ds_read_b128 v[124:127], v222 offset:384\n\t" \
  "v_mfma_f32_16x16x32_bf16 v[32:35], a[72:75], v[248:251], v[32:35]\n\t" \
  "ds_read_b128 v[128:131], v222 offset:448\n\t" \
  "v_mfma_f32_16x16x32_bf16 v[52:55], a[72:75], a[172:175], v[52:55]\n\t" \
  "ds_read_b128 v[132:135], v222 offset:512\n\t" \
  "v_mfma_f32_16x16x32_bf16 v[72:75], a[72:75], v[252:255], v[72:75]\n\t" \
  "ds_read_b128 v[136:139], v222 offset:576\n\t" \
  "v_mfma_f32_16x16x32_bf16 v[16:19], a[76:79], v[244:247], v[16:19]\n\t" \
  "v_mfma_f32_16x16x32_bf16 v[36:39], a[76:79], v[248:251], v[36:39]\n\t" \
  "v_mfma_f32_16x16x32_bf16 v[56:59], a[76:79], a[172:175], v[56:59]\n\t" \
  "v_mfma_f32_16x16x32_bf16 v[76:79], a[76:79], v[252:255], v[76:79]\n\t" \
  "v_mfma_f32_16x16x32_bf16 v[20:23], a[80:83], v[244:247], v[20:23]\n\t" \
  "v_mfma_f32_16x16x32_bf16 v[40:43], a[80:83], v[248:251], v[40:43]\n\t" \
  "v_mfma_f32_16x16x32_bf16 v[60:63], a[80:83], a[172:175], v[60:63]\n\t" \
  "v_mfma_f32_16x16x32_bf16 v[80:83], a[80:83], v[252:255], v[80:83]\n\t" \
  "v_mfma_f32_16x16x32_bf16 v[24:27], a[84:87], v[244:247], v[24:27]\n\t" \
  "v_mfma_f32_16x16x32_bf16 v[44:47], a[84:87], v[248:251], v[44:47]\n\t" \
  "v_mfma_f32_16x16x32_bf16 v[64:67], a[84:87], a[172:175], v[64:67]\n\t" \
  "v_mfma_f32_16x16x32_bf16 v[84:87], a[84:87], v[252:255], v[84:87]\n\t" \
  "s_waitcnt vmcnt(20)\n\t" \
  "v_mfma_f32_16x16x32_bf16 v[8:11], a[88:91], a[176:179], v[8:11]\n\t" \
  "s_waitcnt vmcnt(19)\n\t" \
  "v_mfma_f32_16x16x32_bf16 v[28:31], a[88:91], a[180:183], v[28:31]\n\t" \
  "s_waitcnt vmcnt(18)\n\t" \
  "v_mfma_f32_16x16x32_bf16 v[48:51], a[88:91], a[184:187], v[48:51]\n\t" \
  "s_waitcnt vmcnt(17)\n\t" \
  "v_mfma_f32_16x16x32_bf16 v[68:71], a[88:91], a[188:191], v[68:71]\n\t" \
  "v_mfma_f32_16x16x32_bf16 v[12:15], a[92:95], a[176:179], v[12:15]\n\t" \
  "v_mfma_f32_16x16x32_bf16 v[32:35], a[92:95], a[180:183], v[32:35]\n\t" \
  "v_mfma_f32_16x16x32_bf16 v[52:55], a[92:95], a[184:187], v[52:55]\n\t" \
  "v_mfma_f32_16x16x32_bf16 v[72:75], a[92:95], a[188:191], v[72:75]\n\t" \
  "v_mfma_f32_16x16x32_bf16 v[16:19], a[96:99], a[176:179], v[16:19]\n\t" \
  "v_mfma_f32_16x16x32_bf16 v[36:39], a[96:99], a[180:183], v[36:39]\n\t" \
  "v_mfma_f32_16x16x32_bf16 v[56:59], a[96:99], a[184:187], v[56:59]\n\t" \
  "v_mfma_f32_16x16x32_bf16 v[76:79], a[96:99], a[188:191], v[76:79]\n\t" \
  "v_mfma_f32_16x16x32_bf16 v[20:23], a[100:103], a[176:179], v[20:23]\n\t" \
  "v_mfma_f32_16x16x32_bf16 v[40:43], a[100:103], a[180:183], v[40:43]\n\t" \
  "v_mfma_f32_16x16x32_bf16 v[60:63], a[100:103], a[184:187], v[60:63]\n\t" \
  "v_mfma_f32_16x16x32_bf16 v[80:83], a[100:103], a[188:191], v[80:83]\n\t" \
  "v_mfma_f32_16x16x32_bf16 v[24:27], a[104:107], a[176:179], v[24:27]\n\t" \
  "v_mfma_f32_16x16x32_bf16 v[44:47], a[104:107], a[180:183], v[44:47]\n\t" \
  "v_mfma_f32_16x16x32_bf16 v[64:67], a[104:107], a[184:187], v[64:67]\n\t" \
  "v_mfma_f32_16x16x32_bf16 v[84:87], a[104:107], a[188:191], v[84:87]\n\t" \
  "s_waitcnt vmcnt(16)\n\t" \
  "v_mfma_f32_16x16x32_bf16 v[8:11], a[108:111], v[152:155], v[8:11]\n\t" \
  "s_waitcnt vmcnt(15)\n\t" \
  "v_mfma_f32_16x16x32_bf16 v[28:31], a[108:111], v[156:159], v[28:31]\n\t" \
  "s_waitcnt vmcnt(14)\n\t" \
  "v_mfma_f32_16x16x32_bf16 v[48:51], a[108:111], v[160:163], v[48:51]\n\t" \
  "s_waitcnt vmcnt(13)\n\t" \
  "v_mfma_f32_16x16x32_bf16 v[68:71], a[108:111], a[128:131], v[68:71]\n\t" \
  "v_mfma_f32_16x16x32_bf16 v[12:15], a[112:115], v[152:155], v[12:15]\n\t" \
  "v_mfma_f32_16x16x32_bf16 v[32:35], a[112:115], v[156:159], v[32:35]\n\t" \
  "v_mfma_f32_16x16x32_bf16 v[52:55], a[112:115], v[160:163], v[52:55]\n\t" \
  "v_mfma_f32_16x16x32_bf16 v[72:75], a[112:115], a[128:131], v[72:75]\n\t" \
  "v_mfma_f32_16x16x32_bf16 v[16:19], a[116:119], v[152:155], v[16:19]\n\t" \
  "v_mfma_f32_16x16x32_bf16 v[36:39], a[116:119], v[156:159], v[36:39]\n\t" \
  "ds_write_b128 v208, v[28:31] offset:0\n\t" \
  "v_mfma_f32_16x16x32_bf16 v[56:59], a[116:119], v[160:163], v[56:59]\n\t" \
  "ds_write_b128 v209, v[48:51] offset:0\n\t" \
  "v_mfma_f32_16x16x32_bf16 v[76:79], a[116:119], a[128:131], v[76:79]\n\t" \
  "ds_write_b128 v210, v[68:71] offset:0\n\t" \
  "v_mfma_f32_16x16x32_bf16 v[20:23], a[120:123], v[152:155], v[20:23]\n\t" \
  "v_mfma_f32_16x16x32_bf16 v[40:43], a[120:123], v[156:159], v[40:43]\n\t" \
  "ds_write_b128 v208, v[32:35] offset:1024\n\t" \
  "v_mfma_f32_16x16x32_bf16 v[60:63], a[120:123], v[160:163], v[60:63]\n\t" \
  "ds_write_b128 v209, v[52:55] offset:1024\n\t" \
  "v_mfma_f32_16x16x32_bf16 v[80:83], a[120:123], a[128:131], v[80:83]\n\t" \
  "ds_write_b128 v210, v[72:75] offset:1024\n\t" \
  "v_mfma_f32_16x16x32_bf16 v[24:27], a[124:127], v[152:155], v[24:27]\n\t" \
  "v_mfma_f32_16x16x32_bf16 v[44:47], a[124:127], v[156:159], v[44:47]\n\t" \
  "ds_write_b128 v208, v[36:39] offset:2048\n\t" \
  "v_mfma_f32_16x16x32_bf16 v[64:67], a[124:127], v[160:163], v[64:67]\n\t" \
  "ds_write_b128 v209, v[56:59] offset:2048\n\t" \
  "v_mfma_f32_16x16x32_bf16 v[84:87], a[124:127], a[128:131], v[84:87]\n\t" \
  "ds_write_b128 v210, v[76:79] offset:2048\n\t" \
  ".Lbepi%=:\n\t" \
  "ds_write_b128 v208, v[40:43] offset:3072\n\t" \
  "ds_write_b128 v209, v[60:63] offset:3072\n\t" \
  "ds_write_b128 v210, v[80:83] offset:3072\n\t" \
  "ds_write_b128 v208, v[44:47] offset:4096\n\t" \
  "s_nop 0\n\t" \
  "ds_write_b128 v209, v[64:67] offset:4096\n\t" \
  "s_nop 0\n\t" \
  "ds_write_b128 v210, v[84:87] offset:4096\n\t" \
  "s_waitcnt vmcnt(12)\n\t" \
  "v_pk_add_f32 v[8:9], v[8:9], v[164:165]\n\t" \
  "v_pk_add_f32 v[10:11], v[10:11], v[166:167]\n\t" \
  "s_waitcnt vmcnt(11)\n\t" \
  "v_pk_add_f32 v[12:13], v[12:13], v[168:169]\n\t" \
  "v_pk_add_f32 v[14:15], v[14:15], v[170:171]\n\t" \
  "s_waitcnt vmcnt(10)\n\t" \
  "v_pk_add_f32 v[16:17], v[16:17], v[172:173]\n\t" \
  "v_pk_add_f32 v[18:19], v[18:19], v[174:175]\n\t" \
  "s_waitcnt vmcnt(9)\n\t" \
  "v_pk_add_f32 v[20:21], v[20:21], v[176:177]\n\t" \
  "v_pk_add_f32 v[22:23], v[22:23], v[178:179]\n\t" \
  "s_waitcnt vmcnt(8)\n\t" \
  "v_pk_add_f32 v[24:25], v[24:25], v[180:181]\n\t" \
  "v_pk_add_f32 v[26:27], v[26:27], v[182:183]\n\t" \
  "s_waitcnt lgkmcnt(8)\n\t" \
  "v_lshlrev_b32 v152, 16, v88\n\t" \
  "v_and_b32 v153, 0xffff0000, v88\n\t" \
  "v_lshlrev_b32 v154, 16, v89\n\t" \
  "v_and_b32 v155, 0xffff0000, v89\n\t" \
  "v_lshlrev_b32 v156, 16, v90\n\t" \
  "v_and_b32 v157, 0xffff0000, v90\n\t" \
  "v_lshlrev_b32 v158, 16, v91\n\t" \
  "v_and_b32 v159, 0xffff0000, v91\n\t" \
  "v_lshlrev_b32 v160, 16, v92\n\t" \
  "v_and_b32 v161, 0xffff0000, v92\n\t" \
  "v_lshlrev_b32 v162, 16, v93\n\t" \
  "v_and_b32 v163, 0xffff0000, v93\n\t" \
  "v_lshlrev_b32 v164, 16, v94\n\t" \
  "v_and_b32 v165, 0xffff0000, v94\n\t" \
  "v_lshlrev_b32 v166, 16, v95\n\t" \
  "v_and_b32 v167, 0xffff0000, v95\n\t" \
  "v_lshlrev_b32 v168, 16, v96\n\t" \
  "v_and_b32 v169, 0xffff0000, v96\n\t" \
  "v_lshlrev_b32 v170, 16, v97\n\t" \
  "v_and_b32 v171, 0xffff0000, v97\n\t" \
  "s_waitcnt lgkmcnt(0)\n\t" \
  "s_barrier\n\t" \
  "ds_read_b128 v[28:31], v211 offset:0\n\t" \
  "ds_read_b128 v[32:35], v211 offset:1024\n\t" \
  "ds_read_b128 v[36:39], v211 offset:2048\n\t" \
  "ds_read_b128 v[40:43], v211 offset:3072\n\t" \
  "ds_read_b128 v[44:47], v211 offset:4096\n\t" \
  "ds_read_b128 v[48:51], v211 offset:5120\n\t" \
  "ds_read_b128 v[52:55], v211 offset:6144\n\t" \
  "ds_read_b128 v[56:59], v211 offset:7168\n\t" \
  "ds_read_b128 v[60:63], v211 offset:8192\n\t" \
  "ds_read_b128 v[64:67], v211 offset:9216\n\t" \
  "ds_read_b128 v[68:71], v211 offset:10240\n\t" \
  "ds_read_b128 v[72:75], v211 offset:11264\n\t" \
  "ds_read_b128 v[76:79], v211 offset:12288\n\t" \
  "ds_read_b128 v[80:83], v211 offset:13312\n\t" \
  "ds_read_b128 v[84:87], v211 offset:14336\n\t" \
  "s_waitcnt lgkmcnt(14)\n\t" \
  "v_pk_add_f32 v[8:9], v[8:9], v[28:29]\n\t" \
  "v_pk_add_f32 v[10:11], v[10:11], v[30:31]\n\t" \
  "s_waitcnt lgkmcnt(13)\n\t" \
  "v_pk_add_f32 v[12:13], v[12:13], v[32:33]\n\t" \
  "v_pk_add_f32 v[14:15], v[14:15], v[34:35]\n\t" \
  "s_waitcnt lgkmcnt(12)\n\t" \
  "v_pk_add_f32 v[16:17], v[16:17], v[36:37]\n\t" \
  "v_pk_add_f32 v[18:19], v[18:19], v[38:39]\n\t" \
  "s_waitcnt lgkmcnt(11)\n\t" \
  "v_pk_add_f32 v[20:21], v[20:21], v[40:41]\n\t" \
  "v_pk_add_f32 v[22:23], v[22:23], v[42:43]\n\t" \
  "s_waitcnt lgkmcnt(10)\n\t" \
  "v_pk_add_f32 v[24:25], v[24:25], v[44:45]\n\t" \
  "v_pk_add_f32 v[26:27], v[26:27], v[46:47]\n\t" \
  "s_waitcnt lgkmcnt(4)\n\t" \
  "v_pk_add_f32 v[48:49], v[48:49], v[68:69]\n\t" \
  "v_pk_add_f32 v[50:51], v[50:51], v[70:71]\n\t" \
  "s_waitcnt lgkmcnt(3)\n\t" \
  "v_pk_add_f32 v[52:53], v[52:53], v[72:73]\n\t" \
  "v_pk_add_f32 v[54:55], v[54:55], v[74:75]\n\t" \
  "s_waitcnt lgkmcnt(2)\n\t" \
  "v_pk_add_f32 v[56:57], v[56:57], v[76:77]\n\t" \
  "v_pk_add_f32 v[58:59], v[58:59], v[78:79]\n\t" \
  "s_waitcnt lgkmcnt(1)\n\t" \
  "v_pk_add_f32 v[60:61], v[60:61], v[80:81]\n\t" \
  "v_pk_add_f32 v[62:63], v[62:63], v[82:83]\n\t" \
  "s_waitcnt lgkmcnt(0)\n\t" \
  "v_pk_add_f32 v[64:65], v[64:65], v[84:85]\n\t" \
  "v_pk_add_f32 v[66:67], v[66:67], v[86:87]\n\t" \
  "v_pk_add_f32 v[8:9], v[8:9], v[48:49]\n\t" \
  "v_pk_add_f32 v[10:11], v[10:11], v[50:51]\n\t" \
  "v_pk_add_f32 v[12:13], v[12:13], v[52:53]\n\t" \
  "v_pk_add_f32 v[14:15], v[14:15], v[54:55]\n\t" \
  "v_pk_add_f32 v[16:17], v[16:17], v[56:57]\n\t" \
  "v_pk_add_f32 v[18:19], v[18:19], v[58:59]\n\t" \
  "v_pk_add_f32 v[20:21], v[20:21], v[60:61]\n\t" \
  "v_pk_add_f32 v[22:23], v[22:23], v[62:63]\n\t" \
  "v_pk_add_f32 v[24:25], v[24:25], v[64:65]\n\t" \
  "v_pk_add_f32 v[26:27], v[26:27], v[66:67]\n\t" \
  "v_pk_fma_f32 v[8:9], v[8:9], v[100:101], v[152:153]\n\t" \
  "v_pk_fma_f32 v[10:11], v[10:11], v[102:103], v[154:155]\n\t" \
  "v_pk_fma_f32 v[12:13], v[12:13], v[104:105], v[156:157]\n\t" \
  "v_pk_fma_f32 v[14:15], v[14:15], v[106:107], v[158:159]\n\t" \
  "v_pk_fma_f32 v[16:17], v[16:17], v[108:109], v[160:161]\n\t" \
  "v_pk_fma_f32 v[18:19], v[18:19], v[110:111], v[162:163]\n\t" \
  "v_pk_fma_f32 v[20:21], v[20:21], v[112:113], v[164:165]\n\t" \
  "v_pk_fma_f32 v[22:23], v[22:23], v[114:115], v[166:167]\n\t" \
  "v_pk_fma_f32 v[24:25], v[24:25], v[116:117], v[168:169]\n\t" \
  "v_pk_fma_f32 v[26:27], v[26:27], v[118:119], v[170:171]\n\t" \
  "v_cvt_pk_bf16_f32 v88, v8, v9\n\t" \
  "v_cvt_pk_bf16_f32 v89, v10, v11\n\t" \
  "v_cvt_pk_bf16_f32 v90, v12, v13\n\t" \
  "v_cvt_pk_bf16_f32 v91, v14, v15\n\t" \
  "v_cvt_pk_bf16_f32 v92, v16, v17\n\t" \
  "v_cvt_pk_bf16_f32 v93, v18, v19\n\t" \
  "v_cvt_pk_bf16_f32 v94, v20, v21\n\t" \
  "v_cvt_pk_bf16_f32 v95, v22, v23\n\t" \
  "v_cvt_pk_bf16_f32 v96, v24, v25\n\t" \
  "v_cvt_pk_bf16_f32 v97, v26, v27\n\t" \
  "ds_write_b64 v221, v[88:89] offset:0\n\t" \
  "ds_write_b64 v221, v[90:91] offset:32\n\t" \
  "ds_write_b64 v221, v[92:93] offset:64\n\t" \
  "ds_write_b64 v221, v[94:95] offset:96\n\t" \
  "ds_write_b64 v221, v[96:97] offset:128\n\t" \
  "v_lshlrev_b32 v152, 16, v88\n\t" \
  "v_and_b32 v153, 0xffff0000, v88\n\t" \
  "v_lshlrev_b32 v154, 16, v89\n\t" \
  "v_and_b32 v155, 0xffff0000, v89\n\t" \
  "v_lshlrev_b32 v156, 16, v90\n\t" \
  "v_and_b32 v157, 0xffff0000, v90\n\t" \
  "v_lshlrev_b32 v158, 16, v91\n\t" \
  "v_and_b32 v159, 0xffff0000, v91\n\t" \
  "v_lshlrev_b32 v160, 16, v92\n\t" \
  "v_and_b32 v161, 0xffff0000, v92\n\t" \
  "v_lshlrev_b32 v162, 16, v93\n\t" \
  "v_and_b32 v163, 0xffff0000, v93\n\t" \
  "v_lshlrev_b32 v164, 16, v94\n\t" \
  "v_and_b32 v165, 0xffff0000, v94\n\t" \
  "v_lshlrev_b32 v166, 16, v95\n\t" \
  "v_and_b32 v167, 0xffff0000, v95\n\t" \
  "v_lshlrev_b32 v168, 16, v96\n\t" \
  "v_and_b32 v169, 0xffff0000, v96\n\t" \
  "v_lshlrev_b32 v170, 16, v97\n\t" \
  "v_and_b32 v171, 0xffff0000, v97\n\t" \
  "v_sub_f32 v152, v152, v215\n\t" \
  "v_sub_f32 v153, v153, v215\n\t" \
  "v_sub_f32 v154, v154, v215\n\t" \
  "v_sub_f32 v155, v155, v215\n\t" \
  "v_sub_f32 v156, v156, v215\n\t" \
  "v_sub_f32 v157, v157, v215\n\t" \
  "v_sub_f32 v158, v158, v215\n\t" \
  "v_sub_f32 v159, v159, v215\n\t" \
  "v_sub_f32 v160, v160, v215\n\t" \
  "v_sub_f32 v161, v161, v215\n\t" \
  "v_sub_f32 v162, v162, v215\n\t" \
  "v_sub_f32 v163, v163, v215\n\t" \
  "v_sub_f32 v164, v164, v215\n\t" \
  "v_sub_f32 v165, v165, v215\n\t" \
  "v_sub_f32 v166, v166, v215\n\t" \
  "v_sub_f32 v167, v167, v215\n\t" \
  "v_sub_f32 v168, v168, v215\n\t" \
  "v_sub_f32 v169, v169, v215\n\t" \
  "v_sub_f32 v170, v170, v215\n\t" \
  "v_sub_f32 v171, v171, v215\n\t" \
  "v_pk_mul_f32 v[8:9], v[152:153], v[120:121]\n\t" \
  "v_pk_mul_f32 v[10:11], v[154:155], v[122:123]\n\t" \
  "v_pk_mul_f32 v[12:13], v[156:157], v[124:125]\n\t" \
  "v_pk_mul_f32 v[14:15], v[158:159], v[126:127]\n\t" \
  "v_pk_mul_f32 v[16:17], v[160:161], v[128:129]\n\t" \
  "v_pk_mul_f32 v[18:19], v[162:163], v[130:131]\n\t" \
  "v_pk_mul_f32 v[20:21], v[164:165], v[132:133]\n\t" \
  "v_pk_mul_f32 v[22:23], v[166:167], v[134:135]\n\t" \
  "v_pk_mul_f32 v[24:25], v[168:169], v[136:137]\n\t" \
  "v_pk_mul_f32 v[26:27], v[170:171], v[138:139]\n\t" \
  "v_cvt_pk_bf16_f32 v172, v8, v9\n\t" \
  "v_cvt_pk_bf16_f32 v173, v10, v11\n\t" \
  "v_cvt_pk_bf16_f32 v174, v12, v13\n\t" \
  "v_cvt_pk_bf16_f32 v175, v14, v15\n\t" \
  "v_cvt_pk_bf16_f32 v176, v16, v17\n\t" \
  "v_cvt_pk_bf16_f32 v177, v18, v19\n\t" \
  "v_cvt_pk_bf16_f32 v178, v20, v21\n\t" \
  "v_cvt_pk_bf16_f32 v179, v22, v23\n\t" \
  "v_cvt_pk_bf16_f32 v180, v24, v25\n\t" \
  "v_cvt_pk_bf16_f32 v181, v26, v27\n\t" \
  "s_bitcmp1_b32 %[nt], 0\n\t" \
  "s_cbranch_scc1 .Lyodd%=\n\t" \
  "buffer_store_dwordx4 v[172:175], v202, s[40:43], s85 offen sc1\n\t" \
  "buffer_store_dwordx4 v[176:179], v202, s[40:43], s86 offen sc1\n\t" \
  "buffer_store_dwordx2 v[180:181], v223, s[40:43], s87 offen sc1\n\t" \
  "s_branch .Lydone%=\n\t" \
  ".Lyodd%=:\n\t" \
  "buffer_store_dwordx2 v[172:173], v223, s[40:43], s85 offen offset:512 sc1\n\t" \
  "buffer_store_dwordx4 v[174:177], v202, s[40:43], s86 offen sc1\n\t" \
  "buffer_store_dwordx4 v[178:181], v202, s[40:43], s87 offen sc1\n\t" \
  ".Lydone%=:\n\t" \
  "v_pk_add_f32 v[184:185], v[152:153], v[154:155]\n\t" \
  "v_pk_mul_f32 v[186:187], v[152:153], v[152:153]\n\t" \
  "v_pk_fma_f32 v[186:187], v[154:155], v[154:155], v[186:187]\n\t" \
  "v_pk_add_f32 v[184:185], v[184:185], v[156:157]\n\t" \
  "v_pk_fma_f32 v[186:187], v[156:157], v[156:157], v[186:187]\n\t" \
  "v_pk_add_f32 v[184:185], v[184:185], v[158:159]\n\t" \
  "v_pk_fma_f32 v[186:187], v[158:159], v[158:159], v[186:187]\n\t" \
  "v_pk_add_f32 v[184:185], v[184:185], v[160:161]\n\t" \
  "v_pk_fma_f32 v[186:187], v[160:161], v[160:161], v[186:187]\n\t" \
  "v_pk_add_f32 v[184:185], v[184:185], v[162:163]\n\t" \
  "v_pk_fma_f32 v[186:187], v[162:163], v[162:163], v[186:187]\n\t" \
  "v_pk_add_f32 v[184:185], v[184:185], v[164:165]\n\t" \
  "v_pk_fma_f32 v[186:187], v[164:165], v[164:165], v[186:187]\n\t" \
  "v_pk_add_f32 v[184:185], v[184:185], v[166:167]\n\t" \
  "v_pk_fma_f32 v[186:187], v[166:167], v[166:167], v[186:187]\n\t" \
  "v_pk_add_f32 v[184:185], v[184:185], v[168:169]\n\t" \
  "v_pk_fma_f32 v[186:187], v[168:169], v[168:169], v[186:187]\n\t" \
  "v_pk_add_f32 v[184:185], v[184:185], v[170:171]\n\t" \
  "v_pk_fma_f32 v[186:187], v[170:171], v[170:171], v[186:187]\n\t" \
  "v_add_f32 v184, v184, v185\n\t" \
  "v_add_f32 v185, v186, v187\n\t" \
  "v_mov_b32 v186, v184\n\t" \
  "v_mov_b32 v187, v185\n\t" \
  "s_nop 0\n\t" \
  "v_permlane16_swap_b32 v184, v186\n\t" \
  "v_permlane16_swap_b32 v185, v187\n\t" \
  "v_add_f32 v184, v184, v186\n\t" \
  "v_add_f32 v185, v185, v187\n\t" \
  "v_mov_b32 v186, v184\n\t" \
  "v_mov_b32 v187, v185\n\t" \
  "s_nop 0\n\t" \
  "v_permlane32_swap_b32 v184, v186\n\t" \
  "v_permlane32_swap_b32 v185, v187\n\t" \
  "v_add_f32 v184, v184, v186\n\t" \
  "v_add_f32 v185, v185, v187\n\t" \
  "s_mov_b64 exec, 0xffff\n\t" \
  "global_store_dwordx2 v223, v[184:185], s[68:69] sc1\n\t" \
  "s_mov_b64 exec, -1\n\t" \
  "s_mul_i32 s36, %[nt], 276480\n\t" \
  "s_mul_i32 s72, %[w], 71680\n\t" \
  "s_add_u32 s36, s36, s72\n\t" \
  "s_add_u32 s36, s36, 34652160\n\t" \
  "s_mov_b64 s[72:73], %[ws]\n\t" \
  "s_add_u32 s36, s36, s72\n\t" \
  "s_addc_u32 s37, s73, 0\n\t" \
  "s_and_b32 s37, s37, 0xffff\n\t" \
  "s_mov_b32 s39, 0x20000\n\t" \
  "s_mul_i32 s84, %[w], 71680\n\t" \
  "s_sub_u32 s38, 276480, s84\n\t" \
  "s_mul_i32 s68, %[mt], 1024\n\t" \
  "s_mul_i32 s72, %[nt], 128\n\t" \
  "s_add_u32 s68, s68, s72\n\t" \
  "s_add_u32 s68, s68, 42237952\n\t" \
  "s_mov_b64 s[72:73], %[ws]\n\t" \
  "s_add_u32 s68, s68, s72\n\t" \
  "s_addc_u32 s69, s73, 0\n\t" \
  "s_waitcnt vmcnt(0) lgkmcnt(0)\n\t" \
  "s_barrier\n\t" \
  "s_cmp_lg_u32 %[w], 0\n\t" \
  "s_cbranch_scc1 .Lfy%=\n\t" \
  "s_mov_b64 exec, 0xffffffff\n\t" \
  "v_lshlrev_b32 v200, 2, v203\n\t" \
  "v_mov_b32 v201, %[tag]\n\t" \
  "global_store_dword v200, v201, s[68:69] sc1\n\t" \
  "s_mov_b64 exec, -1\n\t" \
  ".Lfy%=:\n\t" \
  "s_cmp_lt_u32 %[ft], 27\n\t" \
  "s_cbranch_scc0 .Lidle%=\n\t" \
  "s_add_u32 m0, s80, 65536\n\t" \
  "s_mov_b32 s74, 884736\n\t" \
  "buffer_load_dwordx4 v216, s[56:59], s74 offen offset:0 lds\n\t" \
  "buffer_load_dwordx4 v217, s[60:63], s74 offen offset:1024 lds\n\t" \
  "buffer_load_dwordx4 v218, s[56:59], s74 offen offset:2048 lds\n\t" \
  "buffer_load_dwordx4 v219, s[60:63], s74 offen offset:3072 lds\n\t" \
  "s_lshl_b32 s91, %[pc], 2\n\t" \
  "s_add_u32 s91, s91, %[w]\n\t" \
  "s_mul_i32 s44, s91, 81920\n\t" \
  "s_add_u32 s44, s44, 2932736\n\t" \
  "s_mov_b64 s[72:73], %[ws]\n\t" \
  "s_add_u32 s44, s44, s72\n\t" \
  "s_addc_u32 s45, s73, 0\n\t" \
  "s_and_b32 s45, s45, 0xffff\n\t" \
  "s_mov_b32 s46, 0x14000\n\t" \
  "s_mov_b32 s47, 0x20000\n\t" \
  "s_mul_i32 s64, s91, 4096\n\t" \
  "s_add_u32 s64, s64, 180224\n\t" \
  "s_mov_b64 s[72:73], %[ws]\n\t" \
  "s_add_u32 s64, s64, s72\n\t" \
  "s_addc_u32 s65, s73, 0\n\t" \
  "s_and_b32 s65, s65, 0xffff\n\t" \
  "s_mov_b32 s66, 0x1000\n\t" \
  "s_mov_b32 s67, 0x20000\n\t" \
  "s_mul_i32 s48, s91, 221184\n\t" \
  "s_mul_i32 s72, %[ft], 8192\n\t" \
  "s_add_u32 s48, s48, s72\n\t" \
  "s_add_u32 s48, s48, 22331392\n\t" \
  "s_mov_b64 s[72:73], %[ws]\n\t" \
  "s_add_u32 s48, s48, s72\n\t" \
  "s_addc_u32 s49, s73, 0\n\t" \
  "s_and_b32 s49, s49, 0xffff\n\t" \
  "s_mov_b32 s50, 0x2000\n\t" \
  "s_mov_b32 s51, 0x20000\n\t" \
  "v_and_b32 v240, 3, v203\n\t" \
  "v_lshlrev_b32 v200, 3, v240\n\t" \
  "v_bfe_u32 v240, v203, 2, 3\n\t" \
  "v_lshl_or_b32 v200, v240, 6, v200\n\t" \
  "v_bfe_u32 v240, v203, 4, 1\n\t" \
  "v_lshl_or_b32 v200, v240, 5, v200\n\t" \
  "v_lshrrev_b32 v240, 5, v203\n\t" \
  "v_lshl_or_b32 v200, v240, 11, v200\n\t" \
  "v_xor_b32 v201, 32, v200\n\t" \
  "ds_read_b64_tr_b16 a[0:1], v200 offset:0\n\t" \
  "ds_read_b64_tr_b16 a[2:3], v200 offset:4096\n\t" \
  "ds_read_b64_tr_b16 a[4:5], v201 offset:0\n\t" \
  "ds_read_b64_tr_b16 a[6:7], v201 offset:4096\n\t" \
  "ds_read_b64_tr_b16 a[8:9], v200 offset:512\n\t" \
  "ds_read_b64_tr_b16 a[10:11], v200 offset:4608\n\t" \
  "ds_read_b64_tr_b16 a[12:13], v201 offset:512\n\t" \
  "ds_read_b64_tr_b16 a[14:15], v201 offset:4608\n\t" \
  "ds_read_b64_tr_b16 a[16:17], v200 offset:1024\n\t" \
  "ds_read_b64_tr_b16 a[18:19], v200 offset:5120\n\t" \
  "ds_read_b64_tr_b16 a[20:21], v201 offset:1024\n\t" \
  "ds_read_b64_tr_b16 a[22:23], v201 offset:5120\n\t" \
  "ds_read_b64_tr_b16 a[24:25], v200 offset:1536\n\t" \
  "ds_read_b64_tr_b16 a[26:27], v200 offset:5632\n\t" \
  "ds_read_b64_tr_b16 a[28:29], v201 offset:1536\n\t" \
  "ds_read_b64_tr_b16 a[30:31], v201 offset:5632\n\t" \
  "s_mul_i32 s68, s91, 1024\n\t" \
  "s_add_u32 s68, s68, 42237952\n\t" \
  "s_mov_b64 s[72:73], %[ws]\n\t" \
  "s_add_u32 s68, s68, s72\n\t" \
  "s_addc_u32 s69, s73, 0\n\t" \
  "v_and_b32 v242, 7, v203\n\t" \
  "v_mul_u32_u24 v242, 128, v242\n\t" \
  "s_mov_b32 s84, 0\n\t" \
  ".Lpy%=:\n\t" \
  "global_load_dword v243, v242, s[68:69] sc1\n\t" \
  "s_waitcnt vmcnt(0)\n\t" \
  "v_cmp_ne_u32 vcc, %[tag], v243\n\t" \
  "s_cbranch_vccz .Lpyd%=\n\t" \
  "s_sleep 1\n\t" \
  "s_add_u32 s84, s84, 1\n\t" \
  "s_cmp_lt_u32 s84, 30000\n\t" \
  "s_cbranch_scc1 .Lpy%=\n\t" \
  "v_mov_b32 v243, 0\n\t" \
  "v_mov_b32 v244, 1\n\t" \
  "global_store_dword v243, v244, s[70:71] sc1\n\t" \
  "s_waitcnt vmcnt(0)\n\t" \
  ".Lpyd%=:\n\t" \
  "v_and_b32 v240, 15, v203\n\t" \
  "v_lshrrev_b32 v241, 4, v203\n\t" \
  "v_lshlrev_b32 v240, 3, v240\n\t" \
  "v_lshl_or_b32 v240, v241, 10, v240\n\t" \
  "s_mov_b32 s68, 42336256\n\t" \
  "s_mov_b64 s[72:73], %[ws]\n\t" \
  "s_add_u32 s68, s68, s72\n\t" \
  "s_addc_u32 s69, s73, 0\n\t" \
  "v_min_u32 v244, 39, v203\n\t" \
  "v_lshlrev_b32 v244, 7, v244\n\t" \
  "s_mov_b32 s76, 0\n\t" \
  "buffer_load_dwordx4 v[136:139], v202, s[44:47], s76 offen offset:0 sc1\n\t" \
  "buffer_load_dwordx4 v[140:143], v202, s[44:47], s76 offen offset:1024 sc1\n\t" \
  "buffer_load_dwordx4 v[144:147], v202, s[44:47], s76 offen offset:2048 sc1\n\t" \
  "buffer_load_dwordx4 v[148:151], v202, s[44:47], s76 offen offset:3072 sc1\n\t" \
  "s_mov_b32 s76, 4096\n\t" \
  "buffer_load_dwordx4 v[152:155], v202, s[44:47], s76 offen offset:0 sc1\n\t" \
  "buffer_load_dwordx4 v[156:159], v202, s[44:47], s76 offen offset:1024 sc1\n\t" \
  "buffer_load_dwordx4 v[160:163], v202, s[44:47], s76 offen offset:2048 sc1\n\t" \
  "buffer_load_dwordx4 v[164:167], v202, s[44:47], s76 offen offset:3072 sc1\n\t" \
  "s_mov_b32 s76, 8192\n\t" \
  "buffer_load_dwordx2 v[168:169], v223, s[44:47], s76 offen offset:0 sc1\n\t" \
  "buffer_load_dwordx2 v[170:171], v223, s[44:47], s76 offen offset:512 sc1\n\t" \
  "buffer_load_dwordx2 v[172:173], v223, s[44:47], s76 offen offset:1024 sc1\n\t" \
  "buffer_load_dwordx2 v[174:175], v223, s[44:47], s76 offen offset:1536 sc1\n\t" \
  "buffer_load_dwordx2 v[176:177], v223, s[44:47], s76 offen offset:2048 sc1\n\t" \
  "buffer_load_dwordx2 v[178:179], v223, s[44:47], s76 offen offset:2560 sc1\n\t" \
  "buffer_load_dwordx2 v[180:181], v223, s[44:47], s76 offen offset:3072 sc1\n\t" \
  "buffer_load_dwordx2 v[182:183], v223, s[44:47], s76 offen offset:3584 sc1\n\t" \
  "s_mov_b32 s76, 12288\n\t" \
  "buffer_load_dwordx4 v[184:187], v202, s[44:47], s76 offen offset:0 sc1\n\t" \
  "buffer_load_dwordx4 v[188:191], v202, s[44:47], s76 offen offset:1024 sc1\n\t" \
  "buffer_load_dwordx4 v[192:195], v202, s[44:47], s76 offen offset:2048 sc1\n\t" \
  "buffer_load_dwordx4 v[196:199], v202, s[44:47], s76 offen offset:3072 sc1\n\t" \
  "s_waitcnt vmcnt(19) lgkmcnt(0)\n\t" \
  "v_mfma_f32_16x16x32_bf16 v[8:11], a[0:3], v[136:139], 0\n\t" \
  "s_mov_b32 s76, 16384\n\t" \
  "buffer_load_dwordx4 a[64:67], v202, s[44:47], s76 offen offset:0 sc1\n\t" \
  "v_mfma_f32_16x16x32_bf16 v[12:15], a[4:7], v[136:139], 0\n\t" \
  "ds_read_b64_tr_b16 a[32:33], v200 offset:8192\n\t" \
  "v_mfma_f32_16x16x32_bf16 v[16:19], a[8:11], v[136:139], 0\n\t" \
  "ds_read_b64_tr_b16 a[34:35], v200 offset:12288\n\t" \
  "v_mfma_f32_16x16x32_bf16 v[20:23], a[12:15], v[136:139], 0\n\t" \
  "ds_read_b64_tr_b16 a[36:37], v201 offset:8192\n\t" \
  "v_mfma_f32_16x16x32_bf16 v[24:27], a[16:19], v[136:139], 0\n\t" \
  "buffer_load_dwordx4 a[68:71], v202, s[44:47], s76 offen offset:1024 sc1\n\t" \
  "v_mfma_f32_16x16x32_bf16 v[28:31], a[20:23], v[136:139], 0\n\t" \
  "ds_read_b64_tr_b16 a[38:39], v201 offset:12288\n\t" \
  "v_mfma_f32_16x16x32_bf16 v[32:35], a[24:27], v[136:139], 0\n\t" \
  "ds_read_b64_tr_b16 a[40:41], v200 offset:8704\n\t" \
  "v_mfma_f32_16x16x32_bf16 v[36:39], a[28:31], v[136:139], 0\n\t" \
  "ds_read_b64_tr_b16 a[42:43], v200 offset:12800\n\t" \
  "s_waitcnt vmcnt(20)\n\t" \
  "v_mfma_f32_16x16x32_bf16 v[40:43], a[0:3], v[140:143], 0\n\t" \
  "buffer_load_dwordx4 a[72:75], v202, s[44:47], s76 offen offset:2048 sc1\n\t" \
  "v_mfma_f32_16x16x32_bf16 v[44:47], a[4:7], v[140:143], 0\n\t" \
  "ds_read_b64_tr_b16 a[44:45], v201 offset:8704\n\t" \
  "v_mfma_f32_16x16x32_bf16 v[48:51], a[8:11], v[140:143], 0\n\t" \
  "ds_read_b64_tr_b16 a[46:47], v201 offset:12800\n\t" \
  "v_mfma_f32_16x16x32_bf16 v[52:55], a[12:15], v[140:143], 0\n\t" \
  "ds_read_b64_tr_b16 a[48:49], v200 offset:9216\n\t" \
  "v_mfma_f32_16x16x32_bf16 v[56:59], a[16:19], v[140:143], 0\n\t" \
  "buffer_load_dwordx4 a[76:79], v202, s[44:47], s76 offen offset:3072 sc1\n\t" \
  "v_mfma_f32_16x16x32_bf16 v[60:63], a[20:23], v[140:143], 0\n\t" \
  "ds_read_b64_tr_b16 a[50:51], v200 offset:13312\n\t" \
  "v_mfma_f32_16x16x32_bf16 v[64:67], a[24:27], v[140:143], 0\n\t" \
  "ds_read_b64_tr_b16 a[52:53], v201 offset:9216\n\t" \
  "v_mfma_f32_16x16x32_bf16 v[68:71], a[28:31], v[140:143], 0\n\t" \
  "s_mov_b32 s76, 20480\n\t" \
  "ds_read_b64_tr_b16 a[54:55], v201 offset:13312\n\t" \
  "s_waitcnt vmcnt(21)\n\t" \
  "v_mfma_f32_16x16x32_bf16 v[72:75], a[0:3], v[144:147], 0\n\t" \
  "buffer_load_dwordx4 a[80:83], v202, s[44:47], s76 offen offset:0 sc1\n\t" \
  "v_mfma_f32_16x16x32_bf16 v[76:79], a[4:7], v[144:147], 0\n\t" \
  "ds_read_b64_tr_b16 a[56:57], v200 offset:9728\n\t" \
  "v_mfma_f32_16x16x32_bf16 v[80:83], a[8:11], v[144:147], 0\n\t" \
  "ds_read_b64_tr_b16 a[58:59], v200 offset:13824\n\t" \
  "v_mfma_f32_16x16x32_bf16 v[84:87], a[12:15], v[144:147], 0\n\t" \
  "ds_read_b64_tr_b16 a[60:61], v201 offset:9728\n\t" \
  "v_mfma_f32_16x16x32_bf16 v[88:91], a[16:19], v[144:147], 0\n\t" \
  "buffer_load_dwordx4 a[84:87], v202, s[44:47], s76 offen offset:1024 sc1\n\t" \
  "v_mfma_f32_16x16x32_bf16 v[92:95], a[20:23], v[144:147], 0\n\t" \
  "ds_read_b64_tr_b16 a[62:63], v201 offset:13824\n\t" \
  "v_mfma_f32_16x16x32_bf16 v[96:99], a[24:27], v[144:147], 0\n\t" \
  "buffer_load_dwordx4 a[88:91], v202, s[44:47], s76 offen offset:2048 sc1\n\t" \
  "v_mfma_f32_16x16x32_bf16 v[100:103], a[28:31], v[144:147], 0\n\t" \
  "s_waitcnt vmcnt(23)\n\t" \
  "v_mfma_f32_16x16x32_bf16 v[104:107], a[0:3], v[148:151], 0\n\t" \
  "v_mfma_f32_16x16x32_bf16 v[108:111], a[4:7], v[148:151], 0\n\t" \
  "v_mfma_f32_16x16x32_bf16 v[112:115], a[8:11], v[148:151], 0\n\t" \
  "v_mfma_f32_16x16x32_bf16 v[116:119], a[12:15], v[148:151], 0\n\t" \
  "v_mfma_f32_16x16x32_bf16 v[120:123], a[16:19], v[148:151], 0\n\t" \
  "v_mfma_f32_16x16x32_bf16 v[124:127], a[20:23], v[148:151], 0\n\t" \
  "v_mfma_f32_16x16x32_bf16 v[128:131], a[24:27], v[148:151], 0\n\t" \
  "v_mfma_f32_16x16x32_bf16 v[132:135], a[28:31], v[148:151], 0\n\t" \
  "s_waitcnt vmcnt(22) lgkmcnt(4)\n\t" \
  "v_mfma_f32_16x16x32_bf16 v[8:11], a[32:35], v[152:155], v[8:11]\n\t" \
  "s_add_u32 m0, s80, 81920\n\t" \
  "s_mov_b32 s74, 1105920\n\t" \
  "buffer_load_dwordx4 v216, s[56:59], s74 offen offset:0 lds\n\t" \
  "v_mfma_f32_16x16x32_bf16 v[12:15], a[36:39], v[152:155], v[12:15]\n\t" \
  "ds_read_b64_tr_b16 a[0:1], v200 offset:16384\n\t" \
  "v_mfma_f32_16x16x32_bf16 v[16:19], a[40:43], v[152:155], v[16:19]\n\t" \
  "ds_read_b64_tr_b16 a[2:3], v200 offset:20480\n\t" \
  "v_mfma_f32_16x16x32_bf16 v[20:23], a[44:47], v[152:155], v[20:23]\n\t" \
  "ds_read_b64_tr_b16 a[4:5], v201 offset:16384\n\t" \
  "v_mfma_f32_16x16x32_bf16 v[24:27], a[48:51], v[152:155], v[24:27]\n\t" \
  "buffer_load_dwordx4 a[92:95], v202, s[44:47], s76 offen offset:3072 sc1\n\t" \
  "v_mfma_f32_16x16x32_bf16 v[28:31], a[52:55], v[152:155], v[28:31]\n\t" \
  "ds_read_b64_tr_b16 a[6:7], v201 offset:20480\n\t" \
  "s_waitcnt lgkmcnt(4)\n\t" \
  "v_mfma_f32_16x16x32_bf16 v[32:35], a[56:59], v[152:155], v[32:35]\n\t" \
  "ds_read_b64_tr_b16 a[8:9], v200 offset:16896\n\t" \
  "v_mfma_f32_16x16x32_bf16 v[36:39], a[60:63], v[152:155], v[36:39]\n\t" \
  "ds_read_b64_tr_b16 a[10:11], v200 offset:20992\n\t" \
  "s_waitcnt vmcnt(23)\n\t" \
  "v_mfma_f32_16x16x32_bf16 v[40:43], a[32:35], v[156:159], v[40:43]\n\t" \
  "buffer_load_dwordx4 v217, s[60:63], s74 offen offset:1024 lds\n\t" \
  "v_mfma_f32_16x16x32_bf16 v[44:47], a[36:39], v[156:159], v[44:47]\n\t" \
  "ds_read_b64_tr_b16 a[12:13], v201 offset:16896\n\t" \
  "v_mfma_f32_16x16x32_bf16 v[48:51], a[40:43], v[156:159], v[48:51]\n\t" \
  "ds_read_b64_tr_b16 a[14:15], v201 offset:20992\n\t" \
  "v_mfma_f32_16x16x32_bf16 v[52:55], a[44:47], v[156:159], v[52:55]\n\t" \
  "s_mov_b32 s76, 24576\n\t" \
  "ds_read_b64_tr_b16 a[16:17], v200 offset:17408\n\t" \
  "v_mfma_f32_16x16x32_bf16 v[56:59], a[48:51], v[156:159], v[56:59]\n\t" \
  "buffer_load_dwordx4 a[96:99], v202, s[44:47], s76 offen offset:0 sc1\n\t" \
  "v_mfma_f32_16x16x32_bf16 v[60:63], a[52:55], v[156:159], v[60:63]\n\t" \
  "ds_read_b64_tr_b16 a[18:19], v200 offset:21504\n\t" \
  "v_mfma_f32_16x16x32_bf16 v[64:67], a[56:59], v[156:159], v[64:67]\n\t" \
  "ds_read_b64_tr_b16 a[20:21], v201 offset:17408\n\t" \
  "v_mfma_f32_16x16x32_bf16 v[68:71], a[60:63], v[156:159], v[68:71]\n\t" \
  "ds_read_b64_tr_b16 a[22:23], v201 offset:21504\n\t" \
  "s_waitcnt vmcnt(24)\n\t" \
  "v_mfma_f32_16x16x32_bf16 v[72:75], a[32:35], v[160:163], v[72:75]\n\t" \
  "buffer_load_dwordx4 v218, s[56:59], s74 offen offset:2048 lds\n\t" \
  "v_mfma_f32_16x16x32_bf16 v[76:79], a[36:39], v[160:163], v[76:79]\n\t" \
  "ds_read_b64_tr_b16 a[24:25], v200 offset:17920\n\t" \
  "v_mfma_f32_16x16x32_bf16 v[80:83], a[40:43], v[160:163], v[80:83]\n\t" \
  "ds_read_b64_tr_b16 a[26:27], v200 offset:22016\n\t" \
  "v_mfma_f32_16x16x32_bf16 v[84:87], a[44:47], v[160:163], v[84:87]\n\t" \
  "ds_read_b64_tr_b16 a[28:29], v201 offset:17920\n\t" \
  "v_mfma_f32_16x16x32_bf16 v[88:91], a[48:51], v[160:163], v[88:91]\n\t" \
  "buffer_load_dwordx4 a[100:103], v202, s[44:47], s76 offen offset:1024 sc1\n\t" \
  "v_mfma_f32_16x16x32_bf16 v[92:95], a[52:55], v[160:163], v[92:95]\n\t" \
  "ds_read_b64_tr_b16 a[30:31], v201 offset:22016\n\t" \
  "v_mfma_f32_16x16x32_bf16 v[96:99], a[56:59], v[160:163], v[96:99]\n\t" \
  "buffer_load_dwordx4 v219, s[60:63], s74 offen offset:3072 lds\n\t" \
  "v_mfma_f32_16x16x32_bf16 v[100:103], a[60:63], v[160:163], v[100:103]\n\t" \
  "s_waitcnt vmcnt(26)\n\t" \
  "v_mfma_f32_16x16x32_bf16 v[104:107], a[32:35], v[164:167], v[104:107]\n\t" \
  "v_mfma_f32_16x16x32_bf16 v[108:111], a[36:39], v[164:167], v[108:111]\n\t" \
  "v_mfma_f32_16x16x32_bf16 v[112:115], a[40:43], v[164:167], v[112:115]\n\t" \
  "v_mfma_f32_16x16x32_bf16 v[116:119], a[44:47], v[164:167], v[116:119]\n\t" \
  "v_mfma_f32_16x16x32_bf16 v[120:123], a[48:51], v[164:167], v[120:123]\n\t" \
  "v_mfma_f32_16x16x32_bf16 v[124:127], a[52:55], v[164:167], v[124:127]\n\t" \
  "v_mfma_f32_16x16x32_bf16 v[128:131], a[56:59], v[164:167], v[128:131]\n\t" \
  "v_mfma_f32_16x16x32_bf16 v[132:135], a[60:63], v[164:167], v[132:135]\n\t" \
  "s_waitcnt vmcnt(24) lgkmcnt(4)\n\t" \
  "v_mfma_f32_16x16x32_bf16 v[8:11], a[0:3], v[168:171], v[8:11]\n\t" \
  "s_add_u32 m0, s80, 98304\n\t" \
  "s_mov_b32 s74, 1327104\n\t" \
  "buffer_load_dwordx4 v216, s[56:59], s74 offen offset:0 lds\n\t" \
  "v_mfma_f32_16x16x32_bf16 v[12:15], a[4:7], v[168:171], v[12:15]\n\t" \
  "ds_read_b64_tr_b16 a[32:33], v200 offset:24576\n\t" \
  "v_mfma_f32_16x16x32_bf16 v[16:19], a[8:11], v[168:171], v[16:19]\n\t" \
  "ds_read_b64_tr_b16 a[34:35], v200 offset:28672\n\t" \
  "v_mfma_f32_16x16x32_bf16 v[20:23], a[12:15], v[168:171], v[20:23]\n\t" \
  "ds_read_b64_tr_b16 a[36:37], v201 offset:24576\n\t" \
  "v_mfma_f32_16x16x32_bf16 v[24:27], a[16:19], v[168:171], v[24:27]\n\t" \
  "buffer_load_dwordx4 a[104:107], v202, s[44:47], s76 offen offset:2048 sc1\n\t" \
  "v_mfma_f32_16x16x32_bf16 v[28:31], a[20:23], v[168:171], v[28:31]\n\t" \
  "ds_read_b64_tr_b16 a[38:39], v201 offset:28672\n\t" \
  "s_waitcnt lgkmcnt(4)\n\t" \
  "v_mfma_f32_16x16x32_bf16 v[32:35], a[24:27], v[168:171], v[32:35]\n\t" \
  "ds_read_b64_tr_b16 a[40:41], v200 offset:25088\n\t" \
  "v_mfma_f32_16x16x32_bf16 v[36:39], a[28:31], v[168:171], v[36:39]\n\t" \
  "ds_read_b64_tr_b16 a[42:43], v200 offset:29184\n\t" \
  "s_waitcnt vmcnt(24)\n\t" \
  "v_mfma_f32_16x16x32_bf16 v[40:43], a[0:3], v[172:175], v[40:43]\n\t" \
  "buffer_load_dwordx4 v217, s[60:63], s74 offen offset:1024 lds\n\t" \
  "v_mfma_f32_16x16x32_bf16 v[44:47], a[4:7], v[172:175], v[44:47]\n\t" \
  "ds_read_b64_tr_b16 a[44:45], v201 offset:25088\n\t" \
  "v_mfma_f32_16x16x32_bf16 v[48:51], a[8:11], v[172:175], v[48:51]\n\t" \
  "ds_read_b64_tr_b16 a[46:47], v201 offset:29184\n\t" \
  "v_mfma_f32_16x16x32_bf16 v[52:55], a[12:15], v[172:175], v[52:55]\n\t" \
  "ds_read_b64_tr_b16 a[48:49], v200 offset:25600\n\t" \
  "v_mfma_f32_16x16x32_bf16 v[56:59], a[16:19], v[172:175], v[56:59]\n\t" \
  "buffer_load_dwordx4 a[108:111], v202, s[44:47], s76 offen offset:3072 sc1\n\t" \
  "v_mfma_f32_16x16x32_bf16 v[60:63], a[20:23], v[172:175], v[60:63]\n\t" \
  "ds_read_b64_tr_b16 a[50:51], v200 offset:29696\n\t" \
  "v_mfma_f32_16x16x32_bf16 v[64:67], a[24:27], v[172:175], v[64:67]\n\t" \
  "ds_read_b64_tr_b16 a[52:53], v201 offset:25600\n\t" \
  "v_mfma_f32_16x16x32_bf16 v[68:71], a[28:31], v[172:175], v[68:71]\n\t" \
  "ds_read_b64_tr_b16 a[54:55], v201 offset:29696\n\t" \
  "s_waitcnt vmcnt(24)\n\t" \
  "v_mfma_f32_16x16x32_bf16 v[72:75], a[0:3], v[176:179], v[72:75]\n\t" \
  "buffer_load_dwordx4 v218, s[56:59], s74 offen offset:2048 lds\n\t" \
  "v_mfma_f32_16x16x32_bf16 v[76:79], a[4:7], v[176:179], v[76:79]\n\t" \
  "ds_read_b64_tr_b16 a[56:57], v200 offset:26112\n\t" \
  "v_mfma_f32_16x16x32_bf16 v[80:83], a[8:11], v[176:179], v[80:83]\n\t" \
  "ds_read_b64_tr_b16 a[58:59], v200 offset:30208\n\t" \
  "v_mfma_f32_16x16x32_bf16 v[84:87], a[12:15], v[176:179], v[84:87]\n\t" \
  "s_mov_b32 s76, 28672\n\t" \
  "ds_read_b64_tr_b16 a[60:61], v201 offset:26112\n\t" \
  "v_mfma_f32_16x16x32_bf16 v[88:91], a[16:19], v[176:179], v[88:91]\n\t" \
  "buffer_load_dwordx2 a[112:113], v223, s[44:47], s76 offen offset:0 sc1\n\t" \
  "buffer_load_dwordx2 a[114:115], v223, s[44:47], s76 offen offset:512 sc1\n\t" \
  "v_mfma_f32_16x16x32_bf16 v[92:95], a[20:23], v[176:179], v[92:95]\n\t" \
  "ds_read_b64_tr_b16 a[62:63], v201 offset:30208\n\t" \
  "v_mfma_f32_16x16x32_bf16 v[96:99], a[24:27], v[176:179], v[96:99]\n\t" \
  "buffer_load_dwordx4 v219, s[60:63], s74 offen offset:3072 lds\n\t" \
  "v_mfma_f32_16x16x32_bf16 v[100:103], a[28:31], v[176:179], v[100:103]\n\t" \
  "s_waitcnt vmcnt(26)\n\t" \
  "v_mfma_f32_16x16x32_bf16 v[104:107], a[0:3], v[180:183], v[104:107]\n\t" \
  "v_mfma_f32_16x16x32_bf16 v[108:111], a[4:7], v[180:183], v[108:111]\n\t" \
  "v_mfma_f32_16x16x32_bf16 v[112:115], a[8:11], v[180:183], v[112:115]\n\t" \
  "v_mfma_f32_16x16x32_bf16 v[116:119], a[12:15], v[180:183], v[116:119]\n\t" \
  "v_mfma_f32_16x16x32_bf16 v[120:123], a[16:19], v[180:183], v[120:123]\n\t" \
  "v_mfma_f32_16x16x32_bf16 v[124:127], a[20:23], v[180:183], v[124:127]\n\t" \
  "v_add_u32 v200, 0x8000, v200\n\t" \
  "v_mfma_f32_16x16x32_bf16 v[128:131], a[24:27], v[180:183], v[128:131]\n\t" \
  "v_and_b32 v200, 0x1ffff, v200\n\t" \
  "v_mfma_f32_16x16x32_bf16 v[132:135], a[28:31], v[180:183], v[132:135]\n\t" \
  "v_xor_b32 v201, 32, v200\n\t" \
  "s_waitcnt vmcnt(25) lgkmcnt(2)\n\t" \
  "v_mfma_f32_16x16x32_bf16 v[8:11], a[32:35], v[184:187], v[8:11]\n\t" \
  "s_add_u32 m0, s80, 114688\n\t" \
  "s_mov_b32 s74, 1548288\n\t" \
  "buffer_load_dwordx4 v216, s[56:59], s74 offen offset:0 lds\n\t" \
  "v_mfma_f32_16x16x32_bf16 v[12:15], a[36:39], v[184:187], v[12:15]\n\t" \
  "ds_read_b64_tr_b16 a[0:1], v200 offset:0\n\t" \
  "v_mfma_f32_16x16x32_bf16 v[16:19], a[40:43], v[184:187], v[16:19]\n\t" \
  "ds_read_b64_tr_b16 a[2:3], v200 offset:4096\n\t" \
  "v_mfma_f32_16x16x32_bf16 v[20:23], a[44:47], v[184:187], v[20:23]\n\t" \
  "ds_read_b64_tr_b16 a[4:5], v201 offset:0\n\t" \
  "v_mfma_f32_16x16x32_bf16 v[24:27], a[48:51], v[184:187], v[24:27]\n\t" \
  "buffer_load_dwordx2 a[116:117], v223, s[44:47], s76 offen offset:1024 sc1\n\t" \
  "buffer_load_dwordx2 a[118:119], v223, s[44:47], s76 offen offset:1536 sc1\n\t" \
  "v_mfma_f32_16x16x32_bf16 v[28:31], a[52:55], v[184:187], v[28:31]\n\t" \
  "ds_read_b64_tr_b16 a[6:7], v201 offset:4096\n\t" \
  "v_mfma_f32_16x16x32_bf16 v[32:35], a[56:59], v[184:187], v[32:35]\n\t" \
  "ds_read_b64_tr_b16 a[8:9], v200 offset:512\n\t" \
  "s_waitcnt lgkmcnt(5)\n\t" \
  "v_mfma_f32_16x16x32_bf16 v[36:39], a[60:63], v[184:187], v[36:39]\n\t" \
  "ds_read_b64_tr_b16 a[10:11], v200 offset:4608\n\t" \
  "s_waitcnt vmcnt(27)\n\t" \
  "v_mfma_f32_16x16x32_bf16 v[40:43], a[32:35], v[188:191], v[40:43]\n\t" \
  "buffer_load_dwordx4 v217, s[60:63], s74 offen offset:1024 lds\n\t" \
  "v_mfma_f32_16x16x32_bf16 v[44:47], a[36:39], v[188:191], v[44:47]\n\t" \
  "ds_read_b64_tr_b16 a[12:13], v201 offset:512\n\t" \
  "v_mfma_f32_16x16x32_bf16 v[48:51], a[40:43], v[188:191], v[48:51]\n\t" \
  "ds_read_b64_tr_b16 a[14:15], v201 offset:4608\n\t" \
  "v_mfma_f32_16x16x32_bf16 v[52:55], a[44:47], v[188:191], v[52:55]\n\t" \
  "ds_read_b64_tr_b16 a[16:17], v200 offset:1024\n\t" \
  "v_mfma_f32_16x16x32_bf16 v[56:59], a[48:51], v[188:191], v[56:59]\n\t" \
  "buffer_load_dwordx2 a[120:121], v223, s[44:47], s76 offen offset:2048 sc1\n\t" \
  "buffer_load_dwordx2 a[122:123], v223, s[44:47], s76 offen offset:2560 sc1\n\t" \
  "v_mfma_f32_16x16x32_bf16 v[60:63], a[52:55], v[188:191], v[60:63]\n\t" \
  "ds_read_b64_tr_b16 a[18:19], v200 offset:5120\n\t" \
  "v_mfma_f32_16x16x32_bf16 v[64:67], a[56:59], v[188:191], v[64:67]\n\t" \
  "ds_read_b64_tr_b16 a[20:21], v201 offset:1024\n\t" \
  "v_mfma_f32_16x16x32_bf16 v[68:71], a[60:63], v[188:191], v[68:71]\n\t" \
  "ds_read_b64_tr_b16 a[22:23], v201 offset:5120\n\t" \
  "s_waitcnt vmcnt(29)\n\t" \
  "v_mfma_f32_16x16x32_bf16 v[72:75], a[32:35], v[192:195], v[72:75]\n\t" \
  "buffer_load_dwordx4 v218, s[56:59], s74 offen offset:2048 lds\n\t" \
  "v_mfma_f32_16x16x32_bf16 v[76:79], a[36:39], v[192:195], v[76:79]\n\t" \
  "ds_read_b64_tr_b16 a[24:25], v200 offset:1536\n\t" \
  "v_mfma_f32_16x16x32_bf16 v[80:83], a[40:43], v[192:195], v[80:83]\n\t" \
  "ds_read_b64_tr_b16 a[26:27], v200 offset:5632\n\t" \
  "v_mfma_f32_16x16x32_bf16 v[84:87], a[44:47], v[192:195], v[84:87]\n\t" \
  "ds_read_b64_tr_b16 a[28:29], v201 offset:1536\n\t" \
  "v_mfma_f32_16x16x32_bf16 v[88:91], a[48:51], v[192:195], v[88:91]\n\t" \
  "buffer_load_dwordx2 a[124:125], v223, s[44:47], s76 offen offset:3072 sc1\n\t" \
  "buffer_load_dwordx2 a[126:127], v223, s[44:47], s76 offen offset:3584 sc1\n\t" \
  "v_mfma_f32_16x16x32_bf16 v[92:95], a[52:55], v[192:195], v[92:95]\n\t" \
  "ds_read_b64_tr_b16 a[30:31], v201 offset:5632\n\t" \
  "v_mfma_f32_16x16x32_bf16 v[96:99], a[56:59], v[192:195], v[96:99]\n\t" \
  "buffer_load_dwordx4 v219, s[60:63], s74 offen offset:3072 lds\n\t" \
  "v_mfma_f32_16x16x32_bf16 v[100:103], a[60:63], v[192:195], v[100:103]\n\t" \
  "s_waitcnt vmcnt(32)\n\t" \
  "v_mfma_f32_16x16x32_bf16 v[104:107], a[32:35], v[196:199], v[104:107]\n\t" \
  "v_mfma_f32_16x16x32_bf16 v[108:111], a[36:39], v[196:199], v[108:111]\n\t" \
  "v_mfma_f32_16x16x32_bf16 v[112:115], a[40:43], v[196:199], v[112:115]\n\t" \
  "v_mfma_f32_16x16x32_bf16 v[116:119], a[44:47], v[196:199], v[116:119]\n\t" \
  "v_mfma_f32_16x16x32_bf16 v[120:123], a[48:51], v[196:199], v[120:123]\n\t" \
  "v_mfma_f32_16x16x32_bf16 v[124:127], a[52:55], v[196:199], v[124:127]\n\t" \
  "v_mfma_f32_16x16x32_bf16 v[128:131], a[56:59], v[196:199], v[128:131]\n\t" \
  "v_mfma_f32_16x16x32_bf16 v[132:135], a[60:63], v[196:199], v[132:135]\n\t" \
  "s_waitcnt vmcnt(31) lgkmcnt(3)\n\t" \
  "v_mfma_f32_16x16x32_bf16 v[8:11], a[0:3], a[64:67], v[8:11]\n\t" \
  "s_mov_b32 s76, 32768\n\t" \
  "buffer_load_dwordx4 a[128:131], v202, s[44:47], s76 offen offset:0 sc1\n\t" \
  "v_mfma_f32_16x16x32_bf16 v[12:15], a[4:7], a[64:67], v[12:15]\n\t" \
  "ds_read_b64_tr_b16 a[32:33], v200 offset:8192\n\t" \
  "v_mfma_f32_16x16x32_bf16 v[16:19], a[8:11], a[64:67], v[16:19]\n\t" \
  "ds_read_b64_tr_b16 a[34:35], v200 offset:12288\n\t" \
  "v_mfma_f32_16x16x32_bf16 v[20:23], a[12:15], a[64:67], v[20:23]\n\t" \
  "ds_read_b64_tr_b16 a[36:37], v201 offset:8192\n\t" \
  "v_mfma_f32_16x16x32_bf16 v[24:27], a[16:19], a[64:67], v[24:27]\n\t" \
  "buffer_load_dwordx4 a[132:135], v202, s[44:47], s76 offen offset:1024 sc1\n\t" \
  "v_mfma_f32_16x16x32_bf16 v[28:31], a[20:23], a[64:67], v[28:31]\n\t" \
  "ds_read_b64_tr_b16 a[38:39], v201 offset:12288\n\t" \
  "s_waitcnt lgkmcnt(4)\n\t" \
  "v_mfma_f32_16x16x32_bf16 v[32:35], a[24:27], a[64:67], v[32:35]\n\t" \
  "ds_read_b64_tr_b16 a[40:41], v200 offset:8704\n\t" \
  "v_mfma_f32_16x16x32_bf16 v[36:39], a[28:31], a[64:67], v[36:39]\n\t" \
  "ds_read_b64_tr_b16 a[42:43], v200 offset:12800\n\t" \
  "s_waitcnt vmcnt(32)\n\t" \
  "v_mfma_f32_16x16x32_bf16 v[40:43], a[0:3], a[68:71], v[40:43]\n\t" \
  "buffer_load_dwordx4 a[136:139], v202, s[44:47], s76 offen offset:2048 sc1\n\t" \
  "v_mfma_f32_16x16x32_bf16 v[44:47], a[4:7], a[68:71], v[44:47]\n\t" \
  "ds_read_b64_tr_b16 a[44:45], v201 offset:8704\n\t" \
  "v_mfma_f32_16x16x32_bf16 v[48:51], a[8:11], a[68:71], v[48:51]\n\t" \
  "ds_read_b64_tr_b16 a[46:47], v201 offset:12800\n\t" \
  "v_mfma_f32_16x16x32_bf16 v[52:55], a[12:15], a[68:71], v[52:55]\n\t" \
  "ds_read_b64_tr_b16 a[48:49], v200 offset:9216\n\t" \
  "v_mfma_f32_16x16x32_bf16 v[56:59], a[16:19], a[68:71], v[56:59]\n\t" \
  "buffer_load_dwordx4 a[140:143], v202, s[44:47], s76 offen offset:3072 sc1\n\t" \
  "v_mfma_f32_16x16x32_bf16 v[60:63], a[20:23], a[68:71], v[60:63]\n\t" \
  "ds_read_b64_tr_b16 a[50:51], v200 offset:13312\n\t" \
  "v_mfma_f32_16x16x32_bf16 v[64:67], a[24:27], a[68:71], v[64:67]\n\t" \
  "ds_read_b64_tr_b16 a[52:53], v201 offset:9216\n\t" \
  "v_mfma_f32_16x16x32_bf16 v[68:71], a[28:31], a[68:71], v[68:71]\n\t" \
  "s_mov_b32 s76, 36864\n\t" \
  "ds_read_b64_tr_b16 a[54:55], v201 offset:13312\n\t" \
  "s_waitcnt vmcnt(33)\n\t" \
  "v_mfma_f32_16x16x32_bf16 v[72:75], a[0:3], a[72:75], v[72:75]\n\t" \
  "buffer_load_dwordx4 a[144:147], v202, s[44:47], s76 offen offset:0 sc1\n\t" \
  "v_mfma_f32_16x16x32_bf16 v[76:79], a[4:7], a[72:75], v[76:79]\n\t" \
  "ds_read_b64_tr_b16 a[56:57], v200 offset:9728\n\t" \
  "v_mfma_f32_16x16x32_bf16 v[80:83], a[8:11], a[72:75], v[80:83]\n\t" \
  "ds_read_b64_tr_b16 a[58:59], v200 offset:13824\n\t" \
  "v_mfma_f32_16x16x32_bf16 v[84:87], a[12:15], a[72:75], v[84:87]\n\t" \
  "ds_read_b64_tr_b16 a[60:61], v201 offset:9728\n\t" \
  "v_mfma_f32_16x16x32_bf16 v[88:91], a[16:19], a[72:75], v[88:91]\n\t" \
  "buffer_load_dwordx4 a[148:151], v202, s[44:47], s76 offen offset:1024 sc1\n\t" \
  "v_mfma_f32_16x16x32_bf16 v[92:95], a[20:23], a[72:75], v[92:95]\n\t" \
  "ds_read_b64_tr_b16 a[62:63], v201 offset:13824\n\t" \
  "v_mfma_f32_16x16x32_bf16 v[96:99], a[24:27], a[72:75], v[96:99]\n\t" \
  "buffer_load_dwordx4 a[152:155], v202, s[44:47], s76 offen offset:2048 sc1\n\t" \
  "v_mfma_f32_16x16x32_bf16 v[100:103], a[28:31], a[72:75], v[100:103]\n\t" \
  "s_waitcnt vmcnt(35)\n\t" \
  "v_mfma_f32_16x16x32_bf16 v[104:107], a[0:3], a[76:79], v[104:107]\n\t" \
  "v_mfma_f32_16x16x32_bf16 v[108:111], a[4:7], a[76:79], v[108:111]\n\t" \
  "v_mfma_f32_16x16x32_bf16 v[112:115], a[8:11], a[76:79], v[112:115]\n\t" \
  "v_mfma_f32_16x16x32_bf16 v[116:119], a[12:15], a[76:79], v[116:119]\n\t" \
  "v_mfma_f32_16x16x32_bf16 v[120:123], a[16:19], a[76:79], v[120:123]\n\t" \
  "v_mfma_f32_16x16x32_bf16 v[124:127], a[20:23], a[76:79], v[124:127]\n\t" \
  "v_mfma_f32_16x16x32_bf16 v[128:131], a[24:27], a[76:79], v[128:131]\n\t" \
  "v_mfma_f32_16x16x32_bf16 v[132:135], a[28:31], a[76:79], v[132:135]\n\t" \
  "s_waitcnt vmcnt(34) lgkmcnt(4)\n\t" \
  "v_mfma_f32_16x16x32_bf16 v[8:11], a[32:35], a[80:83], v[8:11]\n\t" \
  "buffer_load_dwordx4 a[156:159], v202, s[44:47], s76 offen offset:3072 sc1\n\t" \
  "v_mfma_f32_16x16x32_bf16 v[12:15], a[36:39], a[80:83], v[12:15]\n\t" \
  "ds_read_b64_tr_b16 a[0:1], v200 offset:16384\n\t" \
  "v_mfma_f32_16x16x32_bf16 v[16:19], a[40:43], a[80:83], v[16:19]\n\t" \
  "ds_read_b64_tr_b16 a[2:3], v200 offset:20480\n\t" \
  "v_mfma_f32_16x16x32_bf16 v[20:23], a[44:47], a[80:83], v[20:23]\n\t" \
  "s_mov_b32 s76, 40960\n\t" \
  "ds_read_b64_tr_b16 a[4:5], v201 offset:16384\n\t" \
  "v_mfma_f32_16x16x32_bf16 v[24:27], a[48:51], a[80:83], v[24:27]\n\t" \
  "buffer_load_dwordx4 a[160:163], v202, s[44:47], s76 offen offset:0 sc1\n\t" \
  "v_mfma_f32_16x16x32_bf16 v[28:31], a[52:55], a[80:83], v[28:31]\n\t" \
  "ds_read_b64_tr_b16 a[6:7], v201 offset:20480\n\t" \
  "s_waitcnt lgkmcnt(4)\n\t" \
  "v_mfma_f32_16x16x32_bf16 v[32:35], a[56:59], a[80:83], v[32:35]\n\t" \
  "ds_read_b64_tr_b16 a[8:9], v200 offset:16896\n\t" \
  "v_mfma_f32_16x16x32_bf16 v[36:39], a[60:63], a[80:83], v[36:39]\n\t" \
  "ds_read_b64_tr_b16 a[10:11], v200 offset:20992\n\t" \
  "s_waitcnt vmcnt(35)\n\t" \
  "v_mfma_f32_16x16x32_bf16 v[40:43], a[32:35], a[84:87], v[40:43]\n\t" \
  "buffer_load_dwordx4 a[164:167], v202, s[44:47], s76 offen offset:1024 sc1\n\t" \
  "v_mfma_f32_16x16x32_bf16 v[44:47], a[36:39], a[84:87], v[44:47]\n\t" \
  "ds_read_b64_tr_b16 a[12:13], v201 offset:16896\n\t" \
  "v_mfma_f32_16x16x32_bf16 v[48:51], a[40:43], a[84:87], v[48:51]\n\t" \
  "ds_read_b64_tr_b16 a[14:15], v201 offset:20992\n\t" \
  "v_mfma_f32_16x16x32_bf16 v[52:55], a[44:47], a[84:87], v[52:55]\n\t" \
  "ds_read_b64_tr_b16 a[16:17], v200 offset:17408\n\t" \
  "v_mfma_f32_16x16x32_bf16 v[56:59], a[48:51], a[84:87], v[56:59]\n\t" \
  "buffer_load_dwordx4 a[168:171], v202, s[44:47], s76 offen offset:2048 sc1\n\t" \
  "v_mfma_f32_16x16x32_bf16 v[60:63], a[52:55], a[84:87], v[60:63]\n\t" \
  "ds_read_b64_tr_b16 a[18:19], v200 offset:21504\n\t" \
  "v_mfma_f32_16x16x32_bf16 v[64:67], a[56:59], a[84:87], v[64:67]\n\t" \
  "ds_read_b64_tr_b16 a[20:21], v201 offset:17408\n\t" \
  "v_mfma_f32_16x16x32_bf16 v[68:71], a[60:63], a[84:87], v[68:71]\n\t" \
  "ds_read_b64_tr_b16 a[22:23], v201 offset:21504\n\t" \
  "s_waitcnt vmcnt(36)\n\t" \
  "v_mfma_f32_16x16x32_bf16 v[72:75], a[32:35], a[88:91], v[72:75]\n\t" \
  "buffer_load_dwordx4 a[172:175], v202, s[44:47], s76 offen offset:3072 sc1\n\t" \
  "v_mfma_f32_16x16x32_bf16 v[76:79], a[36:39], a[88:91], v[76:79]\n\t" \
  "ds_read_b64_tr_b16 a[24:25], v200 offset:17920\n\t" \
  "v_mfma_f32_16x16x32_bf16 v[80:83], a[40:43], a[88:91], v[80:83]\n\t" \
  "ds_read_b64_tr_b16 a[26:27], v200 offset:22016\n\t" \
  "v_mfma_f32_16x16x32_bf16 v[84:87], a[44:47], a[88:91], v[84:87]\n\t" \
  "s_mov_b32 s76, 45056\n\t" \
  "ds_read_b64_tr_b16 a[28:29], v201 offset:17920\n\t" \
  "v_mfma_f32_16x16x32_bf16 v[88:91], a[48:51], a[88:91], v[88:91]\n\t" \
  "buffer_load_dwordx4 a[176:179], v202, s[44:47], s76 offen offset:0 sc1\n\t" \
  "v_mfma_f32_16x16x32_bf16 v[92:95], a[52:55], a[88:91], v[92:95]\n\t" \
  "ds_read_b64_tr_b16 a[30:31], v201 offset:22016\n\t" \
  "v_mfma_f32_16x16x32_bf16 v[96:99], a[56:59], a[88:91], v[96:99]\n\t" \
  "buffer_load_dwordx4 a[180:183], v202, s[44:47], s76 offen offset:1024 sc1\n\t" \
  "v_mfma_f32_16x16x32_bf16 v[100:103], a[60:63], a[88:91], v[100:103]\n\t" \
  "s_waitcnt vmcnt(37)\n\t" \
  "v_mfma_f32_16x16x32_bf16 v[104:107], a[32:35], a[92:95], v[104:107]\n\t" \
  "v_mfma_f32_16x16x32_bf16 v[108:111], a[36:39], a[92:95], v[108:111]\n\t" \
  "v_mfma_f32_16x16x32_bf16 v[112:115], a[40:43], a[92:95], v[112:115]\n\t" \
  "v_mfma_f32_16x16x32_bf16 v[116:119], a[44:47], a[92:95], v[116:119]\n\t" \
  "v_mfma_f32_16x16x32_bf16 v[120:123], a[48:51], a[92:95], v[120:123]\n\t" \
  "v_mfma_f32_16x16x32_bf16 v[124:127], a[52:55], a[92:95], v[124:127]\n\t" \
  "v_mfma_f32_16x16x32_bf16 v[128:131], a[56:59], a[92:95], v[128:131]\n\t" \
  "v_mfma_f32_16x16x32_bf16 v[132:135], a[60:63], a[92:95], v[132:135]\n\t" \
  "s_waitcnt vmcnt(35) lgkmcnt(3)\n\t" \
  "v_mfma_f32_16x16x32_bf16 v[8:11], a[0:3], a[96:99], v[8:11]\n\t" \
  "buffer_load_dwordx4 a[184:187], v202, s[44:47], s76 offen offset:2048 sc1\n\t" \
  "v_mfma_f32_16x16x32_bf16 v[12:15], a[4:7], a[96:99], v[12:15]\n\t" \
  "s_lshl_b32 s84, %[w], 10\n\t" \
  "s_add_u32 s84, s84, 147456\n\t" \
  "ds_read_b64_tr_b16 a[32:33], v200 offset:24576\n\t" \
  "v_add_u32 v220, s84, v202\n\t" \
  "v_mfma_f32_16x16x32_bf16 v[16:19], a[8:11], a[96:99], v[16:19]\n\t" \
  "ds_read_b64_tr_b16 a[34:35], v200 offset:28672\n\t" \
  "v_lshrrev_b32 v221, 4, v203\n\t" \
  "v_mfma_f32_16x16x32_bf16 v[20:23], a[12:15], a[96:99], v[20:23]\n\t" \
  "ds_read_b64_tr_b16 a[36:37], v201 offset:24576\n\t" \
  "v_lshl_add_u32 v221, v221, 4, s84\n\t" \
  "v_mfma_f32_16x16x32_bf16 v[24:27], a[16:19], a[96:99], v[24:27]\n\t" \
  "buffer_load_dwordx4 a[188:191], v202, s[44:47], s76 offen offset:3072 sc1\n\t" \
  "v_mfma_f32_16x16x32_bf16 v[28:31], a[20:23], a[96:99], v[28:31]\n\t" \
  "ds_read_b64_tr_b16 a[38:39], v201 offset:28672\n\t" \
  "v_mov_b32 v246, 1.0\n\t" \
  "s_waitcnt lgkmcnt(4)\n\t" \
  "v_mfma_f32_16x16x32_bf16 v[32:35], a[24:27], a[96:99], v[32:35]\n\t" \
  "s_mov_b32 s85, 0x1000\n\t" \
  "s_mul_i32 s40, %[ft], 1024\n\t" \
  "s_add_u32 s40, s40, 42139648\n\t" \
  "s_mov_b64 s[72:73], %[ws]\n\t" \
  "s_add_u32 s40, s40, s72\n\t" \
  "s_addc_u32 s41, s73, 0\n\t" \
  "s_lshl_b32 s84, %[ft], 7\n\t" \
  "s_add_u32 s84, s84, 8192\n\t" \
  "ds_read_b64_tr_b16 a[40:41], v200 offset:25088\n\t" \
  "v_mov_b32 v212, s84\n\t" \
  "v_mfma_f32_16x16x32_bf16 v[36:39], a[28:31], a[96:99], v[36:39]\n\t" \
  "s_mov_b32 s76, 49152\n\t" \
  "ds_read_b64_tr_b16 a[42:43], v200 offset:29184\n\t" \
  "s_waitcnt vmcnt(35)\n\t" \
  "v_mfma_f32_16x16x32_bf16 v[40:43], a[0:3], a[100:103], v[40:43]\n\t" \
  "buffer_load_dwordx2 a[192:193], v223, s[44:47], s76 offen offset:0 sc1\n\t" \
  "buffer_load_dwordx2 a[194:195], v223, s[44:47], s76 offen offset:512 sc1\n\t" \
  "v_mfma_f32_16x16x32_bf16 v[44:47], a[4:7], a[100:103], v[44:47]\n\t" \
  "ds_read_b64_tr_b16 a[44:45], v201 offset:25088\n\t" \
  "v_mfma_f32_16x16x32_bf16 v[48:51], a[8:11], a[100:103], v[48:51]\n\t" \
  "ds_read_b64_tr_b16 a[46:47], v201 offset:29184\n\t" \
  "v_mfma_f32_16x16x32_bf16 v[52:55], a[12:15], a[100:103], v[52:55]\n\t" \
  "ds_read_b64_tr_b16 a[48:49], v200 offset:25600\n\t" \
  "v_mfma_f32_16x16x32_bf16 v[56:59], a[16:19], a[100:103], v[56:59]\n\t" \
  "buffer_load_dwordx2 a[196:197], v223, s[44:47], s76 offen offset:1024 sc1\n\t" \
  "buffer_load_dwordx2 a[198:199], v223, s[44:47], s76 offen offset:1536 sc1\n\t" \
  "v_mfma_f32_16x16x32_bf16 v[60:63], a[20:23], a[100:103], v[60:63]\n\t" \
  "ds_read_b64_tr_b16 a[50:51], v200 offset:29696\n\t" \
  "v_mfma_f32_16x16x32_bf16 v[64:67], a[24:27], a[100:103], v[64:67]\n\t" \
  "ds_read_b64_tr_b16 a[52:53], v201 offset:25600\n\t" \
  "v_mfma_f32_16x16x32_bf16 v[68:71], a[28:31], a[100:103], v[68:71]\n\t" \
  "ds_read_b64_tr_b16 a[54:55], v201 offset:29696\n\t" \
  "s_waitcnt vmcnt(36)\n\t" \
  "v_mfma_f32_16x16x32_bf16 v[72:75], a[0:3], a[104:107], v[72:75]\n\t" \
  "buffer_load_dwordx2 a[200:201], v223, s[44:47], s76 offen offset:2048 sc1\n\t" \
  "buffer_load_dwordx2 a[202:203], v223, s[44:47], s76 offen offset:2560 sc1\n\t" \
  "v_mfma_f32_16x16x32_bf16 v[76:79], a[4:7], a[104:107], v[76:79]\n\t" \
  "ds_read_b64_tr_b16 a[56:57], v200 offset:26112\n\t" \
  "v_mfma_f32_16x16x32_bf16 v[80:83], a[8:11], a[104:107], v[80:83]\n\t" \
  "ds_read_b64_tr_b16 a[58:59], v200 offset:30208\n\t" \
  "v_mfma_f32_16x16x32_bf16 v[84:87], a[12:15], a[104:107], v[84:87]\n\t" \
  "ds_read_b64_tr_b16 a[60:61], v201 offset:26112\n\t" \
  "v_mfma_f32_16x16x32_bf16 v[88:91], a[16:19], a[104:107], v[88:91]\n\t" \
  "buffer_load_dwordx2 a[204:205], v223, s[44:47], s76 offen offset:3072 sc1\n\t" \
  "buffer_load_dwordx2 a[206:207], v223, s[44:47], s76 offen offset:3584 sc1\n\t" \
  "v_mfma_f32_16x16x32_bf16 v[92:95], a[20:23], a[104:107], v[92:95]\n\t" \
  "s_mov_b32 s76, 53248\n\t" \
  "ds_read_b64_tr_b16 a[62:63], v201 offset:30208\n\t" \
  "v_mfma_f32_16x16x32_bf16 v[96:99], a[24:27], a[104:107], v[96:99]\n\t" \
  "buffer_load_dwordx4 a[208:211], v202, s[44:47], s76 offen offset:0 sc1\n\t" \
  "v_mfma_f32_16x16x32_bf16 v[100:103], a[28:31], a[104:107], v[100:103]\n\t" \
  "s_waitcnt vmcnt(39)\n\t" \
  "v_mfma_f32_16x16x32_bf16 v[104:107], a[0:3], a[108:111], v[104:107]\n\t" \
  "v_mfma_f32_16x16x32_bf16 v[108:111], a[4:7], a[108:111], v[108:111]\n\t" \
  "v_mfma_f32_16x16x32_bf16 v[112:115], a[8:11], a[108:111], v[112:115]\n\t" \
  "v_mfma_f32_16x16x32_bf16 v[116:119], a[12:15], a[108:111], v[116:119]\n\t" \
  "v_mfma_f32_16x16x32_bf16 v[120:123], a[16:19], a[108:111], v[120:123]\n\t" \
  "v_mfma_f32_16x16x32_bf16 v[124:127], a[20:23], a[108:111], v[124:127]\n\t" \
  "v_add_u32 v200, 0x8000, v200\n\t" \
  "v_mfma_f32_16x16x32_bf16 v[128:131], a[24:27], a[108:111], v[128:131]\n\t" \
  "v_and_b32 v200, 0x1ffff, v200\n\t" \
  "v_mfma_f32_16x16x32_bf16 v[132:135], a[28:31], a[108:111], v[132:135]\n\t" \
  "v_xor_b32 v201, 32, v200\n\t" \
  "s_waitcnt vmcnt(36) lgkmcnt(1)\n\t" \
  "v_mfma_f32_16x16x32_bf16 v[8:11], a[32:35], a[112:115], v[8:11]\n\t" \
  "s_waitcnt vmcnt(25)\n\t" \
  "s_barrier\n\t" \
  "s_add_u32 m0, s80, 0\n\t" \
  "s_mov_b32 s74, 1769472\n\t" \
  "buffer_load_dwordx4 v216, s[56:59], s74 offen offset:0 lds\n\t" \
  "v_mfma_f32_16x16x32_bf16 v[12:15], a[36:39], a[112:115], v[12:15]\n\t" \
  "ds_read_b64_tr_b16 a[0:1], v200 offset:0\n\t" \
  "v_mfma_f32_16x16x32_bf16 v[16:19], a[40:43], a[112:115], v[16:19]\n\t" \
  "ds_read_b64_tr_b16 a[2:3], v200 offset:4096\n\t" \
  "v_mfma_f32_16x16x32_bf16 v[20:23], a[44:47], a[112:115], v[20:23]\n\t" \
  "ds_read_b64_tr_b16 a[4:5], v201 offset:0\n\t" \
  "v_mfma_f32_16x16x32_bf16 v[24:27], a[48:51], a[112:115], v[24:27]\n\t" \
  "buffer_load_dwordx4 a[212:215], v202, s[44:47], s76 offen offset:1024 sc1\n\t" \
  "v_mfma_f32_16x16x32_bf16 v[28:31], a[52:55], a[112:115], v[28:31]\n\t" \
  "ds_read_b64_tr_b16 a[6:7], v201 offset:4096\n\t" \
  "v_mfma_f32_16x16x32_bf16 v[32:35], a[56:59], a[112:115], v[32:35]\n\t" \
  "ds_read_b64_tr_b16 a[8:9], v200 offset:512\n\t" \
  "s_waitcnt lgkmcnt(5)\n\t" \
  "v_mfma_f32_16x16x32_bf16 v[36:39], a[60:63], a[112:115], v[36:39]\n\t" \
  "ds_read_b64_tr_b16 a[10:11], v200 offset:4608\n\t" \
  "v_mfma_f32_16x16x32_bf16 v[40:43], a[32:35], a[116:119], v[40:43]\n\t" \
  "buffer_load_dwordx4 v217, s[60:63], s74 offen offset:1024 lds\n\t" \
  "v_mfma_f32_16x16x32_bf16 v[44:47], a[36:39], a[116:119], v[44:47]\n\t" \
  "ds_read_b64_tr_b16 a[12:13], v201 offset:512\n\t" \
  "v_mfma_f32_16x16x32_bf16 v[48:51], a[40:43], a[116:119], v[48:51]\n\t" \
  "ds_read_b64_tr_b16 a[14:15], v201 offset:4608\n\t" \
  "v_mfma_f32_16x16x32_bf16 v[52:55], a[44:47], a[116:119], v[52:55]\n\t" \
  "ds_read_b64_tr_b16 a[16:17], v200 offset:1024\n\t" \
  "v_mfma_f32_16x16x32_bf16 v[56:59], a[48:51], a[116:119], v[56:59]\n\t" \
  "buffer_load_dwordx4 a[216:219], v202, s[44:47], s76 offen offset:2048 sc1\n\t" \
  "v_mfma_f32_16x16x32_bf16 v[60:63], a[52:55], a[116:119], v[60:63]\n\t" \
  "ds_read_b64_tr_b16 a[18:19], v200 offset:5120\n\t" \
  "v_mfma_f32_16x16x32_bf16 v[64:67], a[56:59], a[116:119], v[64:67]\n\t" \
  "ds_read_b64_tr_b16 a[20:21], v201 offset:1024\n\t" \
  "v_mfma_f32_16x16x32_bf16 v[68:71], a[60:63], a[116:119], v[68:71]\n\t" \
  "ds_read_b64_tr_b16 a[22:23], v201 offset:5120\n\t" \
  "v_mfma_f32_16x16x32_bf16 v[72:75], a[32:35], a[120:123], v[72:75]\n\t" \
  "buffer_load_dwordx4 v218, s[56:59], s74 offen offset:2048 lds\n\t" \
  "v_mfma_f32_16x16x32_bf16 v[76:79], a[36:39], a[120:123], v[76:79]\n\t" \
  "ds_read_b64_tr_b16 a[24:25], v200 offset:1536\n\t" \
  "v_mfma_f32_16x16x32_bf16 v[80:83], a[40:43], a[120:123], v[80:83]\n\t" \
  "ds_read_b64_tr_b16 a[26:27], v200 offset:5632\n\t" \
  "v_mfma_f32_16x16x32_bf16 v[84:87], a[44:47], a[120:123], v[84:87]\n\t" \
  "ds_read_b64_tr_b16 a[28:29], v201 offset:1536\n\t" \
  "v_mfma_f32_16x16x32_bf16 v[88:91], a[48:51], a[120:123], v[88:91]\n\t" \
  "buffer_load_dwordx4 a[220:223], v202, s[44:47], s76 offen offset:3072 sc1\n\t" \
  "v_mfma_f32_16x16x32_bf16 v[92:95], a[52:55], a[120:123], v[92:95]\n\t" \
  "ds_read_b64_tr_b16 a[30:31], v201 offset:5632\n\t" \
  "v_mfma_f32_16x16x32_bf16 v[96:99], a[56:59], a[120:123], v[96:99]\n\t" \
  "buffer_load_dwordx4 v219, s[60:63], s74 offen offset:3072 lds\n\t" \
  "v_mfma_f32_16x16x32_bf16 v[100:103], a[60:63], a[120:123], v[100:103]\n\t" \
  "v_mfma_f32_16x16x32_bf16 v[104:107], a[32:35], a[124:127], v[104:107]\n\t" \
  "v_mfma_f32_16x16x32_bf16 v[108:111], a[36:39], a[124:127], v[108:111]\n\t" \
  "v_mfma_f32_16x16x32_bf16 v[112:115], a[40:43], a[124:127], v[112:115]\n\t" \
  "v_mfma_f32_16x16x32_bf16 v[116:119], a[44:47], a[124:127], v[116:119]\n\t" \
  "v_mfma_f32_16x16x32_bf16 v[120:123], a[48:51], a[124:127], v[120:123]\n\t" \
  "v_mfma_f32_16x16x32_bf16 v[124:127], a[52:55], a[124:127], v[124:127]\n\t" \
  "v_mfma_f32_16x16x32_bf16 v[128:131], a[56:59], a[124:127], v[128:131]\n\t" \
  "v_mfma_f32_16x16x32_bf16 v[132:135], a[60:63], a[124:127], v[132:135]\n\t" \
  "s_waitcnt vmcnt(31) lgkmcnt(4)\n\t" \
  "v_mfma_f32_16x16x32_bf16 v[8:11], a[0:3], a[128:131], v[8:11]\n\t" \
  "s_add_u32 m0, s80, 16384\n\t" \
  "s_mov_b32 s74, 1990656\n\t" \
  "buffer_load_dwordx4 v216, s[56:59], s74 offen offset:0 lds\n\t" \
  "v_mfma_f32_16x16x32_bf16 v[12:15], a[4:7], a[128:131], v[12:15]\n\t" \
  "ds_read_b64_tr_b16 a[32:33], v200 offset:8192\n\t" \
  "v_mfma_f32_16x16x32_bf16 v[16:19], a[8:11], a[128:131], v[16:19]\n\t" \
  "ds_read_b64_tr_b16 a[34:35], v200 offset:12288\n\t" \
  "v_mfma_f32_16x16x32_bf16 v[20:23], a[12:15], a[128:131], v[20:23]\n\t" \
  "s_mov_b32 s76, 57344\n\t" \
  "ds_read_b64_tr_b16 a[36:37], v201 offset:8192\n\t" \
  "v_mfma_f32_16x16x32_bf16 v[24:27], a[16:19], a[128:131], v[24:27]\n\t" \
  "buffer_load_dwordx4 a[224:227], v202, s[44:47], s76 offen offset:0 sc1\n\t" \
  "v_mfma_f32_16x16x32_bf16 v[28:31], a[20:23], a[128:131], v[28:31]\n\t" \
  "ds_read_b64_tr_b16 a[38:39], v201 offset:12288\n\t" \
  "s_waitcnt lgkmcnt(4)\n\t" \
  "v_mfma_f32_16x16x32_bf16 v[32:35], a[24:27], a[128:131], v[32:35]\n\t" \
  "ds_read_b64_tr_b16 a[40:41], v200 offset:8704\n\t" \
  "v_mfma_f32_16x16x32_bf16 v[36:39], a[28:31], a[128:131], v[36:39]\n\t" \
  "ds_read_b64_tr_b16 a[42:43], v200 offset:12800\n\t" \
  "s_waitcnt vmcnt(32)\n\t" \
  "v_mfma_f32_16x16x32_bf16 v[40:43], a[0:3], a[132:135], v[40:43]\n\t" \
  "buffer_load_dwordx4 v217, s[60:63], s74 offen offset:1024 lds\n\t" \
  "v_mfma_f32_16x16x32_bf16 v[44:47], a[4:7], a[132:135], v[44:47]\n\t" \
  "ds_read_b64_tr_b16 a[44:45], v201 offset:8704\n\t" \
  "v_mfma_f32_16x16x32_bf16 v[48:51], a[8:11], a[132:135], v[48:51]\n\t" \
  "ds_read_b64_tr_b16 a[46:47], v201 offset:12800\n\t" \
  "v_mfma_f32_16x16x32_bf16 v[52:55], a[12:15], a[132:135], v[52:55]\n\t" \
  "ds_read_b64_tr_b16 a[48:49], v200 offset:9216\n\t" \
  "v_mfma_f32_16x16x32_bf16 v[56:59], a[16:19], a[132:135], v[56:59]\n\t" \
  "buffer_load_dwordx4 a[228:231], v202, s[44:47], s76 offen offset:1024 sc1\n\t" \
  "v_mfma_f32_16x16x32_bf16 v[60:63], a[20:23], a[132:135], v[60:63]\n\t" \
  "ds_read_b64_tr_b16 a[50:51], v200 offset:13312\n\t" \
  "v_mfma_f32_16x16x32_bf16 v[64:67], a[24:27], a[132:135], v[64:67]\n\t" \
  "ds_read_b64_tr_b16 a[52:53], v201 offset:9216\n\t" \
  "v_mfma_f32_16x16x32_bf16 v[68:71], a[28:31], a[132:135], v[68:71]\n\t" \
  "ds_read_b64_tr_b16 a[54:55], v201 offset:13312\n\t" \
  "s_waitcnt vmcnt(33)\n\t" \
  "v_mfma_f32_16x16x32_bf16 v[72:75], a[0:3], a[136:139], v[72:75]\n\t" \
  "buffer_load_dwordx4 v218, s[56:59], s74 offen offset:2048 lds\n\t" \
  "v_mfma_f32_16x16x32_bf16 v[76:79], a[4:7], a[136:139], v[76:79]\n\t" \
  "ds_read_b64_tr_b16 a[56:57], v200 offset:9728\n\t" \
  "v_mfma_f32_16x16x32_bf16 v[80:83], a[8:11], a[136:139], v[80:83]\n\t" \
  "ds_read_b64_tr_b16 a[58:59], v200 offset:13824\n\t" \
  "v_mfma_f32_16x16x32_bf16 v[84:87], a[12:15], a[136:139], v[84:87]\n\t" \
  "ds_read_b64_tr_b16 a[60:61], v201 offset:9728\n\t" \
  "v_mfma_f32_16x16x32_bf16 v[88:91], a[16:19], a[136:139], v[88:91]\n\t" \
  "buffer_load_dwordx4 a[232:235], v202, s[44:47], s76 offen offset:2048 sc1\n\t" \
  "v_mfma_f32_16x16x32_bf16 v[92:95], a[20:23], a[136:139], v[92:95]\n\t" \
  "ds_read_b64_tr_b16 a[62:63], v201 offset:13824\n\t" \
  "v_mfma_f32_16x16x32_bf16 v[96:99], a[24:27], a[136:139], v[96:99]\n\t" \
  "buffer_load_dwordx4 v219, s[60:63], s74 offen offset:3072 lds\n\t" \
  "v_mfma_f32_16x16x32_bf16 v[100:103], a[28:31], a[136:139], v[100:103]\n\t" \
  "s_waitcnt vmcnt(35)\n\t" \
  "v_mfma_f32_16x16x32_bf16 v[104:107], a[0:3], a[140:143], v[104:107]\n\t" \
  "v_mfma_f32_16x16x32_bf16 v[108:111], a[4:7], a[140:143], v[108:111]\n\t" \
  "v_mfma_f32_16x16x32_bf16 v[112:115], a[8:11], a[140:143], v[112:115]\n\t" \
  "v_mfma_f32_16x16x32_bf16 v[116:119], a[12:15], a[140:143], v[116:119]\n\t" \
  "v_mfma_f32_16x16x32_bf16 v[120:123], a[16:19], a[140:143], v[120:123]\n\t" \
  "v_mfma_f32_16x16x32_bf16 v[124:127], a[20:23], a[140:143], v[124:127]\n\t" \
  "v_mfma_f32_16x16x32_bf16 v[128:131], a[24:27], a[140:143], v[128:131]\n\t" \
  "v_mfma_f32_16x16x32_bf16 v[132:135], a[28:31], a[140:143], v[132:135]\n\t" \
  "s_waitcnt vmcnt(34) lgkmcnt(4)\n\t" \
  "v_mfma_f32_16x16x32_bf16 v[8:11], a[32:35], a[144:147], v[8:11]\n\t" \
  "buffer_load_dwordx4 a[236:239], v202, s[44:47], s76 offen offset:3072 sc1\n\t" \
  "v_mfma_f32_16x16x32_bf16 v[12:15], a[36:39], a[144:147], v[12:15]\n\t" \
  "ds_read_b64_tr_b16 a[0:1], v200 offset:16384\n\t" \
  "v_mfma_f32_16x16x32_bf16 v[16:19], a[40:43], a[144:147], v[16:19]\n\t" \
  "ds_read_b64_tr_b16 a[2:3], v200 offset:20480\n\t" \
  "v_mfma_f32_16x16x32_bf16 v[20:23], a[44:47], a[144:147], v[20:23]\n\t" \
  "s_mov_b32 s76, 61440\n\t" \
  "ds_read_b64_tr_b16 a[4:5], v201 offset:16384\n\t" \
  "v_mfma_f32_16x16x32_bf16 v[24:27], a[48:51], a[144:147], v[24:27]\n\t" \
  "buffer_load_dwordx4 a[240:243], v202, s[44:47], s76 offen offset:0 sc1\n\t" \
  "v_mfma_f32_16x16x32_bf16 v[28:31], a[52:55], a[144:147], v[28:31]\n\t" \
  "ds_read_b64_tr_b16 a[6:7], v201 offset:20480\n\t" \
  "s_waitcnt lgkmcnt(4)\n\t" \
  "v_mfma_f32_16x16x32_bf16 v[32:35], a[56:59], a[144:147], v[32:35]\n\t" \
  "ds_read_b64_tr_b16 a[8:9], v200 offset:16896\n\t" \
  "v_mfma_f32_16x16x32_bf16 v[36:39], a[60:63], a[144:147], v[36:39]\n\t" \
  "ds_read_b64_tr_b16 a[10:11], v200 offset:20992\n\t" \
  "s_waitcnt vmcnt(35)\n\t" \
  "v_mfma_f32_16x16x32_bf16 v[40:43], a[32:35], a[148:151], v[40:43]\n\t" \
  "buffer_load_dwordx4 a[244:247], v202, s[44:47], s76 offen offset:1024 sc1\n\t" \
  "v_mfma_f32_16x16x32_bf16 v[44:47], a[36:39], a[148:151], v[44:47]\n\t" \
  "ds_read_b64_tr_b16 a[12:13], v201 offset:16896\n\t" \
  "v_mfma_f32_16x16x32_bf16 v[48:51], a[40:43], a[148:151], v[48:51]\n\t" \
  "ds_read_b64_tr_b16 a[14:15], v201 offset:20992\n\t" \
  "v_mfma_f32_16x16x32_bf16 v[52:55], a[44:47], a[148:151], v[52:55]\n\t" \
  "ds_read_b64_tr_b16 a[16:17], v200 offset:17408\n\t" \
  "v_mfma_f32_16x16x32_bf16 v[56:59], a[48:51], a[148:151], v[56:59]\n\t" \
  "buffer_load_dwordx4 a[248:251], v202, s[44:47], s76 offen offset:2048 sc1\n\t" \
  "v_mfma_f32_16x16x32_bf16 v[60:63], a[52:55], a[148:151], v[60:63]\n\t" \
  "ds_read_b64_tr_b16 a[18:19], v200 offset:21504\n\t" \
  "v_mfma_f32_16x16x32_bf16 v[64:67], a[56:59], a[148:151], v[64:67]\n\t" \
  "ds_read_b64_tr_b16 a[20:21], v201 offset:17408\n\t" \
  "v_mfma_f32_16x16x32_bf16 v[68:71], a[60:63], a[148:151], v[68:71]\n\t" \
  "ds_read_b64_tr_b16 a[22:23], v201 offset:21504\n\t" \
  "s_waitcnt vmcnt(36)\n\t" \
  "v_mfma_f32_16x16x32_bf16 v[72:75], a[32:35], a[152:155], v[72:75]\n\t" \
  "buffer_load_dwordx4 a[252:255], v202, s[44:47], s76 offen offset:3072 sc1\n\t" \
  "v_mfma_f32_16x16x32_bf16 v[76:79], a[36:39], a[152:155], v[76:79]\n\t" \
  "ds_read_b64_tr_b16 a[24:25], v200 offset:17920\n\t" \
  "v_mfma_f32_16x16x32_bf16 v[80:83], a[40:43], a[152:155], v[80:83]\n\t" \
  "ds_read_b64_tr_b16 a[26:27], v200 offset:22016\n\t" \
  "v_mfma_f32_16x16x32_bf16 v[84:87], a[44:47], a[152:155], v[84:87]\n\t" \
  "s_mov_b32 s76, 65536\n\t" \
  "ds_read_b64_tr_b16 a[28:29], v201 offset:17920\n\t" \
  "v_mfma_f32_16x16x32_bf16 v[88:91], a[48:51], a[152:155], v[88:91]\n\t" \
  "buffer_load_dwordx4 a[64:67], v202, s[44:47], s76 offen offset:0 sc1\n\t" \
  "v_mfma_f32_16x16x32_bf16 v[92:95], a[52:55], a[152:155], v[92:95]\n\t" \
  "ds_read_b64_tr_b16 a[30:31], v201 offset:22016\n\t" \
  "v_mfma_f32_16x16x32_bf16 v[96:99], a[56:59], a[152:155], v[96:99]\n\t" \
  "buffer_load_dwordx4 a[68:71], v202, s[44:47], s76 offen offset:1024 sc1\n\t" \
  "v_mfma_f32_16x16x32_bf16 v[100:103], a[60:63], a[152:155], v[100:103]\n\t" \
  "s_waitcnt vmcnt(38)\n\t" \
  "v_mfma_f32_16x16x32_bf16 v[104:107], a[32:35], a[156:159], v[104:107]\n\t" \
  "v_mfma_f32_16x16x32_bf16 v[108:111], a[36:39], a[156:159], v[108:111]\n\t" \
  "v_mfma_f32_16x16x32_bf16 v[112:115], a[40:43], a[156:159], v[112:115]\n\t" \
  "v_mfma_f32_16x16x32_bf16 v[116:119], a[44:47], a[156:159], v[116:119]\n\t" \
  "v_mfma_f32_16x16x32_bf16 v[120:123], a[48:51], a[156:159], v[120:123]\n\t" \
  "v_mfma_f32_16x16x32_bf16 v[124:127], a[52:55], a[156:159], v[124:127]\n\t" \
  "v_mfma_f32_16x16x32_bf16 v[128:131], a[56:59], a[156:159], v[128:131]\n\t" \
  "v_mfma_f32_16x16x32_bf16 v[132:135], a[60:63], a[156:159], v[132:135]\n\t" \
  "s_waitcnt vmcnt(37) lgkmcnt(3)\n\t" \
  "v_mfma_f32_16x16x32_bf16 v[8:11], a[0:3], a[160:163], v[8:11]\n\t" \
  "buffer_load_dwordx2 v[224:225], v240, s[64:67], 0 offen offset:0 sc1\n\t" \
  "v_mfma_f32_16x16x32_bf16 v[12:15], a[4:7], a[160:163], v[12:15]\n\t" \
  "ds_read_b64_tr_b16 a[32:33], v200 offset:24576\n\t" \
  "v_mfma_f32_16x16x32_bf16 v[16:19], a[8:11], a[160:163], v[16:19]\n\t" \
  "ds_read_b64_tr_b16 a[34:35], v200 offset:28672\n\t" \
  "v_mfma_f32_16x16x32_bf16 v[20:23], a[12:15], a[160:163], v[20:23]\n\t" \
  "ds_read_b64_tr_b16 a[36:37], v201 offset:24576\n\t" \
  "v_mfma_f32_16x16x32_bf16 v[24:27], a[16:19], a[160:163], v[24:27]\n\t" \
  "buffer_load_dwordx2 v[228:229], v240, s[64:67], 0 offen offset:128 sc1\n\t" \
  "v_mfma_f32_16x16x32_bf16 v[28:31], a[20:23], a[160:163], v[28:31]\n\t" \
  "ds_read_b64_tr_b16 a[38:39], v201 offset:28672\n\t" \
  "s_waitcnt lgkmcnt(4)\n\t" \
  "v_mfma_f32_16x16x32_bf16 v[32:35], a[24:27], a[160:163], v[32:35]\n\t" \
  "ds_read_b64_tr_b16 a[40:41], v200 offset:25088\n\t" \
  "v_mfma_f32_16x16x32_bf16 v[36:39], a[28:31], a[160:163], v[36:39]\n\t" \
  "ds_read_b64_tr_b16 a[42:43], v200 offset:29184\n\t" \
  "s_waitcnt vmcnt(38)\n\t" \
  "v_mfma_f32_16x16x32_bf16 v[40:43], a[0:3], a[164:167], v[40:43]\n\t" \
  "buffer_load_dwordx4 a[72:75], v202, s[44:47], s76 offen offset:2048 sc1\n\t" \
  "v_mfma_f32_16x16x32_bf16 v[44:47], a[4:7], a[164:167], v[44:47]\n\t" \
  "ds_read_b64_tr_b16 a[44:45], v201 offset:25088\n\t" \
  "v_mfma_f32_16x16x32_bf16 v[48:51], a[8:11], a[164:167], v[48:51]\n\t" \
  "ds_read_b64_tr_b16 a[46:47], v201 offset:29184\n\t" \
  "v_mfma_f32_16x16x32_bf16 v[52:55], a[12:15], a[164:167], v[52:55]\n\t" \
  "ds_read_b64_tr_b16 a[48:49], v200 offset:25600\n\t" \
  "v_mfma_f32_16x16x32_bf16 v[56:59], a[16:19], a[164:167], v[56:59]\n\t" \
  "buffer_load_dwordx2 v[232:233], v240, s[64:67], 0 offen offset:256 sc1\n\t" \
  "v_mfma_f32_16x16x32_bf16 v[60:63], a[20:23], a[164:167], v[60:63]\n\t" \
  "ds_read_b64_tr_b16 a[50:51], v200 offset:29696\n\t" \
  "v_mfma_f32_16x16x32_bf16 v[64:67], a[24:27], a[164:167], v[64:67]\n\t" \
  "ds_read_b64_tr_b16 a[52:53], v201 offset:25600\n\t" \
  "v_mfma_f32_16x16x32_bf16 v[68:71], a[28:31], a[164:167], v[68:71]\n\t" \
  "ds_read_b64_tr_b16 a[54:55], v201 offset:29696\n\t" \
  "s_waitcnt vmcnt(39)\n\t" \
  "v_mfma_f32_16x16x32_bf16 v[72:75], a[0:3], a[168:171], v[72:75]\n\t" \
  "buffer_load_dwordx4 a[76:79], v202, s[44:47], s76 offen offset:3072 sc1\n\t" \
  "v_mfma_f32_16x16x32_bf16 v[76:79], a[4:7], a[168:171], v[76:79]\n\t" \
  "ds_read_b64_tr_b16 a[56:57], v200 offset:26112\n\t" \
  "v_mfma_f32_16x16x32_bf16 v[80:83], a[8:11], a[168:171], v[80:83]\n\t" \
  "ds_read_b64_tr_b16 a[58:59], v200 offset:30208\n\t" \
  "v_mfma_f32_16x16x32_bf16 v[84:87], a[12:15], a[168:171], v[84:87]\n\t" \
  "ds_read_b64_tr_b16 a[60:61], v201 offset:26112\n\t" \
  "v_mfma_f32_16x16x32_bf16 v[88:91], a[16:19], a[168:171], v[88:91]\n\t" \
  "buffer_load_dwordx2 v[236:237], v240, s[64:67], 0 offen offset:384 sc1\n\t" \
  "v_mfma_f32_16x16x32_bf16 v[92:95], a[20:23], a[168:171], v[92:95]\n\t" \
  "s_mov_b32 s76, 69632\n\t" \
  "ds_read_b64_tr_b16 a[62:63], v201 offset:30208\n\t" \
  "v_mfma_f32_16x16x32_bf16 v[96:99], a[24:27], a[168:171], v[96:99]\n\t" \
  "buffer_load_dwordx2 a[80:81], v223, s[44:47], s76 offen offset:0 sc1\n\t" \
  "buffer_load_dwordx2 a[82:83], v223, s[44:47], s76 offen offset:512 sc1\n\t" \
  "v_mfma_f32_16x16x32_bf16 v[100:103], a[28:31], a[168:171], v[100:103]\n\t" \
  "s_waitcnt vmcnt(42)\n\t" \
  "v_mfma_f32_16x16x32_bf16 v[104:107], a[0:3], a[172:175], v[104:107]\n\t" \
  "global_load_dword v245, v244, s[68:69] sc1\n\t" \
  "v_mfma_f32_16x16x32_bf16 v[108:111], a[4:7], a[172:175], v[108:111]\n\t" \
  "v_mfma_f32_16x16x32_bf16 v[112:115], a[8:11], a[172:175], v[112:115]\n\t" \
  "v_mfma_f32_16x16x32_bf16 v[116:119], a[12:15], a[172:175], v[116:119]\n\t" \
  "v_mfma_f32_16x16x32_bf16 v[120:123], a[16:19], a[172:175], v[120:123]\n\t" \
  "v_mfma_f32_16x16x32_bf16 v[124:127], a[20:23], a[172:175], v[124:127]\n\t" \
  "v_add_u32 v200, 0x8000, v200\n\t" \
  "v_mfma_f32_16x16x32_bf16 v[128:131], a[24:27], a[172:175], v[128:131]\n\t" \
  "v_and_b32 v200, 0x1ffff, v200\n\t" \
  "v_mfma_f32_16x16x32_bf16 v[132:135], a[28:31], a[172:175], v[132:135]\n\t" \
  "v_xor_b32 v201, 32, v200\n\t" \
  "s_waitcnt vmcnt(42) lgkmcnt(1)\n\t" \
  "v_mfma_f32_16x16x32_bf16 v[8:11], a[32:35], a[176:179], v[8:11]\n\t" \
  "buffer_load_dwordx2 v[226:227], v240, s[64:67], 0 offen offset:512 sc1\n\t" \
  "v_mfma_f32_16x16x32_bf16 v[12:15], a[36:39], a[176:179], v[12:15]\n\t" \
  "ds_read_b64_tr_b16 a[0:1], v200 offset:0\n\t" \
  "v_mfma_f32_16x16x32_bf16 v[16:19], a[40:43], a[176:179], v[16:19]\n\t" \
  "ds_read_b64_tr_b16 a[2:3], v200 offset:4096\n\t" \
  "v_mfma_f32_16x16x32_bf16 v[20:23], a[44:47], a[176:179], v[20:23]\n\t" \
  "ds_read_b64_tr_b16 a[4:5], v201 offset:0\n\t" \
  "v_mfma_f32_16x16x32_bf16 v[24:27], a[48:51], a[176:179], v[24:27]\n\t" \
  "buffer_load_dwordx2 a[84:85], v223, s[44:47], s76 offen offset:1024 sc1\n\t" \
  "buffer_load_dwordx2 a[86:87], v223, s[44:47], s76 offen offset:1536 sc1\n\t" \
  "v_mfma_f32_16x16x32_bf16 v[28:31], a[52:55], a[176:179], v[28:31]\n\t" \
  "ds_read_b64_tr_b16 a[6:7], v201 offset:4096\n\t" \
  "v_mfma_f32_16x16x32_bf16 v[32:35], a[56:59], a[176:179], v[32:35]\n\t" \
  "ds_read_b64_tr_b16 a[8:9], v200 offset:512\n\t" \
  "s_waitcnt lgkmcnt(5)\n\t" \
  "v_mfma_f32_16x16x32_bf16 v[36:39], a[60:63], a[176:179], v[36:39]\n\t" \
  "ds_read_b64_tr_b16 a[10:11], v200 offset:4608\n\t" \
  "s_waitcnt vmcnt(44)\n\t" \
  "v_mfma_f32_16x16x32_bf16 v[40:43], a[32:35], a[180:183], v[40:43]\n\t" \
  "buffer_load_dwordx2 v[230:231], v240, s[64:67], 0 offen offset:640 sc1\n\t" \
  "v_mfma_f32_16x16x32_bf16 v[44:47], a[36:39], a[180:183], v[44:47]\n\t" \
  "ds_read_b64_tr_b16 a[12:13], v201 offset:512\n\t" \
  "v_mfma_f32_16x16x32_bf16 v[48:51], a[40:43], a[180:183], v[48:51]\n\t" \
  "ds_read_b64_tr_b16 a[14:15], v201 offset:4608\n\t" \
  "v_mfma_f32_16x16x32_bf16 v[52:55], a[44:47], a[180:183], v[52:55]\n\t" \
  "ds_read_b64_tr_b16 a[16:17], v200 offset:1024\n\t" \
  "v_mfma_f32_16x16x32_bf16 v[56:59], a[48:51], a[180:183], v[56:59]\n\t" \
  "buffer_load_dwordx2 a[88:89], v223, s[44:47], s76 offen offset:2048 sc1\n\t" \
  "buffer_load_dwordx2 a[90:91], v223, s[44:47], s76 offen offset:2560 sc1\n\t" \
  "v_mfma_f32_16x16x32_bf16 v[60:63], a[52:55], a[180:183], v[60:63]\n\t" \
  "ds_read_b64_tr_b16 a[18:19], v200 offset:5120\n\t" \
  "v_mfma_f32_16x16x32_bf16 v[64:67], a[56:59], a[180:183], v[64:67]\n\t" \
  "ds_read_b64_tr_b16 a[20:21], v201 offset:1024\n\t" \
  "v_mfma_f32_16x16x32_bf16 v[68:71], a[60:63], a[180:183], v[68:71]\n\t" \
  "ds_read_b64_tr_b16 a[22:23], v201 offset:5120\n\t" \
  "s_waitcnt vmcnt(46)\n\t" \
  "v_mfma_f32_16x16x32_bf16 v[72:75], a[32:35], a[184:187], v[72:75]\n\t" \
  "buffer_load_dwordx2 v[234:235], v240, s[64:67], 0 offen offset:768 sc1\n\t" \
  "v_mfma_f32_16x16x32_bf16 v[76:79], a[36:39], a[184:187], v[76:79]\n\t" \
  "ds_read_b64_tr_b16 a[24:25], v200 offset:1536\n\t" \
  "v_mfma_f32_16x16x32_bf16 v[80:83], a[40:43], a[184:187], v[80:83]\n\t" \
  "ds_read_b64_tr_b16 a[26:27], v200 offset:5632\n\t" \
  "v_mfma_f32_16x16x32_bf16 v[84:87], a[44:47], a[184:187], v[84:87]\n\t" \
  "ds_read_b64_tr_b16 a[28:29], v201 offset:1536\n\t" \
  "v_mfma_f32_16x16x32_bf16 v[88:91], a[48:51], a[184:187], v[88:91]\n\t" \
  "buffer_load_dwordx2 a[92:93], v223, s[44:47], s76 offen offset:3072 sc1\n\t" \
  "buffer_load_dwordx2 a[94:95], v223, s[44:47], s76 offen offset:3584 sc1\n\t" \
  "v_mfma_f32_16x16x32_bf16 v[92:95], a[52:55], a[184:187], v[92:95]\n\t" \
  "ds_read_b64_tr_b16 a[30:31], v201 offset:5632\n\t" \
  "v_mfma_f32_16x16x32_bf16 v[96:99], a[56:59], a[184:187], v[96:99]\n\t" \
  "buffer_load_dwordx2 v[238:239], v240, s[64:67], 0 offen offset:896 sc1\n\t" \
  "v_mfma_f32_16x16x32_bf16 v[100:103], a[60:63], a[184:187], v[100:103]\n\t" \
  "s_waitcnt vmcnt(49)\n\t" \
  "v_mfma_f32_16x16x32_bf16 v[104:107], a[32:35], a[188:191], v[104:107]\n\t" \
  "v_mfma_f32_16x16x32_bf16 v[108:111], a[36:39], a[188:191], v[108:111]\n\t" \
  "v_mfma_f32_16x16x32_bf16 v[112:115], a[40:43], a[188:191], v[112:115]\n\t" \
  "v_mfma_f32_16x16x32_bf16 v[116:119], a[44:47], a[188:191], v[116:119]\n\t" \
  "v_mfma_f32_16x16x32_bf16 v[120:123], a[48:51], a[188:191], v[120:123]\n\t" \
  "v_mfma_f32_16x16x32_bf16 v[124:127], a[52:55], a[188:191], v[124:127]\n\t" \
  "v_mfma_f32_16x16x32_bf16 v[128:131], a[56:59], a[188:191], v[128:131]\n\t" \
  "v_mfma_f32_16x16x32_bf16 v[132:135], a[60:63], a[188:191], v[132:135]\n\t" \
  "s_waitcnt vmcnt(47) lgkmcnt(3)\n\t" \
  "v_mfma_f32_16x16x32_bf16 v[8:11], a[0:3], a[192:195], v[8:11]\n\t" \
  "s_mov_b32 s76, 73728\n\t" \
  "buffer_load_dwordx4 a[96:99], v202, s[44:47], s76 offen offset:0 sc1\n\t" \
  "v_mfma_f32_16x16x32_bf16 v[12:15], a[4:7], a[192:195], v[12:15]\n\t" \
  "ds_read_b64_tr_b16 a[32:33], v200 offset:8192\n\t" \
  "s_waitcnt vmcnt(11)\n\t" \
  "v_cmp_ne_u32 vcc, %[tag], v245\n\t" \
  "s_cbranch_vccnz .Lpdspin%=\n\t" \
  ".Lpdback%=:\n\t" \
  "v_mfma_f32_16x16x32_bf16 v[16:19], a[8:11], a[192:195], v[16:19]\n\t" \
  "ds_read_b64_tr_b16 a[34:35], v200 offset:12288\n\t" \
  "v_mfma_f32_16x16x32_bf16 v[20:23], a[12:15], a[192:195], v[20:23]\n\t" \
  "ds_read_b64_tr_b16 a[36:37], v201 offset:8192\n\t" \
  "v_mfma_f32_16x16x32_bf16 v[24:27], a[16:19], a[192:195], v[24:27]\n\t" \
  "buffer_load_dwordx4 a[100:103], v202, s[44:47], s76 offen offset:1024 sc1\n\t" \
  "v_mfma_f32_16x16x32_bf16 v[28:31], a[20:23], a[192:195], v[28:31]\n\t" \
  "ds_read_b64_tr_b16 a[38:39], v201 offset:12288\n\t" \
  "s_waitcnt lgkmcnt(4)\n\t" \
  "v_mfma_f32_16x16x32_bf16 v[32:35], a[24:27], a[192:195], v[32:35]\n\t" \
  "ds_read_b64_tr_b16 a[40:41], v200 offset:8704\n\t" \
  "v_mfma_f32_16x16x32_bf16 v[36:39], a[28:31], a[192:195], v[36:39]\n\t" \
  "ds_read_b64_tr_b16 a[42:43], v200 offset:12800\n\t" \
  "v_mfma_f32_16x16x32_bf16 v[40:43], a[0:3], a[196:199], v[40:43]\n\t" \
  "buffer_load_dwordx4 a[104:107], v202, s[44:47], s76 offen offset:2048 sc1\n\t" \
  "v_mfma_f32_16x16x32_bf16 v[44:47], a[4:7], a[196:199], v[44:47]\n\t" \
  "ds_read_b64_tr_b16 a[44:45], v201 offset:8704\n\t" \
  "v_mfma_f32_16x16x32_bf16 v[48:51], a[8:11], a[196:199], v[48:51]\n\t" \
  "ds_read_b64_tr_b16 a[46:47], v201 offset:12800\n\t" \
  "v_mfma_f32_16x16x32_bf16 v[52:55], a[12:15], a[196:199], v[52:55]\n\t" \
  "ds_read_b64_tr_b16 a[48:49], v200 offset:9216\n\t" \
  "v_mfma_f32_16x16x32_bf16 v[56:59], a[16:19], a[196:199], v[56:59]\n\t" \
  "buffer_load_dwordx4 a[108:111], v202, s[44:47], s76 offen offset:3072 sc1\n\t" \
  "v_mfma_f32_16x16x32_bf16 v[60:63], a[20:23], a[196:199], v[60:63]\n\t" \
  "ds_read_b64_tr_b16 a[50:51], v200 offset:13312\n\t" \
  "v_mfma_f32_16x16x32_bf16 v[64:67], a[24:27], a[196:199], v[64:67]\n\t" \
  "ds_read_b64_tr_b16 a[52:53], v201 offset:9216\n\t" \
  "v_mfma_f32_16x16x32_bf16 v[68:71], a[28:31], a[196:199], v[68:71]\n\t" \
  "s_mov_b32 s76, 77824\n\t" \
  "ds_read_b64_tr_b16 a[54:55], v201 offset:13312\n\t" \
  "v_mfma_f32_16x16x32_bf16 v[72:75], a[0:3], a[200:203], v[72:75]\n\t" \
  "buffer_load_dwordx4 a[112:115], v202, s[44:47], s76 offen offset:0 sc1\n\t" \
  "v_mfma_f32_16x16x32_bf16 v[76:79], a[4:7], a[200:203], v[76:79]\n\t" \
  "ds_read_b64_tr_b16 a[56:57], v200 offset:9728\n\t" \
  "v_mfma_f32_16x16x32_bf16 v[80:83], a[8:11], a[200:203], v[80:83]\n\t" \
  "ds_read_b64_tr_b16 a[58:59], v200 offset:13824\n\t" \
  "v_mfma_f32_16x16x32_bf16 v[84:87], a[12:15], a[200:203], v[84:87]\n\t" \
  "ds_read_b64_tr_b16 a[60:61], v201 offset:9728\n\t" \
  "v_mfma_f32_16x16x32_bf16 v[88:91], a[16:19], a[200:203], v[88:91]\n\t" \
  "buffer_load_dwordx4 a[116:119], v202, s[44:47], s76 offen offset:1024 sc1\n\t" \
  "v_mfma_f32_16x16x32_bf16 v[92:95], a[20:23], a[200:203], v[92:95]\n\t" \
  "ds_read_b64_tr_b16 a[62:63], v201 offset:13824\n\t" \
  "v_mfma_f32_16x16x32_bf16 v[96:99], a[24:27], a[200:203], v[96:99]\n\t" \
  "buffer_load_dwordx4 a[120:123], v202, s[44:47], s76 offen offset:2048 sc1\n\t" \
  "v_mfma_f32_16x16x32_bf16 v[100:103], a[28:31], a[200:203], v[100:103]\n\t" \
  "v_mfma_f32_16x16x32_bf16 v[104:107], a[0:3], a[204:207], v[104:107]\n\t" \
  "v_mfma_f32_16x16x32_bf16 v[108:111], a[4:7], a[204:207], v[108:111]\n\t" \
  "v_mfma_f32_16x16x32_bf16 v[112:115], a[8:11], a[204:207], v[112:115]\n\t" \
  "v_mfma_f32_16x16x32_bf16 v[116:119], a[12:15], a[204:207], v[116:119]\n\t" \
  "v_mfma_f32_16x16x32_bf16 v[120:123], a[16:19], a[204:207], v[120:123]\n\t" \
  "v_mfma_f32_16x16x32_bf16 v[124:127], a[20:23], a[204:207], v[124:127]\n\t" \
  "v_mfma_f32_16x16x32_bf16 v[128:131], a[24:27], a[204:207], v[128:131]\n\t" \
  "v_mfma_f32_16x16x32_bf16 v[132:135], a[28:31], a[204:207], v[132:135]\n\t" \
  "s_waitcnt lgkmcnt(4)\n\t" \
  "v_mfma_f32_16x16x32_bf16 v[8:11], a[32:35], a[208:211], v[8:11]\n\t" \
  "buffer_load_dwordx4 a[124:127], v202, s[44:47], s76 offen offset:3072 sc1\n\t" \
  "v_mfma_f32_16x16x32_bf16 v[12:15], a[36:39], a[208:211], v[12:15]\n\t" \
  "ds_read_b64_tr_b16 a[0:1], v200 offset:16384\n\t" \
  "v_mfma_f32_16x16x32_bf16 v[16:19], a[40:43], a[208:211], v[16:19]\n\t" \
  "ds_read_b64_tr_b16 a[2:3], v200 offset:20480\n\t" \
  "v_mfma_f32_16x16x32_bf16 v[20:23], a[44:47], a[208:211], v[20:23]\n\t" \
  "ds_read_b64_tr_b16 a[4:5], v201 offset:16384\n\t" \
  "v_mfma_f32_16x16x32_bf16 v[24:27], a[48:51], a[208:211], v[24:27]\n\t" \
  "v_mfma_f32_16x16x32_bf16 v[28:31], a[52:55], a[208:211], v[28:31]\n\t" \
  "ds_read_b64_tr_b16 a[6:7], v201 offset:20480\n\t" \
  "s_waitcnt lgkmcnt(4)\n\t" \
  "v_mfma_f32_16x16x32_bf16 v[32:35], a[56:59], a[208:211], v[32:35]\n\t" \
  "ds_read_b64_tr_b16 a[8:9], v200 offset:16896\n\t" \
  "v_mfma_f32_16x16x32_bf16 v[36:39], a[60:63], a[208:211], v[36:39]\n\t" \
  "s_mov_b32 s88, 0\n\t" \
  "ds_read_b64_tr_b16 a[10:11], v200 offset:20992\n\t" \
  "v_mfma_f32_16x16x32_bf16 v[40:43], a[32:35], a[212:215], v[40:43]\n\t" \
  "buffer_load_dwordx4 a[208:211], v202, s[36:39], s88 offen offset:0 sc1\n\t" \
  "v_mfma_f32_16x16x32_bf16 v[44:47], a[36:39], a[212:215], v[44:47]\n\t" \
  "ds_read_b64_tr_b16 a[12:13], v201 offset:16896\n\t" \
  "v_mfma_f32_16x16x32_bf16 v[48:51], a[40:43], a[212:215], v[48:51]\n\t" \
  "ds_read_b64_tr_b16 a[14:15], v201 offset:20992\n\t" \
  "v_mfma_f32_16x16x32_bf16 v[52:55], a[44:47], a[212:215], v[52:55]\n\t" \
  "ds_read_b64_tr_b16 a[16:17], v200 offset:17408\n\t" \
  "v_mfma_f32_16x16x32_bf16 v[56:59], a[48:51], a[212:215], v[56:59]\n\t" \
  "v_mfma_f32_16x16x32_bf16 v[60:63], a[52:55], a[212:215], v[60:63]\n\t" \
  "ds_read_b64_tr_b16 a[18:19], v200 offset:21504\n\t" \
  "v_mfma_f32_16x16x32_bf16 v[64:67], a[56:59], a[212:215], v[64:67]\n\t" \
  "ds_read_b64_tr_b16 a[20:21], v201 offset:17408\n\t" \
  "v_mfma_f32_16x16x32_bf16 v[68:71], a[60:63], a[212:215], v[68:71]\n\t" \
  "ds_read_b64_tr_b16 a[22:23], v201 offset:21504\n\t" \
  "v_mfma_f32_16x16x32_bf16 v[72:75], a[32:35], a[216:219], v[72:75]\n\t" \
  "buffer_load_dwordx4 a[212:215], v202, s[36:39], s88 offen offset:1024 sc1\n\t" \
  "v_mfma_f32_16x16x32_bf16 v[76:79], a[36:39], a[216:219], v[76:79]\n\t" \
  "ds_read_b64_tr_b16 a[24:25], v200 offset:17920\n\t" \
  "v_mfma_f32_16x16x32_bf16 v[80:83], a[40:43], a[216:219], v[80:83]\n\t" \
  "ds_read_b64_tr_b16 a[26:27], v200 offset:22016\n\t" \
  "v_mfma_f32_16x16x32_bf16 v[84:87], a[44:47], a[216:219], v[84:87]\n\t" \
  "ds_read_b64_tr_b16 a[28:29], v201 offset:17920\n\t" \
  "v_mfma_f32_16x16x32_bf16 v[88:91], a[48:51], a[216:219], v[88:91]\n\t" \
  "v_mfma_f32_16x16x32_bf16 v[92:95], a[52:55], a[216:219], v[92:95]\n\t" \
  "ds_read_b64_tr_b16 a[30:31], v201 offset:22016\n\t" \
  "v_mfma_f32_16x16x32_bf16 v[96:99], a[56:59], a[216:219], v[96:99]\n\t" \
  "v_mfma_f32_16x16x32_bf16 v[100:103], a[60:63], a[216:219], v[100:103]\n\t" \
  "v_mfma_f32_16x16x32_bf16 v[104:107], a[32:35], a[220:223], v[104:107]\n\t" \
  "buffer_load_dwordx4 a[216:219], v202, s[36:39], s88 offen offset:2048 sc1\n\t" \
  "v_mfma_f32_16x16x32_bf16 v[108:111], a[36:39], a[220:223], v[108:111]\n\t" \
  "v_mfma_f32_16x16x32_bf16 v[112:115], a[40:43], a[220:223], v[112:115]\n\t" \
  "v_mfma_f32_16x16x32_bf16 v[116:119], a[44:47], a[220:223], v[116:119]\n\t" \
  "v_mfma_f32_16x16x32_bf16 v[120:123], a[48:51], a[220:223], v[120:123]\n\t" \
  "v_mfma_f32_16x16x32_bf16 v[124:127], a[52:55], a[220:223], v[124:127]\n\t" \
  "v_mfma_f32_16x16x32_bf16 v[128:131], a[56:59], a[220:223], v[128:131]\n\t" \
  "v_mfma_f32_16x16x32_bf16 v[132:135], a[60:63], a[220:223], v[132:135]\n\t" \
  "s_waitcnt lgkmcnt(4)\n\t" \
  "v_mfma_f32_16x16x32_bf16 v[8:11], a[0:3], a[224:227], v[8:11]\n\t" \
  "buffer_load_dwordx4 a[220:223], v202, s[36:39], s88 offen offset:3072 sc1\n\t" \
  "v_mfma_f32_16x16x32_bf16 v[12:15], a[4:7], a[224:227], v[12:15]\n\t" \
  "ds_read_b64_tr_b16 a[32:33], v200 offset:24576\n\t" \
  "v_mfma_f32_16x16x32_bf16 v[16:19], a[8:11], a[224:227], v[16:19]\n\t" \
  "ds_read_b64_tr_b16 a[34:35], v200 offset:28672\n\t" \
  "v_mfma_f32_16x16x32_bf16 v[20:23], a[12:15], a[224:227], v[20:23]\n\t" \
  "s_mov_b32 s88, 4096\n\t" \
  "ds_read_b64_tr_b16 a[36:37], v201 offset:24576\n\t" \
  "v_mfma_f32_16x16x32_bf16 v[24:27], a[16:19], a[224:227], v[24:27]\n\t" \
  "buffer_load_dwordx4 a[128:131], v202, s[36:39], s88 offen offset:0 sc1\n\t" \
  "v_mfma_f32_16x16x32_bf16 v[28:31], a[20:23], a[224:227], v[28:31]\n\t" \
  "ds_read_b64_tr_b16 a[38:39], v201 offset:28672\n\t" \
  "s_waitcnt lgkmcnt(4)\n\t" \
  "v_mfma_f32_16x16x32_bf16 v[32:35], a[24:27], a[224:227], v[32:35]\n\t" \
  "ds_read_b64_tr_b16 a[40:41], v200 offset:25088\n\t" \
  "v_mfma_f32_16x16x32_bf16 v[36:39], a[28:31], a[224:227], v[36:39]\n\t" \
  "ds_read_b64_tr_b16 a[42:43], v200 offset:29184\n\t" \
  "v_mfma_f32_16x16x32_bf16 v[40:43], a[0:3], a[228:231], v[40:43]\n\t" \
  "buffer_load_dwordx4 a[132:135], v202, s[36:39], s88 offen offset:1024 sc1\n\t" \
  "v_mfma_f32_16x16x32_bf16 v[44:47], a[4:7], a[228:231], v[44:47]\n\t" \
  "ds_read_b64_tr_b16 a[44:45], v201 offset:25088\n\t" \
  "v_mfma_f32_16x16x32_bf16 v[48:51], a[8:11], a[228:231], v[48:51]\n\t" \
  "ds_read_b64_tr_b16 a[46:47], v201 offset:29184\n\t" \
  "v_mfma_f32_16x16x32_bf16 v[52:55], a[12:15], a[228:231], v[52:55]\n\t" \
  "ds_read_b64_tr_b16 a[48:49], v200 offset:25600\n\t" \
  "v_mfma_f32_16x16x32_bf16 v[56:59], a[16:19], a[228:231], v[56:59]\n\t" \
  "buffer_load_dwordx4 a[136:139], v202, s[36:39], s88 offen offset:2048 sc1\n\t" \
  "v_mfma_f32_16x16x32_bf16 v[60:63], a[20:23], a[228:231], v[60:63]\n\t" \
  "ds_read_b64_tr_b16 a[50:51], v200 offset:29696\n\t" \
  "v_mfma_f32_16x16x32_bf16 v[64:67], a[24:27], a[228:231], v[64:67]\n\t" \
  "ds_read_b64_tr_b16 a[52:53], v201 offset:25600\n\t" \
  "v_mfma_f32_16x16x32_bf16 v[68:71], a[28:31], a[228:231], v[68:71]\n\t" \
  "ds_read_b64_tr_b16 a[54:55], v201 offset:29696\n\t" \
  "v_mfma_f32_16x16x32_bf16 v[72:75], a[0:3], a[232:235], v[72:75]\n\t" \
  "buffer_load_dwordx4 a[140:143], v202, s[36:39], s88 offen offset:3072 sc1\n\t" \
  "v_mfma_f32_16x16x32_bf16 v[76:79], a[4:7], a[232:235], v[76:79]\n\t" \
  "ds_read_b64_tr_b16 a[56:57], v200 offset:26112\n\t" \
  "v_mfma_f32_16x16x32_bf16 v[80:83], a[8:11], a[232:235], v[80:83]\n\t" \
  "ds_read_b64_tr_b16 a[58:59], v200 offset:30208\n\t" \
  "v_mfma_f32_16x16x32_bf16 v[84:87], a[12:15], a[232:235], v[84:87]\n\t" \
  "s_mov_b32 s88, 8192\n\t" \
  "ds_read_b64_tr_b16 a[60:61], v201 offset:26112\n\t" \
  "v_mfma_f32_16x16x32_bf16 v[88:91], a[16:19], a[232:235], v[88:91]\n\t" \
  "buffer_load_dwordx4 a[144:147], v202, s[36:39], s88 offen offset:0 sc1\n\t" \
  "v_mfma_f32_16x16x32_bf16 v[92:95], a[20:23], a[232:235], v[92:95]\n\t" \
  "ds_read_b64_tr_b16 a[62:63], v201 offset:30208\n\t" \
  "v_mfma_f32_16x16x32_bf16 v[96:99], a[24:27], a[232:235], v[96:99]\n\t" \
  "buffer_load_dwordx4 a[148:151], v202, s[36:39], s88 offen offset:1024 sc1\n\t" \
  "v_mfma_f32_16x16x32_bf16 v[100:103], a[28:31], a[232:235], v[100:103]\n\t" \
  "v_mfma_f32_16x16x32_bf16 v[104:107], a[0:3], a[236:239], v[104:107]\n\t" \
  "v_mfma_f32_16x16x32_bf16 v[108:111], a[4:7], a[236:239], v[108:111]\n\t" \
  "v_mfma_f32_16x16x32_bf16 v[112:115], a[8:11], a[236:239], v[112:115]\n\t" \
  "v_mfma_f32_16x16x32_bf16 v[116:119], a[12:15], a[236:239], v[116:119]\n\t" \
  "v_mfma_f32_16x16x32_bf16 v[120:123], a[16:19], a[236:239], v[120:123]\n\t" \
  "v_mfma_f32_16x16x32_bf16 v[124:127], a[20:23], a[236:239], v[124:127]\n\t" \
  "v_add_u32 v200, 0x8000, v200\n\t" \
  "v_mfma_f32_16x16x32_bf16 v[128:131], a[24:27], a[236:239], v[128:131]\n\t" \
  "v_and_b32 v200, 0x1ffff, v200\n\t" \
  "v_mfma_f32_16x16x32_bf16 v[132:135], a[28:31], a[236:239], v[132:135]\n\t" \
  "v_xor_b32 v201, 32, v200\n\t" \
  "s_waitcnt lgkmcnt(2)\n\t" \
  "v_mfma_f32_16x16x32_bf16 v[8:11], a[32:35], a[240:243], v[8:11]\n\t" \
  "s_barrier\n\t" \
  "buffer_load_dwordx4 a[152:155], v202, s[36:39], s88 offen offset:2048 sc1\n\t" \
  "v_mfma_f32_16x16x32_bf16 v[12:15], a[36:39], a[240:243], v[12:15]\n\t" \
  "ds_read_b64_tr_b16 a[0:1], v200 offset:0\n\t" \
  "s_waitcnt vmcnt(28)\n\t" \
  "v_add_f32 v224, v224, v226\n\t" \
  "v_mfma_f32_16x16x32_bf16 v[16:19], a[40:43], a[240:243], v[16:19]\n\t" \
  "ds_read_b64_tr_b16 a[2:3], v200 offset:4096\n\t" \
  "s_waitcnt vmcnt(25)\n\t" \
  "v_add_f32 v228, v228, v230\n\t" \
  "v_mfma_f32_16x16x32_bf16 v[20:23], a[44:47], a[240:243], v[20:23]\n\t" \
  "ds_read_b64_tr_b16 a[4:5], v201 offset:0\n\t" \
  "s_waitcnt vmcnt(22)\n\t" \
  "v_add_f32 v232, v232, v234\n\t" \
  "v_mfma_f32_16x16x32_bf16 v[24:27], a[48:51], a[240:243], v[24:27]\n\t" \
  "buffer_load_dwordx4 a[156:159], v202, s[36:39], s88 offen offset:3072 sc1\n\t" \
  "v_mfma_f32_16x16x32_bf16 v[28:31], a[52:55], a[240:243], v[28:31]\n\t" \
  "ds_read_b64_tr_b16 a[6:7], v201 offset:4096\n\t" \
  "s_waitcnt vmcnt(20)\n\t" \
  "v_add_f32 v236, v236, v238\n\t" \
  "v_mfma_f32_16x16x32_bf16 v[32:35], a[56:59], a[240:243], v[32:35]\n\t" \
  "ds_read_b64_tr_b16 a[8:9], v200 offset:512\n\t" \
  "v_add_f32 v225, v225, v227\n\t" \
  "s_waitcnt lgkmcnt(5)\n\t" \
  "v_mfma_f32_16x16x32_bf16 v[36:39], a[60:63], a[240:243], v[36:39]\n\t" \
  "s_mov_b32 s88, 12288\n\t" \
  "ds_read_b64_tr_b16 a[10:11], v200 offset:4608\n\t" \
  "v_add_f32 v229, v229, v231\n\t" \
  "v_mfma_f32_16x16x32_bf16 v[40:43], a[32:35], a[244:247], v[40:43]\n\t" \
  "buffer_load_dwordx4 a[160:163], v202, s[36:39], s88 offen offset:0 sc1\n\t" \
  "v_mfma_f32_16x16x32_bf16 v[44:47], a[36:39], a[244:247], v[44:47]\n\t" \
  "ds_read_b64_tr_b16 a[12:13], v201 offset:512\n\t" \
  "v_add_f32 v233, v233, v235\n\t" \
  "v_mfma_f32_16x16x32_bf16 v[48:51], a[40:43], a[244:247], v[48:51]\n\t" \
  "ds_read_b64_tr_b16 a[14:15], v201 offset:4608\n\t" \
  "v_add_f32 v237, v237, v239\n\t" \
  "v_mfma_f32_16x16x32_bf16 v[52:55], a[44:47], a[244:247], v[52:55]\n\t" \
  "ds_read_b64_tr_b16 a[16:17], v200 offset:1024\n\t" \
  "v_mov_b32 v226, v224\n\t" \
  "v_mfma_f32_16x16x32_bf16 v[56:59], a[48:51], a[244:247], v[56:59]\n\t" \
  "buffer_load_dwordx4 a[164:167], v202, s[36:39], s88 offen offset:1024 sc1\n\t" \
  "v_mfma_f32_16x16x32_bf16 v[60:63], a[52:55], a[244:247], v[60:63]\n\t" \
  "ds_read_b64_tr_b16 a[18:19], v200 offset:5120\n\t" \
  "v_mov_b32 v230, v228\n\t" \
  "v_mfma_f32_16x16x32_bf16 v[64:67], a[56:59], a[244:247], v[64:67]\n\t" \
  "ds_read_b64_tr_b16 a[20:21], v201 offset:1024\n\t" \
  "v_mov_b32 v234, v232\n\t" \
  "v_mfma_f32_16x16x32_bf16 v[68:71], a[60:63], a[244:247], v[68:71]\n\t" \
  "ds_read_b64_tr_b16 a[22:23], v201 offset:5120\n\t" \
  "v_mov_b32 v238, v236\n\t" \
  "v_mfma_f32_16x16x32_bf16 v[72:75], a[32:35], a[248:251], v[72:75]\n\t" \
  "buffer_load_dwordx4 a[168:171], v202, s[36:39], s88 offen offset:2048 sc1\n\t" \
  "v_mfma_f32_16x16x32_bf16 v[76:79], a[36:39], a[248:251], v[76:79]\n\t" \
  "ds_read_b64_tr_b16 a[24:25], v200 offset:1536\n\t" \
  "v_mov_b32 v227, v225\n\t" \
  "v_mfma_f32_16x16x32_bf16 v[80:83], a[40:43], a[248:251], v[80:83]\n\t" \
  "ds_read_b64_tr_b16 a[26:27], v200 offset:5632\n\t" \
  "v_mov_b32 v231, v229\n\t" \
  "v_mfma_f32_16x16x32_bf16 v[84:87], a[44:47], a[248:251], v[84:87]\n\t" \
  "ds_read_b64_tr_b16 a[28:29], v201 offset:1536\n\t" \
  "v_mov_b32 v235, v233\n\t" \
  "v_mfma_f32_16x16x32_bf16 v[88:91], a[48:51], a[248:251], v[88:91]\n\t" \
  "buffer_load_dwordx4 a[172:175], v202, s[36:39], s88 offen offset:3072 sc1\n\t" \
  "v_mfma_f32_16x16x32_bf16 v[92:95], a[52:55], a[248:251], v[92:95]\n\t" \
  "s_mov_b32 s88, 16384\n\t" \
  "ds_read_b64_tr_b16 a[30:31], v201 offset:5632\n\t" \
  "v_mov_b32 v239, v237\n\t" \
  "v_mfma_f32_16x16x32_bf16 v[96:99], a[56:59], a[248:251], v[96:99]\n\t" \
  "buffer_load_dwordx4 a[176:179], v202, s[36:39], s88 offen offset:0 sc1\n\t" \
  "v_mfma_f32_16x16x32_bf16 v[100:103], a[60:63], a[248:251], v[100:103]\n\t" \
  "v_permlane16_swap_b32 v224, v226\n\t" \
  "v_mfma_f32_16x16x32_bf16 v[104:107], a[32:35], a[252:255], v[104:107]\n\t" \
  "global_load_dword v213, v212, s[68:69] sc1\n\t" \
  "v_permlane16_swap_b32 v228, v230\n\t" \
  "v_mfma_f32_16x16x32_bf16 v[108:111], a[36:39], a[252:255], v[108:111]\n\t" \
  "v_permlane16_swap_b32 v232, v234\n\t" \
  "v_mfma_f32_16x16x32_bf16 v[112:115], a[40:43], a[252:255], v[112:115]\n\t" \
  "v_permlane16_swap_b32 v236, v238\n\t" \
  "v_mfma_f32_16x16x32_bf16 v[116:119], a[44:47], a[252:255], v[116:119]\n\t" \
  "v_permlane16_swap_b32 v225, v227\n\t" \
  "v_mfma_f32_16x16x32_bf16 v[120:123], a[48:51], a[252:255], v[120:123]\n\t" \
  "v_permlane16_swap_b32 v229, v231\n\t" \
  "v_mfma_f32_16x16x32_bf16 v[124:127], a[52:55], a[252:255], v[124:127]\n\t" \
  "v_permlane16_swap_b32 v233, v235\n\t" \
  "v_mfma_f32_16x16x32_bf16 v[128:131], a[56:59], a[252:255], v[128:131]\n\t" \
  "v_permlane16_swap_b32 v237, v239\n\t" \
  "v_mfma_f32_16x16x32_bf16 v[132:135], a[60:63], a[252:255], v[132:135]\n\t" \
  "v_add_f32 v224, v224, v226\n\t" \
  "s_waitcnt lgkmcnt(0)\n\t" \
  "v_mfma_f32_16x16x32_bf16 v[8:11], a[0:3], a[64:67], v[8:11]\n\t" \
  "buffer_load_dwordx4 a[180:183], v202, s[36:39], s88 offen offset:1024 sc1\n\t" \
  "v_mfma_f32_16x16x32_bf16 v[12:15], a[4:7], a[64:67], v[12:15]\n\t" \
  "ds_read_b64_tr_b16 a[32:33], v200 offset:8192\n\t" \
  "v_add_f32 v228, v228, v230\n\t" \
  "v_mfma_f32_16x16x32_bf16 v[16:19], a[8:11], a[64:67], v[16:19]\n\t" \
  "ds_read_b64_tr_b16 a[34:35], v200 offset:12288\n\t" \
  "v_add_f32 v232, v232, v234\n\t" \
  "v_mfma_f32_16x16x32_bf16 v[20:23], a[12:15], a[64:67], v[20:23]\n\t" \
  "ds_read_b64_tr_b16 a[36:37], v201 offset:8192\n\t" \
  "v_add_f32 v236, v236, v238\n\t" \
  "v_mfma_f32_16x16x32_bf16 v[24:27], a[16:19], a[64:67], v[24:27]\n\t" \
  "buffer_load_dwordx4 a[184:187], v202, s[36:39], s88 offen offset:2048 sc1\n\t" \
  "v_mfma_f32_16x16x32_bf16 v[28:31], a[20:23], a[64:67], v[28:31]\n\t" \
  "ds_read_b64_tr_b16 a[38:39], v201 offset:12288\n\t" \
  "v_add_f32 v225, v225, v227\n\t" \
  "v_mfma_f32_16x16x32_bf16 v[32:35], a[24:27], a[64:67], v[32:35]\n\t" \
  "ds_read_b64_tr_b16 a[40:41], v200 offset:8704\n\t" \
  "v_add_f32 v229, v229, v231\n\t" \
  "v_mfma_f32_16x16x32_bf16 v[36:39], a[28:31], a[64:67], v[36:39]\n\t" \
  "ds_read_b64_tr_b16 a[42:43], v200 offset:12800\n\t" \
  "v_add_f32 v233, v233, v235\n\t" \
  "v_mfma_f32_16x16x32_bf16 v[40:43], a[0:3], a[68:71], v[40:43]\n\t" \
  "buffer_load_dwordx4 a[188:191], v202, s[36:39], s88 offen offset:3072 sc1\n\t" \
  "v_mfma_f32_16x16x32_bf16 v[44:47], a[4:7], a[68:71], v[44:47]\n\t" \
  "ds_read_b64_tr_b16 a[44:45], v201 offset:8704\n\t" \
  "v_add_f32 v237, v237, v239\n\t" \
  "v_mfma_f32_16x16x32_bf16 v[48:51], a[8:11], a[68:71], v[48:51]\n\t" \
  "ds_read_b64_tr_b16 a[46:47], v201 offset:12800\n\t" \
  "v_mov_b32 v226, v224\n\t" \
  "v_mfma_f32_16x16x32_bf16 v[52:55], a[12:15], a[68:71], v[52:55]\n\t" \
  "s_mov_b32 s88, 20480\n\t" \
  "ds_read_b64_tr_b16 a[48:49], v200 offset:9216\n\t" \
  "v_mov_b32 v230, v228\n\t" \
  "v_mfma_f32_16x16x32_bf16 v[56:59], a[16:19], a[68:71], v[56:59]\n\t" \
  "buffer_load_dwordx4 a[192:195], v202, s[36:39], s88 offen offset:0 sc1\n\t" \
  "v_mfma_f32_16x16x32_bf16 v[60:63], a[20:23], a[68:71], v[60:63]\n\t" \
  "ds_read_b64_tr_b16 a[50:51], v200 offset:13312\n\t" \
  "v_mov_b32 v234, v232\n\t" \
  "v_mfma_f32_16x16x32_bf16 v[64:67], a[24:27], a[68:71], v[64:67]\n\t" \
  "ds_read_b64_tr_b16 a[52:53], v201 offset:9216\n\t" \
  "v_mov_b32 v238, v236\n\t" \
  "v_mfma_f32_16x16x32_bf16 v[68:71], a[28:31], a[68:71], v[68:71]\n\t" \
  "ds_read_b64_tr_b16 a[54:55], v201 offset:13312\n\t" \
  "v_mov_b32 v227, v225\n\t" \
  "v_mfma_f32_16x16x32_bf16 v[72:75], a[0:3], a[72:75], v[72:75]\n\t" \
  "buffer_load_dwordx4 a[196:199], v202, s[36:39], s88 offen offset:1024 sc1\n\t" \
  "v_mfma_f32_16x16x32_bf16 v[76:79], a[4:7], a[72:75], v[76:79]\n\t" \
  "ds_read_b64_tr_b16 a[56:57], v200 offset:9728\n\t" \
  "v_mov_b32 v231, v229\n\t" \
  "v_mfma_f32_16x16x32_bf16 v[80:83], a[8:11], a[72:75], v[80:83]\n\t" \
  "ds_read_b64_tr_b16 a[58:59], v200 offset:13824\n\t" \
  "v_mov_b32 v235, v233\n\t" \
  "v_mfma_f32_16x16x32_bf16 v[84:87], a[12:15], a[72:75], v[84:87]\n\t" \
  "ds_read_b64_tr_b16 a[60:61], v201 offset:9728\n\t" \
  "v_mov_b32 v239, v237\n\t" \
  "v_mfma_f32_16x16x32_bf16 v[88:91], a[16:19], a[72:75], v[88:91]\n\t" \
  "buffer_load_dwordx4 a[200:203], v202, s[36:39], s88 offen offset:2048 sc1\n\t" \
  "v_mfma_f32_16x16x32_bf16 v[92:95], a[20:23], a[72:75], v[92:95]\n\t" \
  "ds_read_b64_tr_b16 a[62:63], v201 offset:13824\n\t" \
  "v_permlane32_swap_b32 v224, v226\n\t" \
  "v_mfma_f32_16x16x32_bf16 v[96:99], a[24:27], a[72:75], v[96:99]\n\t" \
  "buffer_load_dwordx4 a[204:207], v202, s[36:39], s88 offen offset:3072 sc1\n\t" \
  "v_mfma_f32_16x16x32_bf16 v[100:103], a[28:31], a[72:75], v[100:103]\n\t" \
  "v_permlane32_swap_b32 v228, v230\n\t" \
  "v_mfma_f32_16x16x32_bf16 v[104:107], a[0:3], a[76:79], v[104:107]\n\t" \
  "v_permlane32_swap_b32 v232, v234\n\t" \
  "v_mfma_f32_16x16x32_bf16 v[108:111], a[4:7], a[76:79], v[108:111]\n\t" \
  "v_permlane32_swap_b32 v236, v238\n\t" \
  "v_mfma_f32_16x16x32_bf16 v[112:115], a[8:11], a[76:79], v[112:115]\n\t" \
  "v_permlane32_swap_b32 v225, v227\n\t" \
  "v_mfma_f32_16x16x32_bf16 v[116:119], a[12:15], a[76:79], v[116:119]\n\t" \
  "v_permlane32_swap_b32 v229, v231\n\t" \
  "v_mfma_f32_16x16x32_bf16 v[120:123], a[16:19], a[76:79], v[120:123]\n\t" \
  "v_permlane32_swap_b32 v233, v235\n\t" \
  "v_mfma_f32_16x16x32_bf16 v[124:127], a[20:23], a[76:79], v[124:127]\n\t" \
  "v_permlane32_swap_b32 v237, v239\n\t" \
  "v_mfma_f32_16x16x32_bf16 v[128:131], a[24:27], a[76:79], v[128:131]\n\t" \
  "v_add_f32 v224, v224, v226\n\t" \
  "v_mfma_f32_16x16x32_bf16 v[132:135], a[28:31], a[76:79], v[132:135]\n\t" \
  "v_add_f32 v228, v228, v230\n\t" \
  "s_waitcnt lgkmcnt(0)\n\t" \
  "v_mfma_f32_16x16x32_bf16 v[8:11], a[32:35], a[80:83], v[8:11]\n\t" \
  "s_mov_b32 s88, 24576\n\t" \
  "buffer_load_dwordx4 a[224:227], v202, s[36:39], s88 offen offset:0 sc1\n\t" \
  "s_waitcnt vmcnt(8)\n\t" \
  "v_cmp_ne_u32 vcc, %[tag], v213\n\t" \
  "s_cbranch_vccnz .Lpuspin%=\n\t" \
  ".Lpuback%=:\n\t" \
  "v_mfma_f32_16x16x32_bf16 v[12:15], a[36:39], a[80:83], v[12:15]\n\t" \
  "ds_read_b64_tr_b16 a[0:1], v200 offset:16384\n\t" \
  "v_add_f32 v232, v232, v234\n\t" \
  "v_mfma_f32_16x16x32_bf16 v[16:19], a[40:43], a[80:83], v[16:19]\n\t" \
  "ds_read_b64_tr_b16 a[2:3], v200 offset:20480\n\t" \
  "v_add_f32 v236, v236, v238\n\t" \
  "v_mfma_f32_16x16x32_bf16 v[20:23], a[44:47], a[80:83], v[20:23]\n\t" \
  "ds_read_b64_tr_b16 a[4:5], v201 offset:16384\n\t" \
  "v_add_f32 v225, v225, v227\n\t" \
  "v_mfma_f32_16x16x32_bf16 v[24:27], a[48:51], a[80:83], v[24:27]\n\t" \
  "buffer_load_dwordx4 a[228:231], v202, s[36:39], s88 offen offset:1024 sc1\n\t" \
  "global_load_dwordx4 v[204:207], v202, s[40:41] sc1\n\t" \
  "v_mfma_f32_16x16x32_bf16 v[28:31], a[52:55], a[80:83], v[28:31]\n\t" \
  "ds_read_b64_tr_b16 a[6:7], v201 offset:20480\n\t" \
  "v_add_f32 v229, v229, v231\n\t" \
  "v_mfma_f32_16x16x32_bf16 v[32:35], a[56:59], a[80:83], v[32:35]\n\t" \
  "ds_read_b64_tr_b16 a[8:9], v200 offset:16896\n\t" \
  "v_add_f32 v233, v233, v235\n\t" \
  "v_mfma_f32_16x16x32_bf16 v[36:39], a[60:63], a[80:83], v[36:39]\n\t" \
  "ds_read_b64_tr_b16 a[10:11], v200 offset:20992\n\t" \
  "v_add_f32 v237, v237, v239\n\t" \
  "v_mfma_f32_16x16x32_bf16 v[40:43], a[32:35], a[84:87], v[40:43]\n\t" \
  "buffer_load_dwordx4 a[232:235], v202, s[36:39], s88 offen offset:2048 sc1\n\t" \
  "v_mfma_f32_16x16x32_bf16 v[44:47], a[36:39], a[84:87], v[44:47]\n\t" \
  "ds_read_b64_tr_b16 a[12:13], v201 offset:16896\n\t" \
  "v_mul_f32 v224, 0x3acccccd, v224\n\t" \
  "v_mfma_f32_16x16x32_bf16 v[48:51], a[40:43], a[84:87], v[48:51]\n\t" \
  "ds_read_b64_tr_b16 a[14:15], v201 offset:20992\n\t" \
  "v_mul_f32 v228, 0x3acccccd, v228\n\t" \
  "v_mfma_f32_16x16x32_bf16 v[52:55], a[44:47], a[84:87], v[52:55]\n\t" \
  "ds_read_b64_tr_b16 a[16:17], v200 offset:17408\n\t" \
  "v_mul_f32 v232, 0x3acccccd, v232\n\t" \
  "v_mfma_f32_16x16x32_bf16 v[56:59], a[48:51], a[84:87], v[56:59]\n\t" \
  "buffer_load_dwordx4 a[236:239], v202, s[36:39], s88 offen offset:3072 sc1\n\t" \
  "v_mfma_f32_16x16x32_bf16 v[60:63], a[52:55], a[84:87], v[60:63]\n\t" \
  "ds_read_b64_tr_b16 a[18:19], v200 offset:21504\n\t" \
  "v_mul_f32 v236, 0x3acccccd, v236\n\t" \
  "v_mfma_f32_16x16x32_bf16 v[64:67], a[56:59], a[84:87], v[64:67]\n\t" \
  "ds_read_b64_tr_b16 a[20:21], v201 offset:17408\n\t" \
  "v_mul_f32 v225, 0x3acccccd, v225\n\t" \
  "v_mfma_f32_16x16x32_bf16 v[68:71], a[60:63], a[84:87], v[68:71]\n\t" \
  "s_mov_b32 s88, 28672\n\t" \
  "ds_read_b64_tr_b16 a[22:23], v201 offset:21504\n\t" \
  "v_mul_f32 v229, 0x3acccccd, v229\n\t" \
  "v_mfma_f32_16x16x32_bf16 v[72:75], a[32:35], a[88:91], v[72:75]\n\t" \
  "buffer_load_dwordx4 a[240:243], v202, s[36:39], s88 offen offset:0 sc1\n\t" \
  "v_mfma_f32_16x16x32_bf16 v[76:79], a[36:39], a[88:91], v[76:79]\n\t" \
  "ds_read_b64_tr_b16 a[24:25], v200 offset:17920\n\t" \
  "v_mul_f32 v233, 0x3acccccd, v233\n\t" \
  "v_mfma_f32_16x16x32_bf16 v[80:83], a[40:43], a[88:91], v[80:83]\n\t" \
  "ds_read_b64_tr_b16 a[26:27], v200 offset:22016\n\t" \
  "v_mul_f32 v237, 0x3acccccd, v237\n\t" \
  "v_mfma_f32_16x16x32_bf16 v[84:87], a[44:47], a[88:91], v[84:87]\n\t" \
  "ds_read_b64_tr_b16 a[28:29], v201 offset:17920\n\t" \
  "v_fma_f32 v225, -v224, v224, v225\n\t" \
  "v_mfma_f32_16x16x32_bf16 v[88:91], a[48:51], a[88:91], v[88:91]\n\t" \
  "buffer_load_dwordx4 a[244:247], v202, s[36:39], s88 offen offset:1024 sc1\n\t" \
  "v_mfma_f32_16x16x32_bf16 v[92:95], a[52:55], a[88:91], v[92:95]\n\t" \
  "ds_read_b64_tr_b16 a[30:31], v201 offset:22016\n\t" \
  "v_fma_f32 v229, -v228, v228, v229\n\t" \
  "v_mfma_f32_16x16x32_bf16 v[96:99], a[56:59], a[88:91], v[96:99]\n\t" \
  "buffer_load_dwordx4 a[248:251], v202, s[36:39], s88 offen offset:2048 sc1\n\t" \
  "v_mfma_f32_16x16x32_bf16 v[100:103], a[60:63], a[88:91], v[100:103]\n\t" \
  "v_fma_f32 v233, -v232, v232, v233\n\t" \
  "v_mfma_f32_16x16x32_bf16 v[104:107], a[32:35], a[92:95], v[104:107]\n\t" \
  "v_fma_f32 v237, -v236, v236, v237\n\t" \
  "v_mfma_f32_16x16x32_bf16 v[108:111], a[36:39], a[92:95], v[108:111]\n\t" \
  "v_max_f32 v225, 0, v225\n\t" \
  "v_mfma_f32_16x16x32_bf16 v[112:115], a[40:43], a[92:95], v[112:115]\n\t" \
  "v_max_f32 v229, 0, v229\n\t" \
  "v_mfma_f32_16x16x32_bf16 v[116:119], a[44:47], a[92:95], v[116:119]\n\t" \
  "v_max_f32 v233, 0, v233\n\t" \
  "v_mfma_f32_16x16x32_bf16 v[120:123], a[48:51], a[92:95], v[120:123]\n\t" \
  "v_max_f32 v237, 0, v237\n\t" \
  "v_mfma_f32_16x16x32_bf16 v[124:127], a[52:55], a[92:95], v[124:127]\n\t" \
  "v_add_f32 v225, 0x3727c5ac, v225\n\t" \
  "v_mfma_f32_16x16x32_bf16 v[128:131], a[56:59], a[92:95], v[128:131]\n\t" \
  "v_add_f32 v229, 0x3727c5ac, v229\n\t" \
  "v_mfma_f32_16x16x32_bf16 v[132:135], a[60:63], a[92:95], v[132:135]\n\t" \
  "v_add_f32 v233, 0x3727c5ac, v233\n\t" \
  "s_waitcnt lgkmcnt(0)\n\t" \
  "v_mfma_f32_16x16x32_bf16 v[8:11], a[0:3], a[96:99], v[8:11]\n\t" \
  "buffer_load_dwordx4 a[252:255], v202, s[36:39], s88 offen offset:3072 sc1\n\t" \
  "v_mfma_f32_16x16x32_bf16 v[12:15], a[4:7], a[96:99], v[12:15]\n\t" \
  "ds_read_b64_tr_b16 a[32:33], v200 offset:24576\n\t" \
  "v_add_f32 v237, 0x3727c5ac, v237\n\t" \
  "v_mfma_f32_16x16x32_bf16 v[16:19], a[8:11], a[96:99], v[16:19]\n\t" \
  "ds_read_b64_tr_b16 a[34:35], v200 offset:28672\n\t" \
  "v_rsq_f32 v225, v225\n\t" \
  "v_mfma_f32_16x16x32_bf16 v[20:23], a[12:15], a[96:99], v[20:23]\n\t" \
  "s_mov_b32 s88, 32768\n\t" \
  "ds_read_b64_tr_b16 a[36:37], v201 offset:24576\n\t" \
  "v_rsq_f32 v229, v229\n\t" \
  "v_mfma_f32_16x16x32_bf16 v[24:27], a[16:19], a[96:99], v[24:27]\n\t" \
  "buffer_load_dwordx4 a[64:67], v202, s[36:39], s88 offen offset:0 sc1\n\t" \
  "v_mfma_f32_16x16x32_bf16 v[28:31], a[20:23], a[96:99], v[28:31]\n\t" \
  "ds_read_b64_tr_b16 a[38:39], v201 offset:28672\n\t" \
  "v_rsq_f32 v233, v233\n\t" \
  "v_mfma_f32_16x16x32_bf16 v[32:35], a[24:27], a[96:99], v[32:35]\n\t" \
  "ds_read_b64_tr_b16 a[40:41], v200 offset:25088\n\t" \
  "v_rsq_f32 v237, v237\n\t" \
  "v_mfma_f32_16x16x32_bf16 v[36:39], a[28:31], a[96:99], v[36:39]\n\t" \
  "ds_read_b64_tr_b16 a[42:43], v200 offset:29184\n\t" \
  "v_mul_f32 v227, v224, v225\n\t" \
  "v_mfma_f32_16x16x32_bf16 v[40:43], a[0:3], a[100:103], v[40:43]\n\t" \
  "buffer_load_dwordx4 a[68:71], v202, s[36:39], s88 offen offset:1024 sc1\n\t" \
  "v_mfma_f32_16x16x32_bf16 v[44:47], a[4:7], a[100:103], v[44:47]\n\t" \
  "ds_read_b64_tr_b16 a[44:45], v201 offset:25088\n\t" \
  "v_mul_f32 v231, v228, v229\n\t" \
  "v_mfma_f32_16x16x32_bf16 v[48:51], a[8:11], a[100:103], v[48:51]\n\t" \
  "ds_read_b64_tr_b16 a[46:47], v201 offset:29184\n\t" \
  "v_mul_f32 v235, v232, v233\n\t" \
  "v_mfma_f32_16x16x32_bf16 v[52:55], a[12:15], a[100:103], v[52:55]\n\t" \
  "ds_read_b64_tr_b16 a[48:49], v200 offset:25600\n\t" \
  "v_mul_f32 v239, v236, v237\n\t" \
  "v_mfma_f32_16x16x32_bf16 v[56:59], a[16:19], a[100:103], v[56:59]\n\t" \
  "buffer_load_dwordx4 a[72:75], v202, s[36:39], s88 offen offset:2048 sc1\n\t" \
  "v_mfma_f32_16x16x32_bf16 v[60:63], a[20:23], a[100:103], v[60:63]\n\t" \
  "ds_read_b64_tr_b16 a[50:51], v200 offset:29696\n\t" \
  "v_mul_f32 v224, 0xbfb8aa3b, v225\n\t" \
  "v_mfma_f32_16x16x32_bf16 v[64:67], a[24:27], a[100:103], v[64:67]\n\t" \
  "ds_read_b64_tr_b16 a[52:53], v201 offset:25600\n\t" \
  "v_mul_f32 v228, 0xbfb8aa3b, v229\n\t" \
  "v_mfma_f32_16x16x32_bf16 v[68:71], a[28:31], a[100:103], v[68:71]\n\t" \
  "ds_read_b64_tr_b16 a[54:55], v201 offset:29696\n\t" \
  "v_mul_f32 v232, 0xbfb8aa3b, v233\n\t" \
  "v_mfma_f32_16x16x32_bf16 v[72:75], a[0:3], a[104:107], v[72:75]\n\t" \
  "buffer_load_dwordx4 a[76:79], v202, s[36:39], s88 offen offset:3072 sc1\n\t" \
  "v_mfma_f32_16x16x32_bf16 v[76:79], a[4:7], a[104:107], v[76:79]\n\t" \
  "ds_read_b64_tr_b16 a[56:57], v200 offset:26112\n\t" \
  "v_mul_f32 v236, 0xbfb8aa3b, v237\n\t" \
  "v_mfma_f32_16x16x32_bf16 v[80:83], a[8:11], a[104:107], v[80:83]\n\t" \
  "ds_read_b64_tr_b16 a[58:59], v200 offset:30208\n\t" \
  "v_mul_f32 v226, 0xbf317218, v225\n\t" \
  "v_mfma_f32_16x16x32_bf16 v[84:87], a[12:15], a[104:107], v[84:87]\n\t" \
  "s_mov_b32 s88, 36864\n\t" \
  "ds_read_b64_tr_b16 a[60:61], v201 offset:26112\n\t" \
  "v_mul_f32 v230, 0xbf317218, v229\n\t" \
  "v_mfma_f32_16x16x32_bf16 v[88:91], a[16:19], a[104:107], v[88:91]\n\t" \
  "buffer_load_dwordx4 a[80:83], v202, s[36:39], s88 offen offset:0 sc1\n\t" \
  "v_mfma_f32_16x16x32_bf16 v[92:95], a[20:23], a[104:107], v[92:95]\n\t" \
  "ds_read_b64_tr_b16 a[62:63], v201 offset:30208\n\t" \
  "v_mul_f32 v234, 0xbf317218, v233\n\t" \
  "v_mfma_f32_16x16x32_bf16 v[96:99], a[24:27], a[104:107], v[96:99]\n\t" \
  "buffer_load_dwordx4 a[84:87], v202, s[36:39], s88 offen offset:1024 sc1\n\t" \
  "v_mfma_f32_16x16x32_bf16 v[100:103], a[28:31], a[104:107], v[100:103]\n\t" \
  "v_mul_f32 v238, 0xbf317218, v237\n\t" \
  "v_mfma_f32_16x16x32_bf16 v[104:107], a[0:3], a[108:111], v[104:107]\n\t" \
  "v_mul_f32 v225, 0x3fb8aa3b, v227\n\t" \
  "v_mfma_f32_16x16x32_bf16 v[108:111], a[4:7], a[108:111], v[108:111]\n\t" \
  "v_mul_f32 v229, 0x3fb8aa3b, v231\n\t" \
  "v_mfma_f32_16x16x32_bf16 v[112:115], a[8:11], a[108:111], v[112:115]\n\t" \
  "v_mul_f32 v233, 0x3fb8aa3b, v235\n\t" \
  "v_mfma_f32_16x16x32_bf16 v[116:119], a[12:15], a[108:111], v[116:119]\n\t" \
  "v_mul_f32 v237, 0x3fb8aa3b, v239\n\t" \
  "v_mfma_f32_16x16x32_bf16 v[120:123], a[16:19], a[108:111], v[120:123]\n\t" \
  "v_mul_f32 v227, 0x3f317218, v227\n\t" \
  "v_mfma_f32_16x16x32_bf16 v[124:127], a[20:23], a[108:111], v[124:127]\n\t" \
  "v_mul_f32 v231, 0x3f317218, v231\n\t" \
  "v_mfma_f32_16x16x32_bf16 v[128:131], a[24:27], a[108:111], v[128:131]\n\t" \
  "v_mul_f32 v235, 0x3f317218, v235\n\t" \
  "v_mfma_f32_16x16x32_bf16 v[132:135], a[28:31], a[108:111], v[132:135]\n\t" \
  "v_mul_f32 v239, 0x3f317218, v239\n\t" \
  "s_waitcnt lgkmcnt(0)\n\t" \
  "v_mfma_f32_16x16x32_bf16 v[8:11], a[32:35], a[112:115], v[8:11]\n\t" \
  "buffer_load_dwordx4 a[88:91], v202, s[36:39], s88 offen offset:2048 sc1\n\t" \
  "v_mfma_f32_16x16x32_bf16 v[12:15], a[36:39], a[112:115], v[12:15]\n\t" \
  "v_mfma_f32_16x16x32_bf16 v[16:19], a[40:43], a[112:115], v[16:19]\n\t" \
  "buffer_load_dwordx4 a[92:95], v202, s[36:39], s88 offen offset:3072 sc1\n\t" \
  "v_mfma_f32_16x16x32_bf16 v[20:23], a[44:47], a[112:115], v[20:23]\n\t" \
  "s_mov_b32 s88, 40960\n\t" \
  "v_mfma_f32_16x16x32_bf16 v[24:27], a[48:51], a[112:115], v[24:27]\n\t" \
  "buffer_load_dwordx4 a[96:99], v202, s[36:39], s88 offen offset:0 sc1\n\t" \
  "v_mfma_f32_16x16x32_bf16 v[28:31], a[52:55], a[112:115], v[28:31]\n\t" \
  "v_mfma_f32_16x16x32_bf16 v[32:35], a[56:59], a[112:115], v[32:35]\n\t" \
  "buffer_load_dwordx4 a[100:103], v202, s[36:39], s88 offen offset:1024 sc1\n\t" \
  "v_mfma_f32_16x16x32_bf16 v[36:39], a[60:63], a[112:115], v[36:39]\n\t" \
  "v_mfma_f32_16x16x32_bf16 v[40:43], a[32:35], a[116:119], v[40:43]\n\t" \
  "v_mfma_f32_16x16x32_bf16 v[44:47], a[36:39], a[116:119], v[44:47]\n\t" \
  "v_mfma_f32_16x16x32_bf16 v[48:51], a[40:43], a[116:119], v[48:51]\n\t" \
  "s_waitcnt vmcnt(16)\n\t" \
  "ds_write_b128 v220, v[204:207]\n\t" \
  "v_mfma_f32_16x16x32_bf16 v[52:55], a[44:47], a[116:119], v[52:55]\n\t" \
  "v_mfma_f32_16x16x32_bf16 v[56:59], a[48:51], a[116:119], v[56:59]\n\t" \
  "v_mfma_f32_16x16x32_bf16 v[60:63], a[52:55], a[116:119], v[60:63]\n\t" \
  "ds_read_b128 v[136:139], v221 offset:0\n\t" \
  "v_mfma_f32_16x16x32_bf16 v[64:67], a[56:59], a[116:119], v[64:67]\n\t" \
  "ds_read_b128 v[140:143], v221 offset:64\n\t" \
  "v_mfma_f32_16x16x32_bf16 v[68:71], a[60:63], a[116:119], v[68:71]\n\t" \
  "ds_read_b128 v[144:147], v221 offset:128\n\t" \
  "v_mfma_f32_16x16x32_bf16 v[72:75], a[32:35], a[120:123], v[72:75]\n\t" \
  "ds_read_b128 v[148:151], v221 offset:192\n\t" \
  "v_mfma_f32_16x16x32_bf16 v[76:79], a[36:39], a[120:123], v[76:79]\n\t" \
  "ds_read_b128 v[152:155], v221 offset:256\n\t" \
  "v_mfma_f32_16x16x32_bf16 v[80:83], a[40:43], a[120:123], v[80:83]\n\t" \
  "ds_read_b128 v[156:159], v221 offset:320\n\t" \
  "v_mfma_f32_16x16x32_bf16 v[84:87], a[44:47], a[120:123], v[84:87]\n\t" \
  "ds_read_b128 v[160:163], v221 offset:384\n\t" \
  "v_mfma_f32_16x16x32_bf16 v[88:91], a[48:51], a[120:123], v[88:91]\n\t" \
  "ds_read_b128 v[164:167], v221 offset:448\n\t" \
  "v_mfma_f32_16x16x32_bf16 v[92:95], a[52:55], a[120:123], v[92:95]\n\t" \
  "ds_read_b128 v[168:171], v221 offset:512\n\t" \
  "v_mfma_f32_16x16x32_bf16 v[96:99], a[56:59], a[120:123], v[96:99]\n\t" \
  "ds_read_b128 v[172:175], v221 offset:576\n\t" \
  "v_mfma_f32_16x16x32_bf16 v[100:103], a[60:63], a[120:123], v[100:103]\n\t" \
  "ds_read_b128 v[176:179], v221 offset:640\n\t" \
  "v_mfma_f32_16x16x32_bf16 v[104:107], a[32:35], a[124:127], v[104:107]\n\t" \
  "ds_read_b128 v[180:183], v221 offset:704\n\t" \
  "v_mfma_f32_16x16x32_bf16 v[108:111], a[36:39], a[124:127], v[108:111]\n\t" \
  "ds_read_b128 v[184:187], v221 offset:768\n\t" \
  "v_mfma_f32_16x16x32_bf16 v[112:115], a[40:43], a[124:127], v[112:115]\n\t" \
  "ds_read_b128 v[188:191], v221 offset:832\n\t" \
  "v_mfma_f32_16x16x32_bf16 v[116:119], a[44:47], a[124:127], v[116:119]\n\t" \
  "ds_read_b128 v[192:195], v221 offset:896\n\t" \
  "v_mfma_f32_16x16x32_bf16 v[120:123], a[48:51], a[124:127], v[120:123]\n\t" \
  "ds_read_b128 v[196:199], v221 offset:960\n\t" \
  "v_mfma_f32_16x16x32_bf16 v[124:127], a[52:55], a[124:127], v[124:127]\n\t" \
  "v_mfma_f32_16x16x32_bf16 v[128:131], a[56:59], a[124:127], v[128:131]\n\t" \
  "v_mfma_f32_16x16x32_bf16 v[132:135], a[60:63], a[124:127], v[132:135]\n\t" \
  "s_waitcnt vmcnt(32)\n\t" \
  "buffer_load_dwordx4 a[0:3], v202, s[36:39], s88 offen offset:2048 sc1\n\t" \
  "s_waitcnt lgkmcnt(7)\n\t" \
  "v_pk_fma_f32 v[204:205], v[136:137], v[224:225], v[168:169] op_sel:[0,1,0] op_sel_hi:[1,1,1]\n\t" \
  "v_pk_fma_f32 v[206:207], v[138:139], v[224:225], v[170:171] op_sel:[0,1,0] op_sel_hi:[1,1,1]\n\t" \
  "s_waitcnt lgkmcnt(6)\n\t" \
  "v_pk_fma_f32 v[208:209], v[140:141], v[224:225], v[172:173] op_sel:[0,1,0] op_sel_hi:[1,1,1]\n\t" \
  "v_pk_fma_f32 v[210:211], v[142:143], v[224:225], v[174:175] op_sel:[0,1,0] op_sel_hi:[1,1,1]\n\t" \
  "v_pk_fma_f32 v[8:9], v[8:9], v[224:225], v[204:205] op_sel:[0,0,0] op_sel_hi:[1,0,1]\n\t" \
  "v_pk_fma_f32 v[10:11], v[10:11], v[224:225], v[206:207] op_sel:[0,0,0] op_sel_hi:[1,0,1]\n\t" \
  "v_pk_fma_f32 v[12:13], v[12:13], v[224:225], v[208:209] op_sel:[0,0,0] op_sel_hi:[1,0,1]\n\t" \
  "v_pk_fma_f32 v[14:15], v[14:15], v[224:225], v[210:211] op_sel:[0,0,0] op_sel_hi:[1,0,1]\n\t" \
  "s_waitcnt lgkmcnt(3)\n\t" \
  "v_pk_fma_f32 v[204:205], v[152:153], v[226:227], v[184:185] op_sel:[0,1,0] op_sel_hi:[1,1,1]\n\t" \
  "v_pk_fma_f32 v[206:207], v[154:155], v[226:227], v[186:187] op_sel:[0,1,0] op_sel_hi:[1,1,1]\n\t" \
  "s_waitcnt lgkmcnt(2)\n\t" \
  "v_pk_fma_f32 v[208:209], v[156:157], v[226:227], v[188:189] op_sel:[0,1,0] op_sel_hi:[1,1,1]\n\t" \
  "v_pk_fma_f32 v[210:211], v[158:159], v[226:227], v[190:191] op_sel:[0,1,0] op_sel_hi:[1,1,1]\n\t" \
  "buffer_load_dwordx4 a[4:7], v202, s[36:39], s88 offen offset:3072 sc1\n\t" \
  "v_exp_f32 v248, v8\n\t" \
  "v_exp_f32 v250, v10\n\t" \
  "v_exp_f32 v252, v12\n\t" \
  "v_exp_f32 v254, v14\n\t" \
  "v_exp_f32 v249, v9\n\t" \
  "v_exp_f32 v251, v11\n\t" \
  "v_exp_f32 v253, v13\n\t" \
  "v_exp_f32 v255, v15\n\t" \
  "v_pk_fma_f32 v[24:25], v[24:25], v[226:227], v[204:205] op_sel:[0,0,0] op_sel_hi:[1,0,1]\n\t" \
  "v_pk_fma_f32 v[26:27], v[26:27], v[226:227], v[206:207] op_sel:[0,0,0] op_sel_hi:[1,0,1]\n\t" \
  "v_pk_fma_f32 v[28:29], v[28:29], v[226:227], v[208:209] op_sel:[0,0,0] op_sel_hi:[1,0,1]\n\t" \
  "v_pk_fma_f32 v[30:31], v[30:31], v[226:227], v[210:211] op_sel:[0,0,0] op_sel_hi:[1,0,1]\n\t" \
  "s_mov_b32 s88, 45056\n\t" \
  "buffer_load_dwordx4 a[8:11], v202, s[36:39], s88 offen offset:0 sc1\n\t" \
  "v_pk_add_f32 v[248:249], v[248:249], v[246:247] op_sel_hi:[1,0]\n\t" \
  "v_pk_add_f32 v[250:251], v[250:251], v[246:247] op_sel_hi:[1,0]\n\t" \
  "v_pk_add_f32 v[252:253], v[252:253], v[246:247] op_sel_hi:[1,0]\n\t" \
  "v_pk_add_f32 v[254:255], v[254:255], v[246:247] op_sel_hi:[1,0]\n\t" \
  "v_rcp_f32 v248, v248\n\t" \
  "v_rcp_f32 v250, v250\n\t" \
  "v_rcp_f32 v252, v252\n\t" \
  "v_rcp_f32 v254, v254\n\t" \
  "v_rcp_f32 v249, v249\n\t" \
  "v_rcp_f32 v251, v251\n\t" \
  "v_rcp_f32 v253, v253\n\t" \
  "v_rcp_f32 v255, v255\n\t" \
  "buffer_load_dwordx4 a[12:15], v202, s[36:39], s88 offen offset:1024 sc1\n\t" \
  "v_pk_mul_f32 v[8:9], v[8:9], v[24:25]\n\t" \
  "v_pk_mul_f32 v[10:11], v[10:11], v[26:27]\n\t" \
  "v_pk_mul_f32 v[12:13], v[12:13], v[28:29]\n\t" \
  "v_pk_mul_f32 v[14:15], v[14:15], v[30:31]\n\t" \
  "v_pk_mul_f32 v[8:9], v[8:9], v[248:249]\n\t" \
  "v_pk_mul_f32 v[10:11], v[10:11], v[250:251]\n\t" \
  "v_pk_mul_f32 v[12:13], v[12:13], v[252:253]\n\t" \
  "v_pk_mul_f32 v[14:15], v[14:15], v[254:255]\n\t" \
  "v_cvt_pk_bf16_f32 v24, v8, v9\n\t" \
  "v_cvt_pk_bf16_f32 v25, v10, v11\n\t" \
  "v_cvt_pk_bf16_f32 v26, v12, v13\n\t" \
  "v_cvt_pk_bf16_f32 v27, v14, v15\n\t" \
  "buffer_store_dwordx4 v[24:27], v202, s[48:51], 0 offen offset:0 sc1\n\t" \
  "buffer_load_dwordx4 a[16:19], v202, s[36:39], s88 offen offset:2048 sc1\n\t" \
  "v_pk_fma_f32 v[204:205], v[144:145], v[224:225], v[176:177] op_sel:[0,1,0] op_sel_hi:[1,1,1]\n\t" \
  "v_pk_fma_f32 v[206:207], v[146:147], v[224:225], v[178:179] op_sel:[0,1,0] op_sel_hi:[1,1,1]\n\t" \
  "v_pk_fma_f32 v[208:209], v[148:149], v[224:225], v[180:181] op_sel:[0,1,0] op_sel_hi:[1,1,1]\n\t" \
  "v_pk_fma_f32 v[210:211], v[150:151], v[224:225], v[182:183] op_sel:[0,1,0] op_sel_hi:[1,1,1]\n\t" \
  "v_pk_fma_f32 v[16:17], v[16:17], v[224:225], v[204:205] op_sel:[0,0,0] op_sel_hi:[1,0,1]\n\t" \
  "v_pk_fma_f32 v[18:19], v[18:19], v[224:225], v[206:207] op_sel:[0,0,0] op_sel_hi:[1,0,1]\n\t" \
  "v_pk_fma_f32 v[20:21], v[20:21], v[224:225], v[208:209] op_sel:[0,0,0] op_sel_hi:[1,0,1]\n\t" \
  "v_pk_fma_f32 v[22:23], v[22:23], v[224:225], v[210:211] op_sel:[0,0,0] op_sel_hi:[1,0,1]\n\t" \
  "s_waitcnt lgkmcnt(0)\n\t" \
  "v_pk_fma_f32 v[204:205], v[160:161], v[226:227], v[192:193] op_sel:[0,1,0] op_sel_hi:[1,1,1]\n\t" \
  "v_pk_fma_f32 v[206:207], v[162:163], v[226:227], v[194:195] op_sel:[0,1,0] op_sel_hi:[1,1,1]\n\t" \
  "v_pk_fma_f32 v[208:209], v[164:165], v[226:227], v[196:197] op_sel:[0,1,0] op_sel_hi:[1,1,1]\n\t" \
  "v_pk_fma_f32 v[210:211], v[166:167], v[226:227], v[198:199] op_sel:[0,1,0] op_sel_hi:[1,1,1]\n\t" \
  "buffer_load_dwordx4 a[20:23], v202, s[36:39], s88 offen offset:3072 sc1\n\t" \
  "v_exp_f32 v248, v16\n\t" \
  "v_exp_f32 v250, v18\n\t" \
  "v_exp_f32 v252, v20\n\t" \
  "v_exp_f32 v254, v22\n\t" \
  "v_exp_f32 v249, v17\n\t" \
  "v_exp_f32 v251, v19\n\t" \
  "v_exp_f32 v253, v21\n\t" \
  "v_exp_f32 v255, v23\n\t" \
  "v_pk_fma_f32 v[32:33], v[32:33], v[226:227], v[204:205] op_sel:[0,0,0] op_sel_hi:[1,0,1]\n\t" \
  "v_pk_fma_f32 v[34:35], v[34:35], v[226:227], v[206:207] op_sel:[0,0,0] op_sel_hi:[1,0,1]\n\t" \
  "v_pk_fma_f32 v[36:37], v[36:37], v[226:227], v[208:209] op_sel:[0,0,0] op_sel_hi:[1,0,1]\n\t" \
  "v_pk_fma_f32 v[38:39], v[38:39], v[226:227], v[210:211] op_sel:[0,0,0] op_sel_hi:[1,0,1]\n\t" \
  "s_mov_b32 s88, 49152\n\t" \
  "buffer_load_dwordx4 a[24:27], v202, s[36:39], s88 offen offset:0 sc1\n\t" \
  "v_pk_add_f32 v[248:249], v[248:249], v[246:247] op_sel_hi:[1,0]\n\t" \
  "v_pk_add_f32 v[250:251], v[250:251], v[246:247] op_sel_hi:[1,0]\n\t" \
  "v_pk_add_f32 v[252:253], v[252:253], v[246:247] op_sel_hi:[1,0]\n\t" \
  "v_pk_add_f32 v[254:255], v[254:255], v[246:247] op_sel_hi:[1,0]\n\t" \
  "v_rcp_f32 v248, v248\n\t" \
  "v_rcp_f32 v250, v250\n\t" \
  "v_rcp_f32 v252, v252\n\t" \
  "v_rcp_f32 v254, v254\n\t" \
  "v_rcp_f32 v249, v249\n\t" \
  "v_rcp_f32 v251, v251\n\t" \
  "v_rcp_f32 v253, v253\n\t" \
  "v_rcp_f32 v255, v255\n\t" \
  "buffer_load_dwordx4 a[28:31], v202, s[36:39], s88 offen offset:1024 sc1\n\t" \
  "v_pk_mul_f32 v[16:17], v[16:17], v[32:33]\n\t" \
  "v_pk_mul_f32 v[18:19], v[18:19], v[34:35]\n\t" \
  "v_pk_mul_f32 v[20:21], v[20:21], v[36:37]\n\t" \
  "v_pk_mul_f32 v[22:23], v[22:23], v[38:39]\n\t" \
  "v_pk_mul_f32 v[16:17], v[16:17], v[248:249]\n\t" \
  "v_pk_mul_f32 v[18:19], v[18:19], v[250:251]\n\t" \
  "v_pk_mul_f32 v[20:21], v[20:21], v[252:253]\n\t" \
  "v_pk_mul_f32 v[22:23], v[22:23], v[254:255]\n\t" \
  "v_cvt_pk_bf16_f32 v28, v16, v17\n\t" \
  "v_cvt_pk_bf16_f32 v29, v18, v19\n\t" \
  "v_cvt_pk_bf16_f32 v30, v20, v21\n\t" \
  "v_cvt_pk_bf16_f32 v31, v22, v23\n\t" \
  "buffer_store_dwordx4 v[28:31], v202, s[48:51], s85 offen offset:0 sc1\n\t" \
  "buffer_load_dwordx4 a[32:35], v202, s[36:39], s88 offen offset:2048 sc1\n\t" \
  "v_pk_fma_f32 v[204:205], v[136:137], v[228:229], v[168:169] op_sel:[0,1,0] op_sel_hi:[1,1,1]\n\t" \
  "v_pk_fma_f32 v[206:207], v[138:139], v[228:229], v[170:171] op_sel:[0,1,0] op_sel_hi:[1,1,1]\n\t" \
  "v_pk_fma_f32 v[208:209], v[140:141], v[228:229], v[172:173] op_sel:[0,1,0] op_sel_hi:[1,1,1]\n\t" \
  "v_pk_fma_f32 v[210:211], v[142:143], v[228:229], v[174:175] op_sel:[0,1,0] op_sel_hi:[1,1,1]\n\t" \
  "v_pk_fma_f32 v[40:41], v[40:41], v[228:229], v[204:205] op_sel:[0,0,0] op_sel_hi:[1,0,1]\n\t" \
  "v_pk_fma_f32 v[42:43], v[42:43], v[228:229], v[206:207] op_sel:[0,0,0] op_sel_hi:[1,0,1]\n\t" \
  "v_pk_fma_f32 v[44:45], v[44:45], v[228:229], v[208:209] op_sel:[0,0,0] op_sel_hi:[1,0,1]\n\t" \
  "v_pk_fma_f32 v[46:47], v[46:47], v[228:229], v[210:211] op_sel:[0,0,0] op_sel_hi:[1,0,1]\n\t" \
  "v_pk_fma_f32 v[204:205], v[152:153], v[230:231], v[184:185] op_sel:[0,1,0] op_sel_hi:[1,1,1]\n\t" \
  "v_pk_fma_f32 v[206:207], v[154:155], v[230:231], v[186:187] op_sel:[0,1,0] op_sel_hi:[1,1,1]\n\t" \
  "v_pk_fma_f32 v[208:209], v[156:157], v[230:231], v[188:189] op_sel:[0,1,0] op_sel_hi:[1,1,1]\n\t" \
  "v_pk_fma_f32 v[210:211], v[158:159], v[230:231], v[190:191] op_sel:[0,1,0] op_sel_hi:[1,1,1]\n\t" \
  "buffer_load_dwordx4 a[36:39], v202, s[36:39], s88 offen offset:3072 sc1\n\t" \
  "v_exp_f32 v248, v40\n\t" \
  "v_exp_f32 v250, v42\n\t" \
  "v_exp_f32 v252, v44\n\t" \
  "v_exp_f32 v254, v46\n\t" \
  "v_exp_f32 v249, v41\n\t" \
  "v_exp_f32 v251, v43\n\t" \
  "v_exp_f32 v253, v45\n\t" \
  "v_exp_f32 v255, v47\n\t" \
  "v_pk_fma_f32 v[56:57], v[56:57], v[230:231], v[204:205] op_sel:[0,0,0] op_sel_hi:[1,0,1]\n\t" \
  "v_pk_fma_f32 v[58:59], v[58:59], v[230:231], v[206:207] op_sel:[0,0,0] op_sel_hi:[1,0,1]\n\t" \
  "v_pk_fma_f32 v[60:61], v[60:61], v[230:231], v[208:209] op_sel:[0,0,0] op_sel_hi:[1,0,1]\n\t" \
  "v_pk_fma_f32 v[62:63], v[62:63], v[230:231], v[210:211] op_sel:[0,0,0] op_sel_hi:[1,0,1]\n\t" \
  "s_mov_b32 s88, 53248\n\t" \
  "buffer_load_dwordx4 a[40:43], v202, s[36:39], s88 offen offset:0 sc1\n\t" \
  "v_pk_add_f32 v[248:249], v[248:249], v[246:247] op_sel_hi:[1,0]\n\t" \
  "v_pk_add_f32 v[250:251], v[250:251], v[246:247] op_sel_hi:[1,0]\n\t" \
  "v_pk_add_f32 v[252:253], v[252:253], v[246:247] op_sel_hi:[1,0]\n\t" \
  "v_pk_add_f32 v[254:255], v[254:255], v[246:247] op_sel_hi:[1,0]\n\t" \
  "v_rcp_f32 v248, v248\n\t" \
  "v_rcp_f32 v250, v250\n\t" \
  "v_rcp_f32 v252, v252\n\t" \
  "v_rcp_f32 v254, v254\n\t" \
  "v_rcp_f32 v249, v249\n\t" \
  "v_rcp_f32 v251, v251\n\t" \
  "v_rcp_f32 v253, v253\n\t" \
  "v_rcp_f32 v255, v255\n\t" \
  "buffer_load_dwordx4 a[44:47], v202, s[36:39], s88 offen offset:1024 sc1\n\t" \
  "v_pk_mul_f32 v[40:41], v[40:41], v[56:57]\n\t" \
  "v_pk_mul_f32 v[42:43], v[42:43], v[58:59]\n\t" \
  "v_pk_mul_f32 v[44:45], v[44:45], v[60:61]\n\t" \
  "v_pk_mul_f32 v[46:47], v[46:47], v[62:63]\n\t" \
  "v_pk_mul_f32 v[40:41], v[40:41], v[248:249]\n\t" \
  "v_pk_mul_f32 v[42:43], v[42:43], v[250:251]\n\t" \
  "v_pk_mul_f32 v[44:45], v[44:45], v[252:253]\n\t" \
  "v_pk_mul_f32 v[46:47], v[46:47], v[254:255]\n\t" \
  "v_cvt_pk_bf16_f32 v56, v40, v41\n\t" \
  "v_cvt_pk_bf16_f32 v57, v42, v43\n\t" \
  "v_cvt_pk_bf16_f32 v58, v44, v45\n\t" \
  "v_cvt_pk_bf16_f32 v59, v46, v47\n\t" \
  "buffer_store_dwordx4 v[56:59], v202, s[48:51], 0 offen offset:1024 sc1\n\t" \
  "buffer_load_dwordx4 a[48:51], v202, s[36:39], s88 offen offset:2048 sc1\n\t" \
  "v_pk_fma_f32 v[204:205], v[144:145], v[228:229], v[176:177] op_sel:[0,1,0] op_sel_hi:[1,1,1]\n\t" \
  "v_pk_fma_f32 v[206:207], v[146:147], v[228:229], v[178:179] op_sel:[0,1,0] op_sel_hi:[1,1,1]\n\t" \
  "v_pk_fma_f32 v[208:209], v[148:149], v[228:229], v[180:181] op_sel:[0,1,0] op_sel_hi:[1,1,1]\n\t" \
  "v_pk_fma_f32 v[210:211], v[150:151], v[228:229], v[182:183] op_sel:[0,1,0] op_sel_hi:[1,1,1]\n\t" \
  "v_pk_fma_f32 v[48:49], v[48:49], v[228:229], v[204:205] op_sel:[0,0,0] op_sel_hi:[1,0,1]\n\t" \
  "v_pk_fma_f32 v[50:51], v[50:51], v[228:229], v[206:207] op_sel:[0,0,0] op_sel_hi:[1,0,1]\n\t" \
  "v_pk_fma_f32 v[52:53], v[52:53], v[228:229], v[208:209] op_sel:[0,0,0] op_sel_hi:[1,0,1]\n\t" \
  "v_pk_fma_f32 v[54:55], v[54:55], v[228:229], v[210:211] op_sel:[0,0,0] op_sel_hi:[1,0,1]\n\t" \
  "v_pk_fma_f32 v[204:205], v[160:161], v[230:231], v[192:193] op_sel:[0,1,0] op_sel_hi:[1,1,1]\n\t" \
  "v_pk_fma_f32 v[206:207], v[162:163], v[230:231], v[194:195] op_sel:[0,1,0] op_sel_hi:[1,1,1]\n\t" \
  "v_pk_fma_f32 v[208:209], v[164:165], v[230:231], v[196:197] op_sel:[0,1,0] op_sel_hi:[1,1,1]\n\t" \
  "v_pk_fma_f32 v[210:211], v[166:167], v[230:231], v[198:199] op_sel:[0,1,0] op_sel_hi:[1,1,1]\n\t" \
  "buffer_load_dwordx4 a[52:55], v202, s[36:39], s88 offen offset:3072 sc1\n\t" \
  "v_exp_f32 v248, v48\n\t" \
  "v_exp_f32 v250, v50\n\t" \
  "v_exp_f32 v252, v52\n\t" \
  "v_exp_f32 v254, v54\n\t" \
  "v_exp_f32 v249, v49\n\t" \
  "v_exp_f32 v251, v51\n\t" \
  "v_exp_f32 v253, v53\n\t" \
  "v_exp_f32 v255, v55\n\t" \
  "v_pk_fma_f32 v[64:65], v[64:65], v[230:231], v[204:205] op_sel:[0,0,0] op_sel_hi:[1,0,1]\n\t" \
  "v_pk_fma_f32 v[66:67], v[66:67], v[230:231], v[206:207] op_sel:[0,0,0] op_sel_hi:[1,0,1]\n\t" \
  "v_pk_fma_f32 v[68:69], v[68:69], v[230:231], v[208:209] op_sel:[0,0,0] op_sel_hi:[1,0,1]\n\t" \
  "v_pk_fma_f32 v[70:71], v[70:71], v[230:231], v[210:211] op_sel:[0,0,0] op_sel_hi:[1,0,1]\n\t" \
  "s_mov_b32 s88, 57344\n\t" \
  "buffer_load_dwordx4 a[56:59], v202, s[36:39], s88 offen offset:0 sc1\n\t" \
  "v_pk_add_f32 v[248:249], v[248:249], v[246:247] op_sel_hi:[1,0]\n\t" \
  "v_pk_add_f32 v[250:251], v[250:251], v[246:247] op_sel_hi:[1,0]\n\t" \
  "v_pk_add_f32 v[252:253], v[252:253], v[246:247] op_sel_hi:[1,0]\n\t" \
  "v_pk_add_f32 v[254:255], v[254:255], v[246:247] op_sel_hi:[1,0]\n\t" \
  "v_rcp_f32 v248, v248\n\t" \
  "v_rcp_f32 v250, v250\n\t" \
  "v_rcp_f32 v252, v252\n\t" \
  "v_rcp_f32 v254, v254\n\t" \
  "v_rcp_f32 v249, v249\n\t" \
  "v_rcp_f32 v251, v251\n\t" \
  "v_rcp_f32 v253, v253\n\t" \
  "v_rcp_f32 v255, v255\n\t" \
  "buffer_load_dwordx4 a[60:63], v202, s[36:39], s88 offen offset:1024 sc1\n\t" \
  "v_pk_mul_f32 v[48:49], v[48:49], v[64:65]\n\t" \
  "v_pk_mul_f32 v[50:51], v[50:51], v[66:67]\n\t" \
  "v_pk_mul_f32 v[52:53], v[52:53], v[68:69]\n\t" \
  "v_pk_mul_f32 v[54:55], v[54:55], v[70:71]\n\t" \
  "v_pk_mul_f32 v[48:49], v[48:49], v[248:249]\n\t" \
  "v_pk_mul_f32 v[50:51], v[50:51], v[250:251]\n\t" \
  "v_pk_mul_f32 v[52:53], v[52:53], v[252:253]\n\t" \
  "v_pk_mul_f32 v[54:55], v[54:55], v[254:255]\n\t" \
  "v_cvt_pk_bf16_f32 v60, v48, v49\n\t" \
  "v_cvt_pk_bf16_f32 v61, v50, v51\n\t" \
  "v_cvt_pk_bf16_f32 v62, v52, v53\n\t" \
  "v_cvt_pk_bf16_f32 v63, v54, v55\n\t" \
  "buffer_store_dwordx4 v[60:63], v202, s[48:51], s85 offen offset:1024 sc1\n\t" \
  "buffer_load_dwordx4 a[104:107], v202, s[36:39], s88 offen offset:2048 sc1\n\t" \
  "v_pk_fma_f32 v[204:205], v[136:137], v[232:233], v[168:169] op_sel:[0,1,0] op_sel_hi:[1,1,1]\n\t" \
  "v_pk_fma_f32 v[206:207], v[138:139], v[232:233], v[170:171] op_sel:[0,1,0] op_sel_hi:[1,1,1]\n\t" \
  "v_pk_fma_f32 v[208:209], v[140:141], v[232:233], v[172:173] op_sel:[0,1,0] op_sel_hi:[1,1,1]\n\t" \
  "v_pk_fma_f32 v[210:211], v[142:143], v[232:233], v[174:175] op_sel:[0,1,0] op_sel_hi:[1,1,1]\n\t" \
  "v_pk_fma_f32 v[72:73], v[72:73], v[232:233], v[204:205] op_sel:[0,0,0] op_sel_hi:[1,0,1]\n\t" \
  "v_pk_fma_f32 v[74:75], v[74:75], v[232:233], v[206:207] op_sel:[0,0,0] op_sel_hi:[1,0,1]\n\t" \
  "v_pk_fma_f32 v[76:77], v[76:77], v[232:233], v[208:209] op_sel:[0,0,0] op_sel_hi:[1,0,1]\n\t" \
  "v_pk_fma_f32 v[78:79], v[78:79], v[232:233], v[210:211] op_sel:[0,0,0] op_sel_hi:[1,0,1]\n\t" \
  "v_pk_fma_f32 v[204:205], v[152:153], v[234:235], v[184:185] op_sel:[0,1,0] op_sel_hi:[1,1,1]\n\t" \
  "v_pk_fma_f32 v[206:207], v[154:155], v[234:235], v[186:187] op_sel:[0,1,0] op_sel_hi:[1,1,1]\n\t" \
  "v_pk_fma_f32 v[208:209], v[156:157], v[234:235], v[188:189] op_sel:[0,1,0] op_sel_hi:[1,1,1]\n\t" \
  "v_pk_fma_f32 v[210:211], v[158:159], v[234:235], v[190:191] op_sel:[0,1,0] op_sel_hi:[1,1,1]\n\t" \
  "buffer_load_dwordx4 a[108:111], v202, s[36:39], s88 offen offset:3072 sc1\n\t" \
  "v_exp_f32 v248, v72\n\t" \
  "v_exp_f32 v250, v74\n\t" \
  "v_exp_f32 v252, v76\n\t" \
  "v_exp_f32 v254, v78\n\t" \
  "v_exp_f32 v249, v73\n\t" \
  "v_exp_f32 v251, v75\n\t" \
  "v_exp_f32 v253, v77\n\t" \
  "v_exp_f32 v255, v79\n\t" \
  "v_pk_fma_f32 v[88:89], v[88:89], v[234:235], v[204:205] op_sel:[0,0,0] op_sel_hi:[1,0,1]\n\t" \
  "v_pk_fma_f32 v[90:91], v[90:91], v[234:235], v[206:207] op_sel:[0,0,0] op_sel_hi:[1,0,1]\n\t" \
  "v_pk_fma_f32 v[92:93], v[92:93], v[234:235], v[208:209] op_sel:[0,0,0] op_sel_hi:[1,0,1]\n\t" \
  "v_pk_fma_f32 v[94:95], v[94:95], v[234:235], v[210:211] op_sel:[0,0,0] op_sel_hi:[1,0,1]\n\t" \
  "s_mov_b32 s88, 61440\n\t" \
  "buffer_load_dwordx4 a[112:115], v202, s[36:39], s88 offen offset:0 sc1\n\t" \
  "v_pk_add_f32 v[248:249], v[248:249], v[246:247] op_sel_hi:[1,0]\n\t" \
  "v_pk_add_f32 v[250:251], v[250:251], v[246:247] op_sel_hi:[1,0]\n\t" \
  "v_pk_add_f32 v[252:253], v[252:253], v[246:247] op_sel_hi:[1,0]\n\t" \
  "v_pk_add_f32 v[254:255], v[254:255], v[246:247] op_sel_hi:[1,0]\n\t" \
  "v_rcp_f32 v248, v248\n\t" \
  "v_rcp_f32 v250, v250\n\t" \
  "v_rcp_f32 v252, v252\n\t" \
  "v_rcp_f32 v254, v254\n\t" \
  "v_rcp_f32 v249, v249\n\t" \
  "v_rcp_f32 v251, v251\n\t" \
  "v_rcp_f32 v253, v253\n\t" \
  "v_rcp_f32 v255, v255\n\t" \
  "buffer_load_dwordx4 a[116:119], v202, s[36:39], s88 offen offset:1024 sc1\n\t" \
  "v_pk_mul_f32 v[72:73], v[72:73], v[88:89]\n\t" \
  "v_pk_mul_f32 v[74:75], v[74:75], v[90:91]\n\t" \
  "v_pk_mul_f32 v[76:77], v[76:77], v[92:93]\n\t" \
  "v_pk_mul_f32 v[78:79], v[78:79], v[94:95]\n\t" \
  "v_pk_mul_f32 v[72:73], v[72:73], v[248:249]\n\t" \
  "v_pk_mul_f32 v[74:75], v[74:75], v[250:251]\n\t" \
  "v_pk_mul_f32 v[76:77], v[76:77], v[252:253]\n\t" \
  "v_pk_mul_f32 v[78:79], v[78:79], v[254:255]\n\t" \
  "v_cvt_pk_bf16_f32 v88, v72, v73\n\t" \
  "v_cvt_pk_bf16_f32 v89, v74, v75\n\t" \
  "v_cvt_pk_bf16_f32 v90, v76, v77\n\t" \
  "v_cvt_pk_bf16_f32 v91, v78, v79\n\t" \
  "buffer_store_dwordx4 v[88:91], v202, s[48:51], 0 offen offset:2048 sc1\n\t" \
  "buffer_load_dwordx4 a[120:123], v202, s[36:39], s88 offen offset:2048 sc1\n\t" \
  "v_pk_fma_f32 v[204:205], v[144:145], v[232:233], v[176:177] op_sel:[0,1,0] op_sel_hi:[1,1,1]\n\t" \
  "v_pk_fma_f32 v[206:207], v[146:147], v[232:233], v[178:179] op_sel:[0,1,0] op_sel_hi:[1,1,1]\n\t" \
  "v_pk_fma_f32 v[208:209], v[148:149], v[232:233], v[180:181] op_sel:[0,1,0] op_sel_hi:[1,1,1]\n\t" \
  "v_pk_fma_f32 v[210:211], v[150:151], v[232:233], v[182:183] op_sel:[0,1,0] op_sel_hi:[1,1,1]\n\t" \
  "v_pk_fma_f32 v[80:81], v[80:81], v[232:233], v[204:205] op_sel:[0,0,0] op_sel_hi:[1,0,1]\n\t" \
  "v_pk_fma_f32 v[82:83], v[82:83], v[232:233], v[206:207] op_sel:[0,0,0] op_sel_hi:[1,0,1]\n\t" \
  "v_pk_fma_f32 v[84:85], v[84:85], v[232:233], v[208:209] op_sel:[0,0,0] op_sel_hi:[1,0,1]\n\t" \
  "v_pk_fma_f32 v[86:87], v[86:87], v[232:233], v[210:211] op_sel:[0,0,0] op_sel_hi:[1,0,1]\n\t" \
  "v_pk_fma_f32 v[204:205], v[160:161], v[234:235], v[192:193] op_sel:[0,1,0] op_sel_hi:[1,1,1]\n\t" \
  "v_pk_fma_f32 v[206:207], v[162:163], v[234:235], v[194:195] op_sel:[0,1,0] op_sel_hi:[1,1,1]\n\t" \
  "v_pk_fma_f32 v[208:209], v[164:165], v[234:235], v[196:197] op_sel:[0,1,0] op_sel_hi:[1,1,1]\n\t" \
  "v_pk_fma_f32 v[210:211], v[166:167], v[234:235], v[198:199] op_sel:[0,1,0] op_sel_hi:[1,1,1]\n\t" \
  "buffer_load_dwordx4 a[124:127], v202, s[36:39], s88 offen offset:3072 sc1\n\t" \
  "v_exp_f32 v248, v80\n\t" \
  "v_exp_f32 v250, v82\n\t" \
  "v_exp_f32 v252, v84\n\t" \
  "v_exp_f32 v254, v86\n\t" \
  "v_exp_f32 v249, v81\n\t" \
  "v_exp_f32 v251, v83\n\t" \
  "v_exp_f32 v253, v85\n\t" \
  "v_exp_f32 v255, v87\n\t" \
  "v_pk_fma_f32 v[96:97], v[96:97], v[234:235], v[204:205] op_sel:[0,0,0] op_sel_hi:[1,0,1]\n\t" \
  "v_pk_fma_f32 v[98:99], v[98:99], v[234:235], v[206:207] op_sel:[0,0,0] op_sel_hi:[1,0,1]\n\t" \
  "v_pk_fma_f32 v[100:101], v[100:101], v[234:235], v[208:209] op_sel:[0,0,0] op_sel_hi:[1,0,1]\n\t" \
  "v_pk_fma_f32 v[102:103], v[102:103], v[234:235], v[210:211] op_sel:[0,0,0] op_sel_hi:[1,0,1]\n\t" \
  "v_pk_add_f32 v[248:249], v[248:249], v[246:247] op_sel_hi:[1,0]\n\t" \
  "v_pk_add_f32 v[250:251], v[250:251], v[246:247] op_sel_hi:[1,0]\n\t" \
  "v_pk_add_f32 v[252:253], v[252:253], v[246:247] op_sel_hi:[1,0]\n\t" \
  "v_pk_add_f32 v[254:255], v[254:255], v[246:247] op_sel_hi:[1,0]\n\t" \
  "v_rcp_f32 v248, v248\n\t" \
  "v_rcp_f32 v250, v250\n\t" \
  "v_rcp_f32 v252, v252\n\t" \
  "v_rcp_f32 v254, v254\n\t" \
  "v_rcp_f32 v249, v249\n\t" \
  "v_rcp_f32 v251, v251\n\t" \
  "v_rcp_f32 v253, v253\n\t" \
  "v_rcp_f32 v255, v255\n\t" \
  "v_pk_mul_f32 v[80:81], v[80:81], v[96:97]\n\t" \
  "v_pk_mul_f32 v[82:83], v[82:83], v[98:99]\n\t" \
  "v_pk_mul_f32 v[84:85], v[84:85], v[100:101]\n\t" \
  "v_pk_mul_f32 v[86:87], v[86:87], v[102:103]\n\t" \
  "v_pk_mul_f32 v[80:81], v[80:81], v[248:249]\n\t" \
  "v_pk_mul_f32 v[82:83], v[82:83], v[250:251]\n\t" \
  "v_pk_mul_f32 v[84:85], v[84:85], v[252:253]\n\t" \
  "v_pk_mul_f32 v[86:87], v[86:87], v[254:255]\n\t" \
  "v_cvt_pk_bf16_f32 v92, v80, v81\n\t" \
  "v_cvt_pk_bf16_f32 v93, v82, v83\n\t" \
  "v_cvt_pk_bf16_f32 v94, v84, v85\n\t" \
  "v_cvt_pk_bf16_f32 v95, v86, v87\n\t" \
  "buffer_store_dwordx4 v[92:95], v202, s[48:51], s85 offen offset:2048 sc1\n\t" \
  "v_pk_fma_f32 v[204:205], v[136:137], v[236:237], v[168:169] op_sel:[0,1,0] op_sel_hi:[1,1,1]\n\t" \
  "v_pk_fma_f32 v[206:207], v[138:139], v[236:237], v[170:171] op_sel:[0,1,0] op_sel_hi:[1,1,1]\n\t" \
  "v_pk_fma_f32 v[208:209], v[140:141], v[236:237], v[172:173] op_sel:[0,1,0] op_sel_hi:[1,1,1]\n\t" \
  "v_pk_fma_f32 v[210:211], v[142:143], v[236:237], v[174:175] op_sel:[0,1,0] op_sel_hi:[1,1,1]\n\t" \
  "v_pk_fma_f32 v[104:105], v[104:105], v[236:237], v[204:205] op_sel:[0,0,0] op_sel_hi:[1,0,1]\n\t" \
  "v_pk_fma_f32 v[106:107], v[106:107], v[236:237], v[206:207] op_sel:[0,0,0] op_sel_hi:[1,0,1]\n\t" \
  "v_pk_fma_f32 v[108:109], v[108:109], v[236:237], v[208:209] op_sel:[0,0,0] op_sel_hi:[1,0,1]\n\t" \
  "v_pk_fma_f32 v[110:111], v[110:111], v[236:237], v[210:211] op_sel:[0,0,0] op_sel_hi:[1,0,1]\n\t" \
  "v_pk_fma_f32 v[204:205], v[152:153], v[238:239], v[184:185] op_sel:[0,1,0] op_sel_hi:[1,1,1]\n\t" \
  "v_pk_fma_f32 v[206:207], v[154:155], v[238:239], v[186:187] op_sel:[0,1,0] op_sel_hi:[1,1,1]\n\t" \
  "v_pk_fma_f32 v[208:209], v[156:157], v[238:239], v[188:189] op_sel:[0,1,0] op_sel_hi:[1,1,1]\n\t" \
  "v_pk_fma_f32 v[210:211], v[158:159], v[238:239], v[190:191] op_sel:[0,1,0] op_sel_hi:[1,1,1]\n\t" \
  "v_exp_f32 v248, v104\n\t" \
  "v_exp_f32 v250, v106\n\t" \
  "v_exp_f32 v252, v108\n\t" \
  "v_exp_f32 v254, v110\n\t" \
  "v_exp_f32 v249, v105\n\t" \
  "v_exp_f32 v251, v107\n\t" \
  "v_exp_f32 v253, v109\n\t" \
  "v_exp_f32 v255, v111\n\t" \
  "v_pk_fma_f32 v[120:121], v[120:121], v[238:239], v[204:205] op_sel:[0,0,0] op_sel_hi:[1,0,1]\n\t" \
  "v_pk_fma_f32 v[122:123], v[122:123], v[238:239], v[206:207] op_sel:[0,0,0] op_sel_hi:[1,0,1]\n\t" \
  "v_pk_fma_f32 v[124:125], v[124:125], v[238:239], v[208:209] op_sel:[0,0,0] op_sel_hi:[1,0,1]\n\t" \
  "v_pk_fma_f32 v[126:127], v[126:127], v[238:239], v[210:211] op_sel:[0,0,0] op_sel_hi:[1,0,1]\n\t" \
  "v_pk_add_f32 v[248:249], v[248:249], v[246:247] op_sel_hi:[1,0]\n\t" \
  "v_pk_add_f32 v[250:251], v[250:251], v[246:247] op_sel_hi:[1,0]\n\t" \
  "v_pk_add_f32 v[252:253], v[252:253], v[246:247] op_sel_hi:[1,0]\n\t" \
  "v_pk_add_f32 v[254:255], v[254:255], v[246:247] op_sel_hi:[1,0]\n\t" \
  "v_rcp_f32 v248, v248\n\t" \
  "v_rcp_f32 v250, v250\n\t" \
  "v_rcp_f32 v252, v252\n\t" \
  "v_rcp_f32 v254, v254\n\t" \
  "v_rcp_f32 v249, v249\n\t" \
  "v_rcp_f32 v251, v251\n\t" \
  "v_rcp_f32 v253, v253\n\t" \
  "v_rcp_f32 v255, v255\n\t" \
  "v_pk_mul_f32 v[104:105], v[104:105], v[120:121]\n\t" \
  "v_pk_mul_f32 v[106:107], v[106:107], v[122:123]\n\t" \
  "v_pk_mul_f32 v[108:109], v[108:109], v[124:125]\n\t" \
  "v_pk_mul_f32 v[110:111], v[110:111], v[126:127]\n\t" \
  "v_pk_mul_f32 v[104:105], v[104:105], v[248:249]\n\t" \
  "v_pk_mul_f32 v[106:107], v[106:107], v[250:251]\n\t" \
  "v_pk_mul_f32 v[108:109], v[108:109], v[252:253]\n\t" \
  "v_pk_mul_f32 v[110:111], v[110:111], v[254:255]\n\t" \
  "v_cvt_pk_bf16_f32 v120, v104, v105\n\t" \
  "v_cvt_pk_bf16_f32 v121, v106, v107\n\t" \
  "v_cvt_pk_bf16_f32 v122, v108, v109\n\t" \
  "v_cvt_pk_bf16_f32 v123, v110, v111\n\t" \
  "buffer_store_dwordx4 v[120:123], v202, s[48:51], 0 offen offset:3072 sc1\n\t" \
  "v_pk_fma_f32 v[204:205], v[144:145], v[236:237], v[176:177] op_sel:[0,1,0] op_sel_hi:[1,1,1]\n\t" \
  "v_pk_fma_f32 v[206:207], v[146:147], v[236:237], v[178:179] op_sel:[0,1,0] op_sel_hi:[1,1,1]\n\t" \
  "v_pk_fma_f32 v[208:209], v[148:149], v[236:237], v[180:181] op_sel:[0,1,0] op_sel_hi:[1,1,1]\n\t" \
  "v_pk_fma_f32 v[210:211], v[150:151], v[236:237], v[182:183] op_sel:[0,1,0] op_sel_hi:[1,1,1]\n\t" \
  "v_pk_fma_f32 v[112:113], v[112:113], v[236:237], v[204:205] op_sel:[0,0,0] op_sel_hi:[1,0,1]\n\t" \
  "v_pk_fma_f32 v[114:115], v[114:115], v[236:237], v[206:207] op_sel:[0,0,0] op_sel_hi:[1,0,1]\n\t" \
  "v_pk_fma_f32 v[116:117], v[116:117], v[236:237], v[208:209] op_sel:[0,0,0] op_sel_hi:[1,0,1]\n\t" \
  "v_pk_fma_f32 v[118:119], v[118:119], v[236:237], v[210:211] op_sel:[0,0,0] op_sel_hi:[1,0,1]\n\t" \
  "v_pk_fma_f32 v[204:205], v[160:161], v[238:239], v[192:193] op_sel:[0,1,0] op_sel_hi:[1,1,1]\n\t" \
  "v_pk_fma_f32 v[206:207], v[162:163], v[238:239], v[194:195] op_sel:[0,1,0] op_sel_hi:[1,1,1]\n\t" \
  "v_pk_fma_f32 v[208:209], v[164:165], v[238:239], v[196:197] op_sel:[0,1,0] op_sel_hi:[1,1,1]\n\t" \
  "v_pk_fma_f32 v[210:211], v[166:167], v[238:239], v[198:199] op_sel:[0,1,0] op_sel_hi:[1,1,1]\n\t" \
  "v_exp_f32 v248, v112\n\t" \
  "v_exp_f32 v250, v114\n\t" \
  "v_exp_f32 v252, v116\n\t" \
  "v_exp_f32 v254, v118\n\t" \
  "v_exp_f32 v249, v113\n\t" \
  "v_exp_f32 v251, v115\n\t" \
  "v_exp_f32 v253, v117\n\t" \
  "v_exp_f32 v255, v119\n\t" \
  "v_pk_fma_f32 v[128:129], v[128:129], v[238:239], v[204:205] op_sel:[0,0,0] op_sel_hi:[1,0,1]\n\t" \
  "v_pk_fma_f32 v[130:131], v[130:131], v[238:239], v[206:207] op_sel:[0,0,0] op_sel_hi:[1,0,1]\n\t" \
  "v_pk_fma_f32 v[132:133], v[132:133], v[238:239], v[208:209] op_sel:[0,0,0] op_sel_hi:[1,0,1]\n\t" \
  "v_pk_fma_f32 v[134:135], v[134:135], v[238:239], v[210:211] op_sel:[0,0,0] op_sel_hi:[1,0,1]\n\t" \
  "v_pk_add_f32 v[248:249], v[248:249], v[246:247] op_sel_hi:[1,0]\n\t" \
  "v_pk_add_f32 v[250:251], v[250:251], v[246:247] op_sel_hi:[1,0]\n\t" \
  "v_pk_add_f32 v[252:253], v[252:253], v[246:247] op_sel_hi:[1,0]\n\t" \
  "v_pk_add_f32 v[254:255], v[254:255], v[246:247] op_sel_hi:[1,0]\n\t" \
  "v_rcp_f32 v248, v248\n\t" \
  "v_rcp_f32 v250, v250\n\t" \
  "v_rcp_f32 v252, v252\n\t" \
  "v_rcp_f32 v254, v254\n\t" \
  "v_rcp_f32 v249, v249\n\t" \
  "v_rcp_f32 v251, v251\n\t" \
  "v_rcp_f32 v253, v253\n\t" \
  "v_rcp_f32 v255, v255\n\t" \
  "v_pk_mul_f32 v[112:113], v[112:113], v[128:129]\n\t" \
  "v_pk_mul_f32 v[114:115], v[114:115], v[130:131]\n\t" \
  "v_pk_mul_f32 v[116:117], v[116:117], v[132:133]\n\t" \
  "v_pk_mul_f32 v[118:119], v[118:119], v[134:135]\n\t" \
  "v_pk_mul_f32 v[112:113], v[112:113], v[248:249]\n\t" \
  "v_pk_mul_f32 v[114:115], v[114:115], v[250:251]\n\t" \
  "v_pk_mul_f32 v[116:117], v[116:117], v[252:253]\n\t" \
  "v_pk_mul_f32 v[118:119], v[118:119], v[254:255]\n\t" \
  "v_cvt_pk_bf16_f32 v124, v112, v113\n\t" \
  "v_cvt_pk_bf16_f32 v125, v114, v115\n\t" \
  "v_cvt_pk_bf16_f32 v126, v116, v117\n\t" \
  "v_cvt_pk_bf16_f32 v127, v118, v119\n\t" \
  "buffer_store_dwordx4 v[124:127], v202, s[48:51], s85 offen offset:3072 sc1\n\t" \
  "s_mul_i32 s86, %[ft], 37\n\t" \
  "s_lshr_b32 s86, s86, 8\n\t" \
  "s_mul_i32 s87, s86, 7\n\t" \
  "s_sub_u32 s87, %[ft], s87\n\t" \
  "s_mul_i32 s68, %[pc], 4096\n\t" \
  "s_mul_i32 s72, s86, 1024\n\t" \
  "s_add_u32 s68, s68, s72\n\t" \
  "s_mul_i32 s72, s87, 128\n\t" \
  "s_add_u32 s68, s68, s72\n\t" \
  "s_add_u32 s68, s68, 42270720\n\t" \
  "s_mov_b64 s[72:73], %[ws]\n\t" \
  "s_add_u32 s68, s68, s72\n\t" \
  "s_addc_u32 s69, s73, 0\n\t" \
  "s_waitcnt vmcnt(0) lgkmcnt(0)\n\t" \
  "s_barrier\n\t" \
  "s_cmp_lg_u32 %[w], 0\n\t" \
  "s_cbranch_scc1 .Lfa%=\n\t" \
  "s_mov_b64 exec, 0xffffffff\n\t" \
  "v_lshlrev_b32 v200, 2, v203\n\t" \
  "v_mov_b32 v201, %[tag]\n\t" \
  "global_store_dword v200, v201, s[68:69] sc1\n\t" \
  "s_mov_b64 exec, -1\n\t" \
  ".Lfa%=:\n\t" \
  ".Ldjoin%=:\n\t" \
  "s_mov_b32 s88, 65536\n\t" \
  "buffer_load_dwordx4 v[224:227], v202, s[36:39], s88 offen offset:0 sc1\n\t" \
  "buffer_load_dwordx4 v[228:231], v202, s[36:39], s88 offen offset:1024 sc1\n\t" \
  "buffer_load_dwordx4 v[232:235], v202, s[36:39], s88 offen offset:2048 sc1\n\t" \
  "buffer_load_dwordx4 v[236:239], v202, s[36:39], s88 offen offset:3072 sc1\n\t" \
  "s_mov_b32 s88, 69632\n\t" \
  "buffer_load_dwordx4 v[240:243], v202, s[36:39], s88 offen offset:0 sc1\n\t" \
  "buffer_load_dwordx4 v[244:247], v202, s[36:39], s88 offen offset:1024 sc1\n\t" \
  "s_add_u32 s84, %[w], 0\n\t" \
  "s_and_b32 s84, s84, 3\n\t" \
  "s_lshl_b32 s85, s84, 10\n\t" \
  "v_add_u32 v204, s85, v202\n\t" \
  "s_mul_i32 s84, s84, 15360\n\t" \
  "s_add_u32 s84, s84, 65536\n\t" \
  "v_add_u32 v211, s84, v202\n\t" \
  "s_add_u32 s84, %[w], 1\n\t" \
  "s_and_b32 s84, s84, 3\n\t" \
  "s_lshl_b32 s85, s84, 10\n\t" \
  "v_add_u32 v205, s85, v202\n\t" \
  "s_mul_i32 s84, s84, 15360\n\t" \
  "s_add_u32 s84, s84, 65536\n\t" \
  "v_add_u32 v208, s84, v202\n\t" \
  "s_add_u32 s84, %[w], 2\n\t" \
  "s_and_b32 s84, s84, 3\n\t" \
  "s_lshl_b32 s85, s84, 10\n\t" \
  "v_add_u32 v206, s85, v202\n\t" \
  "s_mul_i32 s84, s84, 15360\n\t" \
  "s_add_u32 s84, s84, 70656\n\t" \
  "v_add_u32 v209, s84, v202\n\t" \
  "s_add_u32 s84, %[w], 3\n\t" \
  "s_and_b32 s84, s84, 3\n\t" \
  "s_lshl_b32 s85, s84, 10\n\t" \
  "v_add_u32 v207, s85, v202\n\t" \
  "s_mul_i32 s84, s84, 15360\n\t" \
  "s_add_u32 s84, s84, 75776\n\t" \
  "v_add_u32 v210, s84, v202\n\t" \
  "v_and_b32 v221, 15, v203\n\t" \
  "v_mul_u32_u24 v221, 160, v221\n\t" \
  "v_lshrrev_b32 v222, 4, v203\n\t" \
  "v_lshl_add_u32 v221, v222, 3, v221\n\t" \
  "s_mul_i32 s84, %[w], 2560\n\t" \
  "s_add_u32 s84, s84, 152576\n\t" \
  "v_add_u32 v221, s84, v221\n\t" \
  "v_lshlrev_b32 v222, 4, v222\n\t" \
  "v_add_u32 v222, 151552, v222\n\t" \
  "s_mul_i32 s44, %[mt], 221184\n\t" \
  "s_mul_i32 s72, %[w], 57344\n\t" \
  "s_add_u32 s44, s44, s72\n\t" \
  "s_add_u32 s44, s44, 22331392\n\t" \
  "s_mov_b64 s[72:73], %[ws]\n\t" \
  "s_add_u32 s44, s44, s72\n\t" \
  "s_addc_u32 s45, s73, 0\n\t" \
  "s_and_b32 s45, s45, 0xffff\n\t" \
  "s_mov_b32 s47, 0x20000\n\t" \
  "s_mul_i32 s84, %[w], 57344\n\t" \
  "s_sub_u32 s46, 221184, s84\n\t" \
  "s_mul_i32 s48, %[mt], 81920\n\t" \
  "s_mul_i32 s72, %[w], 20480\n\t" \
  "s_add_u32 s48, s48, s72\n\t" \
  "s_mul_i32 s72, %[nt], 160\n\t" \
  "s_add_u32 s48, s48, s72\n\t" \
  "s_mov_b64 s[72:73], %[out]\n\t" \
  "s_add_u32 s48, s48, s72\n\t" \
  "s_addc_u32 s49, s73, 0\n\t" \
  "s_and_b32 s49, s49, 0xffff\n\t" \
  "s_mov_b32 s50, 0x4ba0\n\t" \
  "s_mov_b32 s51, 0x20000\n\t" \
  "v_add_u32 v216, 0, v203\n\t" \
  "v_mul_u32_u24 v217, 205, v216\n\t" \
  "v_lshrrev_b32 v217, 11, v217\n\t" \
  "v_mul_u32_u24 v217, 1120, v217\n\t" \
  "v_lshl_add_u32 v212, v216, 4, v217\n\t" \
  "v_add_u32 v216, 64, v203\n\t" \
  "v_mul_u32_u24 v217, 205, v216\n\t" \
  "v_lshrrev_b32 v217, 11, v217\n\t" \
  "v_mul_u32_u24 v217, 1120, v217\n\t" \
  "v_lshl_add_u32 v213, v216, 4, v217\n\t" \
  "v_add_u32 v216, 128, v203\n\t" \
  "v_mul_u32_u24 v217, 205, v216\n\t" \
  "v_lshrrev_b32 v217, 11, v217\n\t" \
  "v_mul_u32_u24 v217, 1120, v217\n\t" \
  "v_lshl_add_u32 v214, v216, 4, v217\n\t" \
  "s_mul_i32 s84, %[w], 2560\n\t" \
  "s_add_u32 s84, s84, 152576\n\t" \
  "v_add_u32 v215, s84, v202\n\t" \
  "s_lshr_b32 s86, %[mt], 2\n\t" \
  "s_cmp_lt_u32 %[w], 3\n\t" \
  "s_cselect_b32 s87, 6, 5\n\t" \
  "s_mul_i32 s68, s86, 4096\n\t" \
  "s_mul_i32 s72, %[w], 1024\n\t" \
  "s_add_u32 s68, s68, s72\n\t" \
  "s_add_u32 s68, s68, 42270720\n\t" \
  "s_mov_b64 s[72:73], %[ws]\n\t" \
  "s_add_u32 s68, s68, s72\n\t" \
  "s_addc_u32 s69, s73, 0\n\t" \
  "v_and_b32 v200, 7, v203\n\t" \
  "v_min_u32 v200, s87, v200\n\t" \
  "v_mul_u32_u24 v200, 128, v200\n\t" \
  "s_mov_b32 s84, 0\n\t" \
  ".Lpa%=:\n\t" \
  "global_load_dword v201, v200, s[68:69] sc1\n\t" \
  "s_waitcnt vmcnt(0)\n\t" \
  "v_cmp_ne_u32 vcc, %[tag], v201\n\t" \
  "s_cbranch_vccz .Lpad%=\n\t" \
  "s_sleep 1\n\t" \
  "s_add_u32 s84, s84, 1\n\t" \
  "s_cmp_lt_u32 s84, 30000\n\t" \
  "s_cbranch_scc1 .Lpa%=\n\t" \
  "v_mov_b32 v201, 0\n\t" \
  "v_mov_b32 v202, 1\n\t" \
  "global_store_dword v201, v202, s[70:71] sc1\n\t" \
  "s_waitcnt vmcnt(0)\n\t" \
  ".Lpad%=:\n\t" \
  "s_mov_b32 s76, 0\n\t" \
  "buffer_load_dwordx4 v[88:91], v204, s[44:47], s76 offen sc1\n\t" \
  "buffer_load_dwordx4 v[92:95], v205, s[44:47], s76 offen sc1\n\t" \
  "buffer_load_dwordx4 v[96:99], v206, s[44:47], s76 offen sc1\n\t" \
  "buffer_load_dwordx4 v[100:103], v207, s[44:47], s76 offen sc1\n\t" \
  "s_mov_b32 s76, 4096\n\t" \
  "buffer_load_dwordx4 v[104:107], v204, s[44:47], s76 offen sc1\n\t" \
  "buffer_load_dwordx4 v[108:111], v205, s[44:47], s76 offen sc1\n\t" \
  "buffer_load_dwordx4 v[112:115], v206, s[44:47], s76 offen sc1\n\t" \
  "buffer_load_dwordx4 v[116:119], v207, s[44:47], s76 offen sc1\n\t" \
  "s_mov_b32 s76, 8192\n\t" \
  "buffer_load_dwordx4 v[120:123], v204, s[44:47], s76 offen sc1\n\t" \
  "buffer_load_dwordx4 v[124:127], v205, s[44:47], s76 offen sc1\n\t" \
  "buffer_load_dwordx4 v[128:131], v206, s[44:47], s76 offen sc1\n\t" \
  "buffer_load_dwordx4 v[132:135], v207, s[44:47], s76 offen sc1\n\t" \
  "s_mov_b32 s76, 12288\n\t" \
  "buffer_load_dwordx4 v[136:139], v204, s[44:47], s76 offen sc1\n\t" \
  "buffer_load_dwordx4 v[140:143], v205, s[44:47], s76 offen sc1\n\t" \
  "buffer_load_dwordx4 v[144:147], v206, s[44:47], s76 offen sc1\n\t" \
  "buffer_load_dwordx4 v[148:151], v207, s[44:47], s76 offen sc1\n\t" \
  "s_mov_b32 s76, 16384\n\t" \
  "buffer_load_dwordx4 v[152:155], v204, s[44:47], s76 offen sc1\n\t" \
  "buffer_load_dwordx4 v[156:159], v205, s[44:47], s76 offen sc1\n\t" \
  "buffer_load_dwordx4 v[160:163], v206, s[44:47], s76 offen sc1\n\t" \
  "buffer_load_dwordx4 v[164:167], v207, s[44:47], s76 offen sc1\n\t" \
  "s_mov_b32 s76, 20480\n\t" \
  "buffer_load_dwordx4 v[168:171], v204, s[44:47], s76 offen sc1\n\t" \
  "buffer_load_dwordx4 v[172:175], v205, s[44:47], s76 offen sc1\n\t" \
  "buffer_load_dwordx4 v[176:179], v206, s[44:47], s76 offen sc1\n\t" \
  "buffer_load_dwordx4 v[180:183], v207, s[44:47], s76 offen sc1\n\t" \
  "s_mov_b32 s76, 24576\n\t" \
  "buffer_load_dwordx4 v[184:187], v204, s[44:47], s76 offen sc1\n\t" \
  "buffer_load_dwordx4 v[188:191], v205, s[44:47], s76 offen sc1\n\t" \
  "buffer_load_dwordx4 v[192:195], v206, s[44:47], s76 offen sc1\n\t" \
  "buffer_load_dwordx4 v[196:199], v207, s[44:47], s76 offen sc1\n\t" \
  "s_mov_b32 s76, 28672\n\t" \
  "buffer_load_dwordx4 v[248:251], v204, s[44:47], s76 offen sc1\n\t" \
  "buffer_load_dwordx4 v[252:255], v205, s[44:47], s76 offen sc1\n\t" \
  "s_waitcnt vmcnt(29)\n\t" \
  "v_mfma_f32_16x16x32_bf16 v[8:11], a[208:211], v[88:91], 0\n\t" \
  "s_waitcnt vmcnt(28)\n\t" \
  "v_mfma_f32_16x16x32_bf16 v[28:31], a[208:211], v[92:95], 0\n\t" \
  "s_waitcnt vmcnt(27)\n\t" \
  "v_mfma_f32_16x16x32_bf16 v[48:51], a[208:211], v[96:99], 0\n\t" \
  "s_waitcnt vmcnt(26)\n\t" \
  "v_mfma_f32_16x16x32_bf16 v[68:71], a[208:211], v[100:103], 0\n\t" \
  "v_mfma_f32_16x16x32_bf16 v[12:15], a[212:215], v[88:91], 0\n\t" \
  "buffer_load_dwordx4 a[208:211], v206, s[44:47], s76 offen sc1\n\t" \
  "v_mfma_f32_16x16x32_bf16 v[32:35], a[212:215], v[92:95], 0\n\t" \
  "v_mfma_f32_16x16x32_bf16 v[52:55], a[212:215], v[96:99], 0\n\t" \
  "v_mfma_f32_16x16x32_bf16 v[72:75], a[212:215], v[100:103], 0\n\t" \
  "v_mfma_f32_16x16x32_bf16 v[16:19], a[216:219], v[88:91], 0\n\t" \
  "buffer_load_dwordx4 a[212:215], v207, s[44:47], s76 offen sc1\n\t" \
  "v_mfma_f32_16x16x32_bf16 v[36:39], a[216:219], v[92:95], 0\n\t" \
  "v_mfma_f32_16x16x32_bf16 v[56:59], a[216:219], v[96:99], 0\n\t" \
  "v_mfma_f32_16x16x32_bf16 v[76:79], a[216:219], v[100:103], 0\n\t" \
  "s_mov_b32 s76, 32768\n\t" \
  "v_mfma_f32_16x16x32_bf16 v[20:23], a[220:223], v[88:91], 0\n\t" \
  "buffer_load_dwordx4 a[216:219], v204, s[44:47], s76 offen sc1\n\t" \
  "v_mfma_f32_16x16x32_bf16 v[40:43], a[220:223], v[92:95], 0\n\t" \
  "v_mfma_f32_16x16x32_bf16 v[60:63], a[220:223], v[96:99], 0\n\t" \
  "v_mfma_f32_16x16x32_bf16 v[80:83], a[220:223], v[100:103], 0\n\t" \
  "v_mfma_f32_16x16x32_bf16 v[24:27], a[128:131], v[88:91], 0\n\t" \
  "buffer_load_dwordx4 a[220:223], v205, s[44:47], s76 offen sc1\n\t" \
  "v_mfma_f32_16x16x32_bf16 v[44:47], a[128:131], v[92:95], 0\n\t" \
  "v_mfma_f32_16x16x32_bf16 v[64:67], a[128:131], v[96:99], 0\n\t" \
  "v_mfma_f32_16x16x32_bf16 v[84:87], a[128:131], v[100:103], 0\n\t" \
  "s_waitcnt vmcnt(29)\n\t" \
  "v_mfma_f32_16x16x32_bf16 v[8:11], a[132:135], v[104:107], v[8:11]\n\t" \
  "buffer_load_dwordx4 a[128:131], v206, s[44:47], s76 offen sc1\n\t" \
  "s_waitcnt vmcnt(29)\n\t" \
  "v_mfma_f32_16x16x32_bf16 v[28:31], a[132:135], v[108:111], v[28:31]\n\t" \
  "s_waitcnt vmcnt(28)\n\t" \
  "v_mfma_f32_16x16x32_bf16 v[48:51], a[132:135], v[112:115], v[48:51]\n\t" \
  "s_waitcnt vmcnt(27)\n\t" \
  "v_mfma_f32_16x16x32_bf16 v[68:71], a[132:135], v[116:119], v[68:71]\n\t" \
  "v_mfma_f32_16x16x32_bf16 v[12:15], a[136:139], v[104:107], v[12:15]\n\t" \
  "buffer_load_dwordx4 a[132:135], v207, s[44:47], s76 offen sc1\n\t" \
  "v_mfma_f32_16x16x32_bf16 v[32:35], a[136:139], v[108:111], v[32:35]\n\t" \
  "v_mfma_f32_16x16x32_bf16 v[52:55], a[136:139], v[112:115], v[52:55]\n\t" \
  "v_mfma_f32_16x16x32_bf16 v[72:75], a[136:139], v[116:119], v[72:75]\n\t" \
  "s_mov_b32 s76, 36864\n\t" \
  "v_mfma_f32_16x16x32_bf16 v[16:19], a[140:143], v[104:107], v[16:19]\n\t" \
  "buffer_load_dwordx4 a[136:139], v204, s[44:47], s76 offen sc1\n\t" \
  "v_mfma_f32_16x16x32_bf16 v[36:39], a[140:143], v[108:111], v[36:39]\n\t" \
  "v_mfma_f32_16x16x32_bf16 v[56:59], a[140:143], v[112:115], v[56:59]\n\t" \
  "v_mfma_f32_16x16x32_bf16 v[76:79], a[140:143], v[116:119], v[76:79]\n\t" \
  "v_mfma_f32_16x16x32_bf16 v[20:23], a[144:147], v[104:107], v[20:23]\n\t" \
  "buffer_load_dwordx4 a[140:143], v205, s[44:47], s76 offen sc1\n\t" \
  "v_mfma_f32_16x16x32_bf16 v[40:43], a[144:147], v[108:111], v[40:43]\n\t" \
  "v_mfma_f32_16x16x32_bf16 v[60:63], a[144:147], v[112:115], v[60:63]\n\t" \
  "v_mfma_f32_16x16x32_bf16 v[80:83], a[144:147], v[116:119], v[80:83]\n\t" \
  "v_mfma_f32_16x16x32_bf16 v[24:27], a[148:151], v[104:107], v[24:27]\n\t" \
  "buffer_load_dwordx4 a[144:147], v206, s[44:47], s76 offen sc1\n\t" \
  "v_mfma_f32_16x16x32_bf16 v[44:47], a[148:151], v[108:111], v[44:47]\n\t" \
  "v_mfma_f32_16x16x32_bf16 v[64:67], a[148:151], v[112:115], v[64:67]\n\t" \
  "v_mfma_f32_16x16x32_bf16 v[84:87], a[148:151], v[116:119], v[84:87]\n\t" \
  "s_waitcnt vmcnt(30)\n\t" \
  "v_mfma_f32_16x16x32_bf16 v[8:11], a[152:155], v[120:123], v[8:11]\n\t" \
  "buffer_load_dwordx4 a[148:151], v207, s[44:47], s76 offen sc1\n\t" \
  "s_waitcnt vmcnt(30)\n\t" \
  "v_mfma_f32_16x16x32_bf16 v[28:31], a[152:155], v[124:127], v[28:31]\n\t" \
  "s_waitcnt vmcnt(29)\n\t" \
  "v_mfma_f32_16x16x32_bf16 v[48:51], a[152:155], v[128:131], v[48:51]\n\t" \
  "s_waitcnt vmcnt(28)\n\t" \
  "v_mfma_f32_16x16x32_bf16 v[68:71], a[152:155], v[132:135], v[68:71]\n\t" \
  "s_mov_b32 s76, 40960\n\t" \
  "v_mfma_f32_16x16x32_bf16 v[12:15], a[156:159], v[120:123], v[12:15]\n\t" \
  "buffer_load_dwordx4 a[152:155], v204, s[44:47], s76 offen sc1\n\t" \
  "v_mfma_f32_16x16x32_bf16 v[32:35], a[156:159], v[124:127], v[32:35]\n\t" \
  "v_mfma_f32_16x16x32_bf16 v[52:55], a[156:159], v[128:131], v[52:55]\n\t" \
  "v_mfma_f32_16x16x32_bf16 v[72:75], a[156:159], v[132:135], v[72:75]\n\t" \
  "v_mfma_f32_16x16x32_bf16 v[16:19], a[160:163], v[120:123], v[16:19]\n\t" \
  "buffer_load_dwordx4 a[156:159], v205, s[44:47], s76 offen sc1\n\t" \
  "v_mfma_f32_16x16x32_bf16 v[36:39], a[160:163], v[124:127], v[36:39]\n\t" \
  "v_mfma_f32_16x16x32_bf16 v[56:59], a[160:163], v[128:131], v[56:59]\n\t" \
  "v_mfma_f32_16x16x32_bf16 v[76:79], a[160:163], v[132:135], v[76:79]\n\t" \
  "v_mfma_f32_16x16x32_bf16 v[20:23], a[164:167], v[120:123], v[20:23]\n\t" \
  "buffer_load_dwordx4 a[160:163], v206, s[44:47], s76 offen sc1\n\t" \
  "v_mfma_f32_16x16x32_bf16 v[40:43], a[164:167], v[124:127], v[40:43]\n\t" \
  "v_mfma_f32_16x16x32_bf16 v[60:63], a[164:167], v[128:131], v[60:63]\n\t" \
  "v_mfma_f32_16x16x32_bf16 v[80:83], a[164:167], v[132:135], v[80:83]\n\t" \
  "v_mfma_f32_16x16x32_bf16 v[24:27], a[168:171], v[120:123], v[24:27]\n\t" \
  "buffer_load_dwordx4 a[164:167], v207, s[44:47], s76 offen sc1\n\t" \
  "v_mfma_f32_16x16x32_bf16 v[44:47], a[168:171], v[124:127], v[44:47]\n\t" \
  "v_mfma_f32_16x16x32_bf16 v[64:67], a[168:171], v[128:131], v[64:67]\n\t" \
  "v_mfma_f32_16x16x32_bf16 v[84:87], a[168:171], v[132:135], v[84:87]\n\t" \
  "s_waitcnt vmcnt(31)\n\t" \
  "v_mfma_f32_16x16x32_bf16 v[8:11], a[172:175], v[136:139], v[8:11]\n\t" \
  "s_mov_b32 s76, 45056\n\t" \
  "buffer_load_dwordx4 a[168:171], v204, s[44:47], s76 offen sc1\n\t" \
  "s_waitcnt vmcnt(31)\n\t" \
  "v_mfma_f32_16x16x32_bf16 v[28:31], a[172:175], v[140:143], v[28:31]\n\t" \
  "s_waitcnt vmcnt(30)\n\t" \
  "v_mfma_f32_16x16x32_bf16 v[48:51], a[172:175], v[144:147], v[48:51]\n\t" \
  "s_waitcnt vmcnt(29)\n\t" \
  "v_mfma_f32_16x16x32_bf16 v[68:71], a[172:175], v[148:151], v[68:71]\n\t" \
  "v_mfma_f32_16x16x32_bf16 v[12:15], a[176:179], v[136:139], v[12:15]\n\t" \
  "buffer_load_dwordx4 a[172:175], v205, s[44:47], s76 offen sc1\n\t" \
  "v_mfma_f32_16x16x32_bf16 v[32:35], a[176:179], v[140:143], v[32:35]\n\t" \
  "v_mfma_f32_16x16x32_bf16 v[52:55], a[176:179], v[144:147], v[52:55]\n\t" \
  "v_mfma_f32_16x16x32_bf16 v[72:75], a[176:179], v[148:151], v[72:75]\n\t" \
  "v_mfma_f32_16x16x32_bf16 v[16:19], a[180:183], v[136:139], v[16:19]\n\t" \
  "buffer_load_dwordx4 a[176:179], v206, s[44:47], s76 offen sc1\n\t" \
  "v_mfma_f32_16x16x32_bf16 v[36:39], a[180:183], v[140:143], v[36:39]\n\t" \
  "v_mfma_f32_16x16x32_bf16 v[56:59], a[180:183], v[144:147], v[56:59]\n\t" \
  "v_mfma_f32_16x16x32_bf16 v[76:79], a[180:183], v[148:151], v[76:79]\n\t" \
  "v_mfma_f32_16x16x32_bf16 v[20:23], a[184:187], v[136:139], v[20:23]\n\t" \
  "buffer_load_dwordx4 a[180:183], v207, s[44:47], s76 offen sc1\n\t" \
  "v_mfma_f32_16x16x32_bf16 v[40:43], a[184:187], v[140:143], v[40:43]\n\t" \
  "v_mfma_f32_16x16x32_bf16 v[60:63], a[184:187], v[144:147], v[60:63]\n\t" \
  "v_mfma_f32_16x16x32_bf16 v[80:83], a[184:187], v[148:151], v[80:83]\n\t" \
  "s_mov_b32 s76, 49152\n\t" \
  "v_mfma_f32_16x16x32_bf16 v[24:27], a[188:191], v[136:139], v[24:27]\n\t" \
  "buffer_load_dwordx4 a[184:187], v204, s[44:47], s76 offen sc1\n\t" \
  "v_mfma_f32_16x16x32_bf16 v[44:47], a[188:191], v[140:143], v[44:47]\n\t" \
  "v_mfma_f32_16x16x32_bf16 v[64:67], a[188:191], v[144:147], v[64:67]\n\t" \
  "buffer_load_dwordx4 v[140:143], v205, s[44:47], s76 offen sc1\n\t" \
  "v_mfma_f32_16x16x32_bf16 v[84:87], a[188:191], v[148:151], v[84:87]\n\t" \
  "s_waitcnt vmcnt(33)\n\t" \
  "v_mfma_f32_16x16x32_bf16 v[8:11], a[192:195], v[152:155], v[8:11]\n\t" \
  "buffer_load_dwordx4 v[144:147], v206, s[44:47], s76 offen sc1\n\t" \
  "s_waitcnt vmcnt(33)\n\t" \
  "v_mfma_f32_16x16x32_bf16 v[28:31], a[192:195], v[156:159], v[28:31]\n\t" \
  "s_waitcnt vmcnt(32)\n\t" \
  "v_mfma_f32_16x16x32_bf16 v[48:51], a[192:195], v[160:163], v[48:51]\n\t" \
  "buffer_load_dwordx4 a[188:191], v207, s[44:47], s76 offen sc1\n\t" \
  "s_waitcnt vmcnt(32)\n\t" \
  "v_mfma_f32_16x16x32_bf16 v[68:71], a[192:195], v[164:167], v[68:71]\n\t" \
  "s_mov_b32 s76, 53248\n\t" \
  "v_mfma_f32_16x16x32_bf16 v[12:15], a[196:199], v[152:155], v[12:15]\n\t" \
  "buffer_load_dwordx4 v[148:151], v204, s[44:47], s76 offen sc1\n\t" \
  "v_mfma_f32_16x16x32_bf16 v[32:35], a[196:199], v[156:159], v[32:35]\n\t" \
  "v_mfma_f32_16x16x32_bf16 v[52:55], a[196:199], v[160:163], v[52:55]\n\t" \
  "buffer_load_dwordx4 a[192:195], v205, s[44:47], s76 offen sc1\n\t" \
  "v_mfma_f32_16x16x32_bf16 v[72:75], a[196:199], v[164:167], v[72:75]\n\t" \
  "v_mfma_f32_16x16x32_bf16 v[16:19], a[200:203], v[152:155], v[16:19]\n\t" \
  "buffer_load_dwordx4 a[196:199], v206, s[44:47], s76 offen sc1\n\t" \
  "v_mfma_f32_16x16x32_bf16 v[36:39], a[200:203], v[156:159], v[36:39]\n\t" \
  "v_mfma_f32_16x16x32_bf16 v[56:59], a[200:203], v[160:163], v[56:59]\n\t" \
  "v_mfma_f32_16x16x32_bf16 v[76:79], a[200:203], v[164:167], v[76:79]\n\t" \
  "v_mfma_f32_16x16x32_bf16 v[20:23], a[204:207], v[152:155], v[20:23]\n\t" \
  "buffer_load_dwordx4 a[200:203], v207, s[44:47], s76 offen sc1\n\t" \
  "v_mfma_f32_16x16x32_bf16 v[40:43], a[204:207], v[156:159], v[40:43]\n\t" \
  "v_mfma_f32_16x16x32_bf16 v[60:63], a[204:207], v[160:163], v[60:63]\n\t" \
  "v_mfma_f32_16x16x32_bf16 v[80:83], a[204:207], v[164:167], v[80:83]\n\t" \
  "v_mfma_f32_16x16x32_bf16 v[24:27], a[224:227], v[152:155], v[24:27]\n\t" \
  "v_mfma_f32_16x16x32_bf16 v[44:47], a[224:227], v[156:159], v[44:47]\n\t" \
  "v_mfma_f32_16x16x32_bf16 v[64:67], a[224:227], v[160:163], v[64:67]\n\t" \
  "v_mfma_f32_16x16x32_bf16 v[84:87], a[224:227], v[164:167], v[84:87]\n\t" \
  "s_waitcnt vmcnt(35)\n\t" \
  "v_mfma_f32_16x16x32_bf16 v[8:11], a[228:231], v[168:171], v[8:11]\n\t" \
  "s_waitcnt vmcnt(34)\n\t" \
  "v_mfma_f32_16x16x32_bf16 v[28:31], a[228:231], v[172:175], v[28:31]\n\t" \
  "s_waitcnt vmcnt(33)\n\t" \
  "v_mfma_f32_16x16x32_bf16 v[48:51], a[228:231], v[176:179], v[48:51]\n\t" \
  "s_waitcnt vmcnt(32)\n\t" \
  "v_mfma_f32_16x16x32_bf16 v[68:71], a[228:231], v[180:183], v[68:71]\n\t" \
  "v_mfma_f32_16x16x32_bf16 v[12:15], a[232:235], v[168:171], v[12:15]\n\t" \
  "v_mfma_f32_16x16x32_bf16 v[32:35], a[232:235], v[172:175], v[32:35]\n\t" \
  "v_mfma_f32_16x16x32_bf16 v[52:55], a[232:235], v[176:179], v[52:55]\n\t" \
  "v_mfma_f32_16x16x32_bf16 v[72:75], a[232:235], v[180:183], v[72:75]\n\t" \
  "v_mfma_f32_16x16x32_bf16 v[16:19], a[236:239], v[168:171], v[16:19]\n\t" \
  "v_mfma_f32_16x16x32_bf16 v[36:39], a[236:239], v[172:175], v[36:39]\n\t" \
  "v_mfma_f32_16x16x32_bf16 v[56:59], a[236:239], v[176:179], v[56:59]\n\t" \
  "v_mfma_f32_16x16x32_bf16 v[76:79], a[236:239], v[180:183], v[76:79]\n\t" \
  "v_mfma_f32_16x16x32_bf16 v[20:23], a[240:243], v[168:171], v[20:23]\n\t" \
  "v_mfma_f32_16x16x32_bf16 v[40:43], a[240:243], v[172:175], v[40:43]\n\t" \
  "v_mfma_f32_16x16x32_bf16 v[60:63], a[240:243], v[176:179], v[60:63]\n\t" \
  "v_mfma_f32_16x16x32_bf16 v[80:83], a[240:243], v[180:183], v[80:83]\n\t" \
  "v_mfma_f32_16x16x32_bf16 v[24:27], a[244:247], v[168:171], v[24:27]\n\t" \
  "v_mfma_f32_16x16x32_bf16 v[44:47], a[244:247], v[172:175], v[44:47]\n\t" \
  "v_mfma_f32_16x16x32_bf16 v[64:67], a[244:247], v[176:179], v[64:67]\n\t" \
  "v_mfma_f32_16x16x32_bf16 v[84:87], a[244:247], v[180:183], v[84:87]\n\t" \
  "s_waitcnt vmcnt(31)\n\t" \
  "v_mfma_f32_16x16x32_bf16 v[8:11], a[248:251], v[184:187], v[8:11]\n\t" \
  "s_waitcnt vmcnt(30)\n\t" \
  "v_mfma_f32_16x16x32_bf16 v[28:31], a[248:251], v[188:191], v[28:31]\n\t" \
  "s_waitcnt vmcnt(29)\n\t" \
  "v_mfma_f32_16x16x32_bf16 v[48:51], a[248:251], v[192:195], v[48:51]\n\t" \
  "s_waitcnt vmcnt(28)\n\t" \
  "v_mfma_f32_16x16x32_bf16 v[68:71], a[248:251], v[196:199], v[68:71]\n\t" \
  "v_mfma_f32_16x16x32_bf16 v[12:15], a[252:255], v[184:187], v[12:15]\n\t" \
  "v_mfma_f32_16x16x32_bf16 v[32:35], a[252:255], v[188:191], v[32:35]\n\t" \
  "v_mfma_f32_16x16x32_bf16 v[52:55], a[252:255], v[192:195], v[52:55]\n\t" \
  "v_mfma_f32_16x16x32_bf16 v[72:75], a[252:255], v[196:199], v[72:75]\n\t" \
  "v_mfma_f32_16x16x32_bf16 v[16:19], a[64:67], v[184:187], v[16:19]\n\t" \
  "v_mfma_f32_16x16x32_bf16 v[36:39], a[64:67], v[188:191], v[36:39]\n\t" \
  "v_mfma_f32_16x16x32_bf16 v[56:59], a[64:67], v[192:195], v[56:59]\n\t" \
  "v_mfma_f32_16x16x32_bf16 v[76:79], a[64:67], v[196:199], v[76:79]\n\t" \
  "v_mfma_f32_16x16x32_bf16 v[20:23], a[68:71], v[184:187], v[20:23]\n\t" \
  "v_mfma_f32_16x16x32_bf16 v[40:43], a[68:71], v[188:191], v[40:43]\n\t" \
  "v_mfma_f32_16x16x32_bf16 v[60:63], a[68:71], v[192:195], v[60:63]\n\t" \
  "v_mfma_f32_16x16x32_bf16 v[80:83], a[68:71], v[196:199], v[80:83]\n\t" \
  "v_mfma_f32_16x16x32_bf16 v[24:27], a[72:75], v[184:187], v[24:27]\n\t" \
  "v_mfma_f32_16x16x32_bf16 v[44:47], a[72:75], v[188:191], v[44:47]\n\t" \
  "v_mfma_f32_16x16x32_bf16 v[64:67], a[72:75], v[192:195], v[64:67]\n\t" \
  "v_mfma_f32_16x16x32_bf16 v[84:87], a[72:75], v[196:199], v[84:87]\n\t" \
  "s_waitcnt vmcnt(27)\n\t" \
  "v_mfma_f32_16x16x32_bf16 v[8:11], a[76:79], v[248:251], v[8:11]\n\t" \
  "s_waitcnt vmcnt(26)\n\t" \
  "v_mfma_f32_16x16x32_bf16 v[28:31], a[76:79], v[252:255], v[28:31]\n\t" \
  "s_waitcnt vmcnt(25)\n\t" \
  "v_mfma_f32_16x16x32_bf16 v[48:51], a[76:79], a[208:211], v[48:51]\n\t" \
  "s_waitcnt vmcnt(24)\n\t" \
  "v_mfma_f32_16x16x32_bf16 v[68:71], a[76:79], a[212:215], v[68:71]\n\t" \
  "v_mfma_f32_16x16x32_bf16 v[12:15], a[80:83], v[248:251], v[12:15]\n\t" \
  "v_mfma_f32_16x16x32_bf16 v[32:35], a[80:83], v[252:255], v[32:35]\n\t" \
  "v_mfma_f32_16x16x32_bf16 v[52:55], a[80:83], a[208:211], v[52:55]\n\t" \
  "v_mfma_f32_16x16x32_bf16 v[72:75], a[80:83], a[212:215], v[72:75]\n\t" \
  "v_mfma_f32_16x16x32_bf16 v[16:19], a[84:87], v[248:251], v[16:19]\n\t" \
  "v_mfma_f32_16x16x32_bf16 v[36:39], a[84:87], v[252:255], v[36:39]\n\t" \
  "v_mfma_f32_16x16x32_bf16 v[56:59], a[84:87], a[208:211], v[56:59]\n\t" \
  "v_mfma_f32_16x16x32_bf16 v[76:79], a[84:87], a[212:215], v[76:79]\n\t" \
  "v_mfma_f32_16x16x32_bf16 v[20:23], a[88:91], v[248:251], v[20:23]\n\t" \
  "v_mfma_f32_16x16x32_bf16 v[40:43], a[88:91], v[252:255], v[40:43]\n\t" \
  "v_mfma_f32_16x16x32_bf16 v[60:63], a[88:91], a[208:211], v[60:63]\n\t" \
  "v_mfma_f32_16x16x32_bf16 v[80:83], a[88:91], a[212:215], v[80:83]\n\t" \
  "v_mfma_f32_16x16x32_bf16 v[24:27], a[92:95], v[248:251], v[24:27]\n\t" \
  "v_mfma_f32_16x16x32_bf16 v[44:47], a[92:95], v[252:255], v[44:47]\n\t" \
  "v_mfma_f32_16x16x32_bf16 v[64:67], a[92:95], a[208:211], v[64:67]\n\t" \
  "v_mfma_f32_16x16x32_bf16 v[84:87], a[92:95], a[212:215], v[84:87]\n\t" \
  "s_waitcnt vmcnt(23)\n\t" \
  "v_mfma_f32_16x16x32_bf16 v[8:11], a[96:99], a[216:219], v[8:11]\n\t" \
  "s_waitcnt vmcnt(22)\n\t" \
  "v_mfma_f32_16x16x32_bf16 v[28:31], a[96:99], a[220:223], v[28:31]\n\t" \
  "s_waitcnt vmcnt(21)\n\t" \
  "v_mfma_f32_16x16x32_bf16 v[48:51], a[96:99], a[128:131], v[48:51]\n\t" \
  "s_waitcnt vmcnt(20)\n\t" \
  "v_mfma_f32_16x16x32_bf16 v[68:71], a[96:99], a[132:135], v[68:71]\n\t" \
  "v_mfma_f32_16x16x32_bf16 v[12:15], a[100:103], a[216:219], v[12:15]\n\t" \
  "v_mfma_f32_16x16x32_bf16 v[32:35], a[100:103], a[220:223], v[32:35]\n\t" \
  "v_mfma_f32_16x16x32_bf16 v[52:55], a[100:103], a[128:131], v[52:55]\n\t" \
  "v_mfma_f32_16x16x32_bf16 v[72:75], a[100:103], a[132:135], v[72:75]\n\t" \
  "v_mfma_f32_16x16x32_bf16 v[16:19], a[0:3], a[216:219], v[16:19]\n\t" \
  "v_mfma_f32_16x16x32_bf16 v[36:39], a[0:3], a[220:223], v[36:39]\n\t" \
  "v_mfma_f32_16x16x32_bf16 v[56:59], a[0:3], a[128:131], v[56:59]\n\t" \
  "v_mfma_f32_16x16x32_bf16 v[76:79], a[0:3], a[132:135], v[76:79]\n\t" \
  "v_mfma_f32_16x16x32_bf16 v[20:23], a[4:7], a[216:219], v[20:23]\n\t" \
  "v_mfma_f32_16x16x32_bf16 v[40:43], a[4:7], a[220:223], v[40:43]\n\t" \
  "v_mfma_f32_16x16x32_bf16 v[60:63], a[4:7], a[128:131], v[60:63]\n\t" \
  "v_mfma_f32_16x16x32_bf16 v[80:83], a[4:7], a[132:135], v[80:83]\n\t" \
  "v_mfma_f32_16x16x32_bf16 v[24:27], a[8:11], a[216:219], v[24:27]\n\t" \
  "v_mfma_f32_16x16x32_bf16 v[44:47], a[8:11], a[220:223], v[44:47]\n\t" \
  "v_mfma_f32_16x16x32_bf16 v[64:67], a[8:11], a[128:131], v[64:67]\n\t" \
  "v_mfma_f32_16x16x32_bf16 v[84:87], a[8:11], a[132:135], v[84:87]\n\t" \
  "s_waitcnt vmcnt(19)\n\t" \
  "v_mfma_f32_16x16x32_bf16 v[8:11], a[12:15], a[136:139], v[8:11]\n\t" \
  "s_waitcnt vmcnt(18)\n\t" \
  "v_mfma_f32_16x16x32_bf16 v[28:31], a[12:15], a[140:143], v[28:31]\n\t" \
  "s_waitcnt vmcnt(17)\n\t" \
  "v_mfma_f32_16x16x32_bf16 v[48:51], a[12:15], a[144:147], v[48:51]\n\t" \
  "s_waitcnt vmcnt(16)\n\t" \
  "v_mfma_f32_16x16x32_bf16 v[68:71], a[12:15], a[148:151], v[68:71]\n\t" \
  "v_mfma_f32_16x16x32_bf16 v[12:15], a[16:19], a[136:139], v[12:15]\n\t" \
  "v_mfma_f32_16x16x32_bf16 v[32:35], a[16:19], a[140:143], v[32:35]\n\t" \
  "v_mfma_f32_16x16x32_bf16 v[52:55], a[16:19], a[144:147], v[52:55]\n\t" \
  "v_mfma_f32_16x16x32_bf16 v[72:75], a[16:19], a[148:151], v[72:75]\n\t" \
  "v_mfma_f32_16x16x32_bf16 v[16:19], a[20:23], a[136:139], v[16:19]\n\t" \
  "v_mfma_f32_16x16x32_bf16 v[36:39], a[20:23], a[140:143], v[36:39]\n\t" \
  "v_mfma_f32_16x16x32_bf16 v[56:59], a[20:23], a[144:147], v[56:59]\n\t" \
  "v_mfma_f32_16x16x32_bf16 v[76:79], a[20:23], a[148:151], v[76:79]\n\t" \
  "v_mfma_f32_16x16x32_bf16 v[20:23], a[24:27], a[136:139], v[20:23]\n\t" \
  "v_mfma_f32_16x16x32_bf16 v[40:43], a[24:27], a[140:143], v[40:43]\n\t" \
  "v_mfma_f32_16x16x32_bf16 v[60:63], a[24:27], a[144:147], v[60:63]\n\t" \
  "v_mfma_f32_16x16x32_bf16 v[80:83], a[24:27], a[148:151], v[80:83]\n\t" \
  "v_mfma_f32_16x16x32_bf16 v[24:27], a[28:31], a[136:139], v[24:27]\n\t" \
  "v_mfma_f32_16x16x32_bf16 v[44:47], a[28:31], a[140:143], v[44:47]\n\t" \
  "v_mfma_f32_16x16x32_bf16 v[64:67], a[28:31], a[144:147], v[64:67]\n\t" \
  "v_mfma_f32_16x16x32_bf16 v[84:87], a[28:31], a[148:151], v[84:87]\n\t" \
  "s_waitcnt vmcnt(15)\n\t" \
  "v_mfma_f32_16x16x32_bf16 v[8:11], a[32:35], a[152:155], v[8:11]\n\t" \
  "ds_read_b64 v[88:89], v221 offset:0\n\t" \
  "s_waitcnt vmcnt(14)\n\t" \
  "v_mfma_f32_16x16x32_bf16 v[28:31], a[32:35], a[156:159], v[28:31]\n\t" \
  "ds_read_b64 v[90:91], v221 offset:32\n\t" \
  "s_waitcnt vmcnt(13)\n\t" \
  "v_mfma_f32_16x16x32_bf16 v[48:51], a[32:35], a[160:163], v[48:51]\n\t" \
  "ds_read_b64 v[92:93], v221 offset:64\n\t" \
  "s_waitcnt vmcnt(12)\n\t" \
  "v_mfma_f32_16x16x32_bf16 v[68:71], a[32:35], a[164:167], v[68:71]\n\t" \
  "ds_read_b64 v[94:95], v221 offset:96\n\t" \
  "v_mfma_f32_16x16x32_bf16 v[12:15], a[36:39], a[152:155], v[12:15]\n\t" \
  "ds_read_b64 v[96:97], v221 offset:128\n\t" \
  "v_mfma_f32_16x16x32_bf16 v[32:35], a[36:39], a[156:159], v[32:35]\n\t" \
  "ds_read_b128 v[100:103], v222 offset:640\n\t" \
  "v_mfma_f32_16x16x32_bf16 v[52:55], a[36:39], a[160:163], v[52:55]\n\t" \
  "ds_read_b128 v[104:107], v222 offset:704\n\t" \
  "v_mfma_f32_16x16x32_bf16 v[72:75], a[36:39], a[164:167], v[72:75]\n\t" \
  "ds_read_b128 v[108:111], v222 offset:768\n\t" \
  "v_mfma_f32_16x16x32_bf16 v[16:19], a[40:43], a[152:155], v[16:19]\n\t" \
  "ds_read_b128 v[112:115], v222 offset:832\n\t" \
  "v_mfma_f32_16x16x32_bf16 v[36:39], a[40:43], a[156:159], v[36:39]\n\t" \
  "ds_read_b128 v[116:119], v222 offset:896\n\t" \
  "v_mfma_f32_16x16x32_bf16 v[56:59], a[40:43], a[160:163], v[56:59]\n\t" \
  "v_mfma_f32_16x16x32_bf16 v[76:79], a[40:43], a[164:167], v[76:79]\n\t" \
  "v_mfma_f32_16x16x32_bf16 v[20:23], a[44:47], a[152:155], v[20:23]\n\t" \
  "v_mfma_f32_16x16x32_bf16 v[40:43], a[44:47], a[156:159], v[40:43]\n\t" \
  "v_mfma_f32_16x16x32_bf16 v[60:63], a[44:47], a[160:163], v[60:63]\n\t" \
  "v_mfma_f32_16x16x32_bf16 v[80:83], a[44:47], a[164:167], v[80:83]\n\t" \
  "v_mfma_f32_16x16x32_bf16 v[24:27], a[48:51], a[152:155], v[24:27]\n\t" \
  "v_mfma_f32_16x16x32_bf16 v[44:47], a[48:51], a[156:159], v[44:47]\n\t" \
  "v_mfma_f32_16x16x32_bf16 v[64:67], a[48:51], a[160:163], v[64:67]\n\t" \
  "v_mfma_f32_16x16x32_bf16 v[84:87], a[48:51], a[164:167], v[84:87]\n\t" \
  "s_waitcnt vmcnt(11)\n\t" \
  "v_mfma_f32_16x16x32_bf16 v[8:11], a[52:55], a[168:171], v[8:11]\n\t" \
  "s_waitcnt vmcnt(10)\n\t" \
  "v_mfma_f32_16x16x32_bf16 v[28:31], a[52:55], a[172:175], v[28:31]\n\t" \
  "s_waitcnt vmcnt(9)\n\t" \
  "v_mfma_f32_16x16x32_bf16 v[48:51], a[52:55], a[176:179], v[48:51]\n\t" \
  "s_waitcnt vmcnt(8)\n\t" \
  "v_mfma_f32_16x16x32_bf16 v[68:71], a[52:55], a[180:183], v[68:71]\n\t" \
  "v_mfma_f32_16x16x32_bf16 v[12:15], a[56:59], a[168:171], v[12:15]\n\t" \
  "v_mfma_f32_16x16x32_bf16 v[32:35], a[56:59], a[172:175], v[32:35]\n\t" \
  "v_mfma_f32_16x16x32_bf16 v[52:55], a[56:59], a[176:179], v[52:55]\n\t" \
  "v_mfma_f32_16x16x32_bf16 v[72:75], a[56:59], a[180:183], v[72:75]\n\t" \
  "v_mfma_f32_16x16x32_bf16 v[16:19], a[60:63], a[168:171], v[16:19]\n\t" \
  "v_mfma_f32_16x16x32_bf16 v[36:39], a[60:63], a[172:175], v[36:39]\n\t" \
  "v_mfma_f32_16x16x32_bf16 v[56:59], a[60:63], a[176:179], v[56:59]\n\t" \
  "v_mfma_f32_16x16x32_bf16 v[76:79], a[60:63], a[180:183], v[76:79]\n\t" \
  "v_mfma_f32_16x16x32_bf16 v[20:23], a[104:107], a[168:171], v[20:23]\n\t" \
  "v_mfma_f32_16x16x32_bf16 v[40:43], a[104:107], a[172:175], v[40:43]\n\t" \
  "v_mfma_f32_16x16x32_bf16 v[60:63], a[104:107], a[176:179], v[60:63]\n\t" \
  "v_mfma_f32_16x16x32_bf16 v[80:83], a[104:107], a[180:183], v[80:83]\n\t" \
  "v_mfma_f32_16x16x32_bf16 v[24:27], a[108:111], a[168:171], v[24:27]\n\t" \
  "v_mfma_f32_16x16x32_bf16 v[44:47], a[108:111], a[172:175], v[44:47]\n\t" \
  "v_mfma_f32_16x16x32_bf16 v[64:67], a[108:111], a[176:179], v[64:67]\n\t" \
  "v_mfma_f32_16x16x32_bf16 v[84:87], a[108:111], a[180:183], v[84:87]\n\t" \
  "s_waitcnt vmcnt(7)\n\t" \
  "v_mfma_f32_16x16x32_bf16 v[8:11], a[112:115], a[184:187], v[8:11]\n\t" \
  "s_waitcnt lgkmcnt(0)\n\t" \
  "v_lshlrev_b32 v120, 16, v88\n\t" \
  "v_and_b32 v121, 0xffff0000, v88\n\t" \
  "s_waitcnt vmcnt(6)\n\t" \
  "v_mfma_f32_16x16x32_bf16 v[28:31], a[112:115], v[140:143], v[28:31]\n\t" \
  "v_lshlrev_b32 v122, 16, v89\n\t" \
  "v_and_b32 v123, 0xffff0000, v89\n\t" \
  "s_waitcnt vmcnt(5)\n\t" \
  "v_mfma_f32_16x16x32_bf16 v[48:51], a[112:115], v[144:147], v[48:51]\n\t" \
  "v_lshlrev_b32 v124, 16, v90\n\t" \
  "v_and_b32 v125, 0xffff0000, v90\n\t" \
  "s_waitcnt vmcnt(4)\n\t" \
  "v_mfma_f32_16x16x32_bf16 v[68:71], a[112:115], a[188:191], v[68:71]\n\t" \
  "v_lshlrev_b32 v126, 16, v91\n\t" \
  "v_and_b32 v127, 0xffff0000, v91\n\t" \
  "v_mfma_f32_16x16x32_bf16 v[12:15], a[116:119], a[184:187], v[12:15]\n\t" \
  "v_lshlrev_b32 v128, 16, v92\n\t" \
  "v_and_b32 v129, 0xffff0000, v92\n\t" \
  "v_mfma_f32_16x16x32_bf16 v[32:35], a[116:119], v[140:143], v[32:35]\n\t" \
  "v_lshlrev_b32 v130, 16, v93\n\t" \
  "v_and_b32 v131, 0xffff0000, v93\n\t" \
  "v_mfma_f32_16x16x32_bf16 v[52:55], a[116:119], v[144:147], v[52:55]\n\t" \
  "v_lshlrev_b32 v132, 16, v94\n\t" \
  "v_and_b32 v133, 0xffff0000, v94\n\t" \
  "v_mfma_f32_16x16x32_bf16 v[72:75], a[116:119], a[188:191], v[72:75]\n\t" \
  "v_lshlrev_b32 v134, 16, v95\n\t" \
  "v_and_b32 v135, 0xffff0000, v95\n\t" \
  "v_mfma_f32_16x16x32_bf16 v[16:19], a[120:123], a[184:187], v[16:19]\n\t" \
  "v_lshlrev_b32 v136, 16, v96\n\t" \
  "v_and_b32 v137, 0xffff0000, v96\n\t" \
  "v_mfma_f32_16x16x32_bf16 v[36:39], a[120:123], v[140:143], v[36:39]\n\t" \
  "v_lshlrev_b32 v138, 16, v97\n\t" \
  "v_and_b32 v139, 0xffff0000, v97\n\t" \
  "v_mfma_f32_16x16x32_bf16 v[56:59], a[120:123], v[144:147], v[56:59]\n\t" \
  "v_mfma_f32_16x16x32_bf16 v[76:79], a[120:123], a[188:191], v[76:79]\n\t" \
  "v_mfma_f32_16x16x32_bf16 v[20:23], a[124:127], a[184:187], v[20:23]\n\t" \
  "v_mfma_f32_16x16x32_bf16 v[40:43], a[124:127], v[140:143], v[40:43]\n\t" \
  "v_mfma_f32_16x16x32_bf16 v[60:63], a[124:127], v[144:147], v[60:63]\n\t" \
  "v_mfma_f32_16x16x32_bf16 v[80:83], a[124:127], a[188:191], v[80:83]\n\t" \
  "v_mfma_f32_16x16x32_bf16 v[24:27], v[224:227], a[184:187], v[24:27]\n\t" \
  "v_mfma_f32_16x16x32_bf16 v[44:47], v[224:227], v[140:143], v[44:47]\n\t" \
  "v_mfma_f32_16x16x32_bf16 v[64:67], v[224:227], v[144:147], v[64:67]\n\t" \
  "v_mfma_f32_16x16x32_bf16 v[84:87], v[224:227], a[188:191], v[84:87]\n\t" \
  "s_waitcnt vmcnt(3)\n\t" \
  "v_mfma_f32_16x16x32_bf16 v[8:11], v[228:231], v[148:151], v[8:11]\n\t" \
  "s_waitcnt vmcnt(2)\n\t" \
  "v_mfma_f32_16x16x32_bf16 v[28:31], v[228:231], a[192:195], v[28:31]\n\t" \
  "s_waitcnt vmcnt(1)\n\t" \
  "v_mfma_f32_16x16x32_bf16 v[48:51], v[228:231], a[196:199], v[48:51]\n\t" \
  "s_waitcnt vmcnt(0)\n\t" \
  "v_mfma_f32_16x16x32_bf16 v[68:71], v[228:231], a[200:203], v[68:71]\n\t" \
  "v_mfma_f32_16x16x32_bf16 v[12:15], v[232:235], v[148:151], v[12:15]\n\t" \
  "v_mfma_f32_16x16x32_bf16 v[32:35], v[232:235], a[192:195], v[32:35]\n\t" \
  "v_mfma_f32_16x16x32_bf16 v[52:55], v[232:235], a[196:199], v[52:55]\n\t" \
  "v_mfma_f32_16x16x32_bf16 v[72:75], v[232:235], a[200:203], v[72:75]\n\t" \
  "v_mfma_f32_16x16x32_bf16 v[16:19], v[236:239], v[148:151], v[16:19]\n\t" \
  "v_mfma_f32_16x16x32_bf16 v[36:39], v[236:239], a[192:195], v[36:39]\n\t" \
  "ds_write_b128 v208, v[28:31] offset:0\n\t" \
  "v_mfma_f32_16x16x32_bf16 v[56:59], v[236:239], a[196:199], v[56:59]\n\t" \
  "ds_write_b128 v209, v[48:51] offset:0\n\t" \
  "v_mfma_f32_16x16x32_bf16 v[76:79], v[236:239], a[200:203], v[76:79]\n\t" \
  "ds_write_b128 v210, v[68:71] offset:0\n\t" \
  "v_mfma_f32_16x16x32_bf16 v[20:23], v[240:243], v[148:151], v[20:23]\n\t" \
  "v_mfma_f32_16x16x32_bf16 v[40:43], v[240:243], a[192:195], v[40:43]\n\t" \
  "ds_write_b128 v208, v[32:35] offset:1024\n\t" \
  "v_mfma_f32_16x16x32_bf16 v[60:63], v[240:243], a[196:199], v[60:63]\n\t" \
  "ds_write_b128 v209, v[52:55] offset:1024\n\t" \
  "v_mfma_f32_16x16x32_bf16 v[80:83], v[240:243], a[200:203], v[80:83]\n\t" \
  "ds_write_b128 v210, v[72:75] offset:1024\n\t" \
  "v_mfma_f32_16x16x32_bf16 v[24:27], v[244:247], v[148:151], v[24:27]\n\t" \
  "v_mfma_f32_16x16x32_bf16 v[44:47], v[244:247], a[192:195], v[44:47]\n\t" \
  "ds_write_b128 v208, v[36:39] offset:2048\n\t" \
  "v_mfma_f32_16x16x32_bf16 v[64:67], v[244:247], a[196:199], v[64:67]\n\t" \
  "ds_write_b128 v209, v[56:59] offset:2048\n\t" \
  "v_mfma_f32_16x16x32_bf16 v[84:87], v[244:247], a[200:203], v[84:87]\n\t" \
  "ds_write_b128 v210, v[76:79] offset:2048\n\t" \
  "ds_write_b128 v208, v[40:43] offset:3072\n\t" \
  "ds_write_b128 v209, v[60:63] offset:3072\n\t" \
  "ds_write_b128 v210, v[80:83] offset:3072\n\t" \
  "ds_write_b128 v208, v[44:47] offset:4096\n\t" \
  "s_nop 0\n\t" \
  "ds_write_b128 v209, v[64:67] offset:4096\n\t" \
  "s_nop 0\n\t" \
  "ds_write_b128 v210, v[84:87] offset:4096\n\t" \
  "s_waitcnt lgkmcnt(0)\n\t" \
  "s_barrier\n\t" \
  "ds_read_b128 v[28:31], v211 offset:0\n\t" \
  "ds_read_b128 v[32:35], v211 offset:1024\n\t" \
  "ds_read_b128 v[36:39], v211 offset:2048\n\t" \
  "ds_read_b128 v[40:43], v211 offset:3072\n\t" \
  "ds_read_b128 v[44:47], v211 offset:4096\n\t" \
  "ds_read_b128 v[48:51], v211 offset:5120\n\t" \
  "ds_read_b128 v[52:55], v211 offset:6144\n\t" \
  "ds_read_b128 v[56:59], v211 offset:7168\n\t" \
  "ds_read_b128 v[60:63], v211 offset:8192\n\t" \
  "ds_read_b128 v[64:67], v211 offset:9216\n\t" \
  "ds_read_b128 v[68:71], v211 offset:10240\n\t" \
  "ds_read_b128 v[72:75], v211 offset:11264\n\t" \
  "ds_read_b128 v[76:79], v211 offset:12288\n\t" \
  "ds_read_b128 v[80:83], v211 offset:13312\n\t" \
  "ds_read_b128 v[84:87], v211 offset:14336\n\t" \
  "s_waitcnt lgkmcnt(14)\n\t" \
  "v_pk_add_f32 v[8:9], v[8:9], v[28:29]\n\t" \
  "v_pk_add_f32 v[10:11], v[10:11], v[30:31]\n\t" \
  "s_waitcnt lgkmcnt(13)\n\t" \
  "v_pk_add_f32 v[12:13], v[12:13], v[32:33]\n\t" \
  "v_pk_add_f32 v[14:15], v[14:15], v[34:35]\n\t" \
  "s_waitcnt lgkmcnt(12)\n\t" \
  "v_pk_add_f32 v[16:17], v[16:17], v[36:37]\n\t" \
  "v_pk_add_f32 v[18:19], v[18:19], v[38:39]\n\t" \
  "s_waitcnt lgkmcnt(11)\n\t" \
  "v_pk_add_f32 v[20:21], v[20:21], v[40:41]\n\t" \
  "v_pk_add_f32 v[22:23], v[22:23], v[42:43]\n\t" \
  "s_waitcnt lgkmcnt(10)\n\t" \
  "v_pk_add_f32 v[24:25], v[24:25], v[44:45]\n\t" \
  "v_pk_add_f32 v[26:27], v[26:27], v[46:47]\n\t" \
  "s_waitcnt lgkmcnt(4)\n\t" \
  "v_pk_add_f32 v[48:49], v[48:49], v[68:69]\n\t" \
  "v_pk_add_f32 v[50:51], v[50:51], v[70:71]\n\t" \
  "s_waitcnt lgkmcnt(3)\n\t" \
  "v_pk_add_f32 v[52:53], v[52:53], v[72:73]\n\t" \
  "v_pk_add_f32 v[54:55], v[54:55], v[74:75]\n\t" \
  "s_waitcnt lgkmcnt(2)\n\t" \
  "v_pk_add_f32 v[56:57], v[56:57], v[76:77]\n\t" \
  "v_pk_add_f32 v[58:59], v[58:59], v[78:79]\n\t" \
  "s_waitcnt lgkmcnt(1)\n\t" \
  "v_pk_add_f32 v[60:61], v[60:61], v[80:81]\n\t" \
  "v_pk_add_f32 v[62:63], v[62:63], v[82:83]\n\t" \
  "s_waitcnt lgkmcnt(0)\n\t" \
  "v_pk_add_f32 v[64:65], v[64:65], v[84:85]\n\t" \
  "v_pk_add_f32 v[66:67], v[66:67], v[86:87]\n\t" \
  "v_pk_add_f32 v[8:9], v[8:9], v[48:49]\n\t" \
  "v_pk_add_f32 v[10:11], v[10:11], v[50:51]\n\t" \
  "v_pk_add_f32 v[12:13], v[12:13], v[52:53]\n\t" \
  "v_pk_add_f32 v[14:15], v[14:15], v[54:55]\n\t" \
  "v_pk_add_f32 v[16:17], v[16:17], v[56:57]\n\t" \
  "v_pk_add_f32 v[18:19], v[18:19], v[58:59]\n\t" \
  "v_pk_add_f32 v[20:21], v[20:21], v[60:61]\n\t" \
  "v_pk_add_f32 v[22:23], v[22:23], v[62:63]\n\t" \
  "v_pk_add_f32 v[24:25], v[24:25], v[64:65]\n\t" \
  "v_pk_add_f32 v[26:27], v[26:27], v[66:67]\n\t" \
  "v_pk_fma_f32 v[8:9], v[8:9], v[100:101], v[120:121]\n\t" \
  "v_pk_fma_f32 v[10:11], v[10:11], v[102:103], v[122:123]\n\t" \
  "v_pk_fma_f32 v[12:13], v[12:13], v[104:105], v[124:125]\n\t" \
  "v_pk_fma_f32 v[14:15], v[14:15], v[106:107], v[126:127]\n\t" \
  "v_pk_fma_f32 v[16:17], v[16:17], v[108:109], v[128:129]\n\t" \
  "v_pk_fma_f32 v[18:19], v[18:19], v[110:111], v[130:131]\n\t" \
  "v_pk_fma_f32 v[20:21], v[20:21], v[112:113], v[132:133]\n\t" \
  "v_pk_fma_f32 v[22:23], v[22:23], v[114:115], v[134:135]\n\t" \
  "v_pk_fma_f32 v[24:25], v[24:25], v[116:117], v[136:137]\n\t" \
  "v_pk_fma_f32 v[26:27], v[26:27], v[118:119], v[138:139]\n\t" \
  "v_cvt_pk_bf16_f32 v88, v8, v9\n\t" \
  "v_cvt_pk_bf16_f32 v89, v10, v11\n\t" \
  "v_cvt_pk_bf16_f32 v90, v12, v13\n\t" \
  "v_cvt_pk_bf16_f32 v91, v14, v15\n\t" \
  "v_cvt_pk_bf16_f32 v92, v16, v17\n\t" \
  "v_cvt_pk_bf16_f32 v93, v18, v19\n\t" \
  "v_cvt_pk_bf16_f32 v94, v20, v21\n\t" \
  "v_cvt_pk_bf16_f32 v95, v22, v23\n\t" \
  "v_cvt_pk_bf16_f32 v96, v24, v25\n\t" \
  "v_cvt_pk_bf16_f32 v97, v26, v27\n\t" \
  "ds_write_b64 v221, v[88:89] offset:0\n\t" \
  "ds_write_b64 v221, v[90:91] offset:32\n\t" \
  "ds_write_b64 v221, v[92:93] offset:64\n\t" \
  "ds_write_b64 v221, v[94:95] offset:96\n\t" \
  "ds_write_b64 v221, v[96:97] offset:128\n\t" \
  "ds_read_b128 v[28:31], v215 offset:0\n\t" \
  "ds_read_b128 v[32:35], v215 offset:1024\n\t" \
  "ds_read_b128 v[36:39], v215 offset:2048\n\t" \
  "s_waitcnt lgkmcnt(2)\n\t" \
  "buffer_store_dwordx4 v[28:31], v212, s[48:51], 0 offen\n\t" \
  "s_waitcnt lgkmcnt(1)\n\t" \
  "buffer_store_dwordx4 v[32:35], v213, s[48:51], 0 offen\n\t" \
  "s_waitcnt lgkmcnt(0)\n\t" \
  "buffer_store_dwordx4 v[36:39], v214, s[48:51], 0 offen\n\t" \
  "s_mov_b32 %[bad], 0\n\t" \
  "s_branch .Lend%=\n\t" \
  ".Lprspin%=:\n\t" \
  "s_mov_b32 s84, 0\n\t" \
  ".Lpr%=:\n\t" \
  "global_load_dwordx4 v[244:247], v202, s[68:69] sc1\n\t" \
  "s_waitcnt vmcnt(0)\n\t" \
  "v_xor_b32 v244, %[tag], v244\n\t" \
  "v_xor_b32 v245, %[tag], v245\n\t" \
  "v_xor_b32 v246, %[tag], v246\n\t" \
  "v_xor_b32 v247, %[tag], v247\n\t" \
  "v_or3_b32 v244, v244, v245, v246\n\t" \
  "v_or_b32 v244, v244, v247\n\t" \
  "v_cmp_ne_u32 vcc, 0, v244\n\t" \
  "s_cbranch_vccz .Lprd%=\n\t" \
  "s_sleep 1\n\t" \
  "s_add_u32 s84, s84, 1\n\t" \
  "s_cmp_lt_u32 s84, 30000\n\t" \
  "s_cbranch_scc1 .Lpr%=\n\t" \
  "v_mov_b32 v244, 0\n\t" \
  "v_mov_b32 v245, 1\n\t" \
  "global_store_dword v244, v245, s[70:71] sc1\n\t" \
  "s_waitcnt vmcnt(0)\n\t" \
  ".Lprd%=:\n\t" \
  "s_branch .Lprback%=\n\t" \
  ".Lppspin%=:\n\t" \
  "s_mov_b32 s84, 0\n\t" \
  ".Lpp%=:\n\t" \
  "global_load_dword v213, v212, s[68:69] sc1\n\t" \
  "s_waitcnt vmcnt(0)\n\t" \
  "v_cmp_ne_u32 vcc, %[tag], v213\n\t" \
  "s_cbranch_vccz .Lppd%=\n\t" \
  "s_sleep 1\n\t" \
  "s_add_u32 s84, s84, 1\n\t" \
  "s_cmp_lt_u32 s84, 30000\n\t" \
  "s_cbranch_scc1 .Lpp%=\n\t" \
  "v_mov_b32 v213, 0\n\t" \
  "v_mov_b32 v214, 1\n\t" \
  "global_store_dword v213, v214, s[70:71] sc1\n\t" \
  "s_waitcnt vmcnt(0)\n\t" \
  ".Lppd%=:\n\t" \
  "s_branch .Lppback%=\n\t" \
  ".Lp2spin%=:\n\t" \
  "s_mov_b32 s84, 0\n\t" \
  ".Lp2%=:\n\t" \
  "global_load_dword v213, v212, s[42:43] sc1\n\t" \
  "s_waitcnt vmcnt(0)\n\t" \
  "v_cmp_ne_u32 vcc, %[tag], v213\n\t" \
  "s_cbranch_vccz .Lp2d%=\n\t" \
  "s_sleep 1\n\t" \
  "s_add_u32 s84, s84, 1\n\t" \
  "s_cmp_lt_u32 s84, 30000\n\t" \
  "s_cbranch_scc1 .Lp2%=\n\t" \
  "v_mov_b32 v213, 0\n\t" \
  "v_mov_b32 v214, 1\n\t" \
  "global_store_dword v213, v214, s[70:71] sc1\n\t" \
  "s_waitcnt vmcnt(0)\n\t" \
  ".Lp2d%=:\n\t" \
  "s_branch .Lp2back%=\n\t" \
  ".Lpdspin%=:\n\t" \
  "s_mov_b32 s84, 0\n\t" \
  ".Lpd%=:\n\t" \
  "global_load_dword v245, v244, s[68:69] sc1\n\t" \
  "s_waitcnt vmcnt(0)\n\t" \
  "v_cmp_ne_u32 vcc, %[tag], v245\n\t" \
  "s_cbranch_vccz .Lpdd%=\n\t" \
  "s_sleep 1\n\t" \
  "s_add_u32 s84, s84, 1\n\t" \
  "s_cmp_lt_u32 s84, 30000\n\t" \
  "s_cbranch_scc1 .Lpd%=\n\t" \
  "v_mov_b32 v245, 0\n\t" \
  "v_mov_b32 v246, 1\n\t" \
  "global_store_dword v245, v246, s[70:71] sc1\n\t" \
  "s_waitcnt vmcnt(0)\n\t" \
  ".Lpdd%=:\n\t" \
  "s_branch .Lpdback%=\n\t" \
  ".Lpuspin%=:\n\t" \
  "s_mov_b32 s84, 0\n\t" \
  ".Lpu%=:\n\t" \
  "global_load_dword v213, v212, s[68:69] sc1\n\t" \
  "s_waitcnt vmcnt(0)\n\t" \
  "v_cmp_ne_u32 vcc, %[tag], v213\n\t" \
  "s_cbranch_vccz .Lpud%=\n\t" \
  "s_sleep 1\n\t" \
  "s_add_u32 s84, s84, 1\n\t" \
  "s_cmp_lt_u32 s84, 30000\n\t" \
  "s_cbranch_scc1 .Lpu%=\n\t" \
  "v_mov_b32 v213, 0\n\t" \
  "v_mov_b32 v214, 1\n\t" \
  "global_store_dword v213, v214, s[70:71] sc1\n\t" \
  "s_waitcnt vmcnt(0)\n\t" \
  ".Lpud%=:\n\t" \
  "s_branch .Lpuback%=\n\t" \
  ".Lidle%=:\n\t" \
  "s_mov_b64 s[52:53], %[wd]\n\t" \
  "s_and_b32 s53, s53, 0xffff\n\t" \
  "s_mov_b32 s54, 0x21c000\n\t" \
  "s_mov_b32 s55, 0x20000\n\t" \
  "s_mov_b32 s48, 29409280\n\t" \
  "s_mov_b64 s[72:73], %[ws]\n\t" \
  "s_add_u32 s48, s48, s72\n\t" \
  "s_addc_u32 s49, s73, 0\n\t" \
  "s_and_b32 s49, s49, 0xffff\n\t" \
  "s_mov_b32 s50, 0x71c000\n\t" \
  "s_mov_b32 s51, 0x20000\n\t" \
  "v_bfe_u32 v232, v203, 2, 3\n\t" \
  "s_mul_i32 s84, %[w], 8\n\t" \
  "v_add_u32 v232, s84, v232\n\t" \
  "v_mul_u32_u24 v213, 1280, v232\n\t" \
  "v_lshrrev_b32 v232, 3, v203\n\t" \
  "v_and_b32 v232, 2, v232\n\t" \
  "v_and_b32 v233, 3, v203\n\t" \
  "v_xor_b32 v232, v232, v233\n\t" \
  "v_lshl_add_u32 v213, v232, 4, v213\n\t" \
  "v_lshrrev_b32 v233, 5, v203\n\t" \
  "v_lshl_add_u32 v213, v233, 6, v213\n\t" \
  "s_lshl_b32 s90, %[w], 10\n\t" \
  "s_add_u32 s90, s90, 0x10000\n\t" \
  "v_and_b32 v232, 3, v203\n\t" \
  "v_lshlrev_b32 v214, 3, v232\n\t" \
  "v_bfe_u32 v232, v203, 2, 3\n\t" \
  "v_lshl_or_b32 v214, v232, 6, v214\n\t" \
  "v_bfe_u32 v232, v203, 4, 1\n\t" \
  "v_lshl_or_b32 v214, v232, 5, v214\n\t" \
  "v_lshrrev_b32 v232, 5, v203\n\t" \
  "v_lshl_or_b32 v214, v232, 10, v214\n\t" \
  "s_and_b32 s84, %[w], 1\n\t" \
  "s_lshl_b32 s84, s84, 5\n\t" \
  "v_xor_b32 v214, s84, v214\n\t" \
  "s_lshr_b32 s84, %[w], 1\n\t" \
  "s_lshl_b32 s84, s84, 9\n\t" \
  "s_add_u32 s84, s84, 0x10000\n\t" \
  "v_add_u32 v214, s84, v214\n\t" \
  "v_and_b32 v232, 3, v203\n\t" \
  "v_lshlrev_b32 v224, 3, v232\n\t" \
  "v_bfe_u32 v232, v203, 2, 3\n\t" \
  "v_lshl_or_b32 v224, v232, 6, v224\n\t" \
  "v_bfe_u32 v232, v203, 4, 1\n\t" \
  "v_lshl_or_b32 v224, v232, 5, v224\n\t" \
  "v_lshrrev_b32 v232, 5, v203\n\t" \
  "v_lshl_or_b32 v224, v232, 11, v224\n\t" \
  "s_lshl_b32 s84, %[w], 9\n\t" \
  "v_add_u32 v224, s84, v224\n\t" \
  "v_xor_b32 v225, 32, v224\n\t" \
  "v_add_u32 v226, 131072, v224\n\t" \
  "v_add_u32 v227, 131072, v225\n\t" \
  "v_lshrrev_b32 v232, 4, v203\n\t" \
  "v_and_b32 v233, 1, v203\n\t" \
  "v_lshl_or_b32 v232, v232, 1, v233\n\t" \
  "v_lshlrev_b32 v228, 4, v232\n\t" \
  "v_add_u32 v228, 147456, v228\n\t" \
  "s_sub_u32 s86, %[ft], 27\n\t" \
  "s_mul_i32 s85, s86, 26\n\t" \
  "s_lshr_b32 s85, s85, 8\n\t" \
  "s_mul_i32 s87, s85, 10\n\t" \
  "s_sub_u32 s87, s86, s87\n\t" \
  "s_mul_i32 s92, s85, 40960\n\t" \
  "s_lshl_b32 s84, s87, 7\n\t" \
  "s_add_u32 s92, s92, s84\n\t" \
  "s_add_u32 s88, s92, 0\n\t" \
  "s_add_u32 m0, s90, 0\n\t" \
  "s_nop 0\n\t" \
  "buffer_load_dwordx4 v213, s[52:55], s88 offen nt lds\n\t" \
  "s_add_u32 s88, s92, 163840\n\t" \
  "s_add_u32 m0, s90, 4096\n\t" \
  "s_nop 0\n\t" \
  "buffer_load_dwordx4 v213, s[52:55], s88 offen nt lds\n\t" \
  "s_add_u32 s88, s92, 327680\n\t" \
  "s_add_u32 m0, s90, 8192\n\t" \
  "s_nop 0\n\t" \
  "buffer_load_dwordx4 v213, s[52:55], s88 offen nt lds\n\t" \
  "s_add_u32 s88, s92, 491520\n\t" \
  "s_add_u32 m0, s90, 12288\n\t" \
  "s_nop 0\n\t" \
  "buffer_load_dwordx4 v213, s[52:55], s88 offen nt lds\n\t" \
  "s_add_u32 s88, s92, 655360\n\t" \
  "s_add_u32 m0, s90, 16384\n\t" \
  "s_nop 0\n\t" \
  "buffer_load_dwordx4 v213, s[52:55], s88 offen nt lds\n\t" \
  "s_add_u32 s88, s92, 819200\n\t" \
  "s_add_u32 m0, s90, 20480\n\t" \
  "s_nop 0\n\t" \
  "buffer_load_dwordx4 v213, s[52:55], s88 offen nt lds\n\t" \
  "s_add_u32 s88, s92, 983040\n\t" \
  "s_add_u32 m0, s90, 24576\n\t" \
  "s_nop 0\n\t" \
  "buffer_load_dwordx4 v213, s[52:55], s88 offen nt lds\n\t" \
  "s_add_u32 s88, s92, 1146880\n\t" \
  "s_add_u32 m0, s90, 28672\n\t" \
  "s_nop 0\n\t" \
  "buffer_load_dwordx4 v213, s[52:55], s88 offen nt lds\n\t" \
  "s_add_u32 s88, s92, 1310720\n\t" \
  "s_add_u32 m0, s90, 32768\n\t" \
  "s_nop 0\n\t" \
  "buffer_load_dwordx4 v213, s[52:55], s88 offen nt lds\n\t" \
  "s_add_u32 s88, s92, 1474560\n\t" \
  "s_add_u32 m0, s90, 36864\n\t" \
  "s_nop 0\n\t" \
  "buffer_load_dwordx4 v213, s[52:55], s88 offen nt lds\n\t" \
  "s_add_u32 s88, s92, 1638400\n\t" \
  "s_add_u32 m0, s90, 40960\n\t" \
  "s_nop 0\n\t" \
  "buffer_load_dwordx4 v213, s[52:55], s88 offen nt lds\n\t" \
  "s_add_u32 s88, s92, 1802240\n\t" \
  "s_add_u32 m0, s90, 45056\n\t" \
  "s_nop 0\n\t" \
  "buffer_load_dwordx4 v213, s[52:55], s88 offen nt lds\n\t" \
  "s_add_u32 s88, s92, 1966080\n\t" \
  "s_add_u32 m0, s90, 49152\n\t" \
  "s_nop 0\n\t" \
  "buffer_load_dwordx4 v213, s[52:55], s88 offen nt lds\n\t" \
  "s_add_u32 s88, s92, 2129920\n\t" \
  "s_add_u32 m0, s90, 53248\n\t" \
  "s_nop 0\n\t" \
  "buffer_load_dwordx4 v213, s[52:55], s88 offen nt lds\n\t" \
  "s_lshl_b32 s87, s87, 2\n\t" \
  "s_add_u32 s87, s87, %[w]\n\t" \
  "s_mul_i32 s84, s87, 52\n\t" \
  "s_lshr_b32 s84, s84, 8\n\t" \
  "s_mul_i32 s84, s84, 271360\n\t" \
  "s_lshl_b32 s87, s87, 10\n\t" \
  "s_add_u32 s87, s87, s84\n\t" \
  "s_mul_i32 s84, s85, 5120\n\t" \
  "s_add_u32 s87, s87, s84\n\t" \
  "s_add_u32 m0, s80, 131072\n\t" \
  "s_mov_b32 s74, 884736\n\t" \
  "buffer_load_dwordx4 v216, s[56:59], s74 offen offset:0 lds\n\t" \
  "buffer_load_dwordx4 v217, s[60:63], s74 offen offset:1024 lds\n\t" \
  "buffer_load_dwordx4 v218, s[56:59], s74 offen offset:2048 lds\n\t" \
  "buffer_load_dwordx4 v219, s[60:63], s74 offen offset:3072 lds\n\t" \
  "s_mov_b32 s64, 114688\n\t" \
  "s_mov_b64 s[72:73], %[ws]\n\t" \
  "s_add_u32 s64, s64, s72\n\t" \
  "s_addc_u32 s65, s73, 0\n\t" \
  "s_and_b32 s65, s65, 0xffff\n\t" \
  "s_mov_b32 s66, 0xa000\n\t" \
  "s_mov_b32 s67, 0x20000\n\t" \
  "s_mov_b32 s74, 10240\n\t" \
  "s_mov_b32 s75, 20480\n\t" \
  "s_mov_b32 s89, 30720\n\t" \
  "s_lshl_b32 s84, %[w], 6\n\t" \
  "v_add_u32 v252, s84, v203\n\t" \
  "v_min_u32 v252, 159, v252\n\t" \
  "v_lshlrev_b32 v253, 4, v252\n\t" \
  "v_add_u32 v253, 2560, v253\n\t" \
  "buffer_load_dwordx4 v[8:11], v253, s[64:67], 0 offen sc1\n\t" \
  "buffer_load_dwordx4 v[24:27], v253, s[64:67], 0 offen offset:2560 sc1\n\t" \
  "buffer_load_dwordx4 v[12:15], v253, s[64:67], s74 offen sc1\n\t" \
  "buffer_load_dwordx4 v[28:31], v253, s[64:67], s74 offen offset:2560 sc1\n\t" \
  "buffer_load_dwordx4 v[16:19], v253, s[64:67], s75 offen sc1\n\t" \
  "buffer_load_dwordx4 v[32:35], v253, s[64:67], s75 offen offset:2560 sc1\n\t" \
  "buffer_load_dwordx4 v[20:23], v253, s[64:67], s89 offen sc1\n\t" \
  "buffer_load_dwordx4 v[36:39], v253, s[64:67], s89 offen offset:2560 sc1\n\t" \
  "v_lshrrev_b32 v253, 3, v252\n\t" \
  "v_lshlrev_b32 v249, 7, v253\n\t" \
  "v_and_b32 v253, 3, v252\n\t" \
  "v_lshl_or_b32 v249, v253, 5, v249\n\t" \
  "v_bfe_u32 v253, v252, 2, 1\n\t" \
  "v_lshl_or_b32 v249, v253, 3, v249\n\t" \
  "v_add_u32 v249, 147456, v249\n\t" \
  "s_waitcnt vmcnt(5)\n\t" \
  "v_pk_add_f32 v[8:9], v[8:9], v[12:13]\n\t" \
  "s_waitcnt vmcnt(1)\n\t" \
  "v_pk_add_f32 v[16:17], v[16:17], v[20:21]\n\t" \
  "v_pk_add_f32 v[10:11], v[10:11], v[14:15]\n\t" \
  "v_pk_add_f32 v[18:19], v[18:19], v[22:23]\n\t" \
  "v_pk_add_f32 v[8:9], v[8:9], v[16:17]\n\t" \
  "v_pk_add_f32 v[10:11], v[10:11], v[18:19]\n\t" \
  "v_cvt_pk_bf16_f32 v12, v8, v9\n\t" \
  "v_cvt_pk_bf16_f32 v13, v10, v11\n\t" \
  "v_pk_add_f32 v[24:25], v[24:25], v[28:29]\n\t" \
  "s_waitcnt vmcnt(0)\n\t" \
  "v_pk_add_f32 v[32:33], v[32:33], v[36:37]\n\t" \
  "v_pk_add_f32 v[26:27], v[26:27], v[30:31]\n\t" \
  "v_pk_add_f32 v[34:35], v[34:35], v[38:39]\n\t" \
  "v_pk_add_f32 v[24:25], v[24:25], v[32:33]\n\t" \
  "v_pk_add_f32 v[26:27], v[26:27], v[34:35]\n\t" \
  "v_cvt_pk_bf16_f32 v28, v24, v25\n\t" \
  "v_cvt_pk_bf16_f32 v29, v26, v27\n\t" \
  "v_lshlrev_b32 v24, 16, v28\n\t" \
  "v_and_b32 v25, 0xffff0000, v28\n\t" \
  "v_lshlrev_b32 v26, 16, v29\n\t" \
  "v_and_b32 v27, 0xffff0000, v29\n\t" \
  "v_add_f32 v24, 1.0, v24\n\t" \
  "v_add_f32 v25, 1.0, v25\n\t" \
  "v_add_f32 v26, 1.0, v26\n\t" \
  "v_add_f32 v27, 1.0, v27\n\t" \
  "v_cvt_pk_bf16_f32 v28, v24, v25\n\t" \
  "v_cvt_pk_bf16_f32 v29, v26, v27\n\t" \
  "ds_write_b64 v249, v[12:13] offset:16\n\t" \
  "ds_write_b64 v249, v[28:29]\n\t" \
  "s_waitcnt vmcnt(0) lgkmcnt(0)\n\t" \
  "s_barrier\n\t" \
  "ds_read_b64_tr_b16 v[8:9], v214 offset:0\n\t" \
  "ds_read_b64_tr_b16 v[10:11], v214 offset:2048\n\t" \
  "ds_read_b64_tr_b16 v[12:13], v214 offset:4096\n\t" \
  "ds_read_b64_tr_b16 v[14:15], v214 offset:6144\n\t" \
  "ds_read_b64_tr_b16 v[16:17], v214 offset:8192\n\t" \
  "ds_read_b64_tr_b16 v[18:19], v214 offset:10240\n\t" \
  "ds_read_b64_tr_b16 v[20:21], v214 offset:12288\n\t" \
  "ds_read_b64_tr_b16 v[22:23], v214 offset:14336\n\t" \
  "ds_read_b64_tr_b16 v[24:25], v214 offset:16384\n\t" \
  "ds_read_b64_tr_b16 v[26:27], v214 offset:18432\n\t" \
  "ds_read_b64_tr_b16 v[28:29], v214 offset:20480\n\t" \
  "ds_read_b64_tr_b16 v[30:31], v214 offset:22528\n\t" \
  "ds_read_b64_tr_b16 v[32:33], v214 offset:24576\n\t" \
  "ds_read_b64_tr_b16 v[34:35], v214 offset:26624\n\t" \
  "ds_read_b64_tr_b16 v[36:37], v214 offset:28672\n\t" \
  "ds_read_b64_tr_b16 v[38:39], v214 offset:30720\n\t" \
  "ds_read_b64_tr_b16 v[40:41], v214 offset:32768\n\t" \
  "ds_read_b64_tr_b16 v[42:43], v214 offset:34816\n\t" \
  "ds_read_b64_tr_b16 v[44:45], v214 offset:36864\n\t" \
  "ds_read_b64_tr_b16 v[46:47], v214 offset:38912\n\t" \
  "ds_read_b64_tr_b16 v[48:49], v214 offset:40960\n\t" \
  "ds_read_b64_tr_b16 v[50:51], v214 offset:43008\n\t" \
  "ds_read_b64_tr_b16 v[52:53], v214 offset:45056\n\t" \
  "ds_read_b64_tr_b16 v[54:55], v214 offset:47104\n\t" \
  "ds_read_b64_tr_b16 v[56:57], v214 offset:49152\n\t" \
  "ds_read_b64_tr_b16 v[58:59], v214 offset:51200\n\t" \
  "ds_read_b64_tr_b16 v[60:61], v214 offset:53248\n\t" \
  "ds_read_b64_tr_b16 v[62:63], v214 offset:55296\n\t" \
  "s_add_u32 s89, s87, 5242880\n\t" \
  "s_waitcnt lgkmcnt(11)\n\t" \
  "buffer_store_dwordx4 v[8:11], v202, s[48:51], s89 offen sc1\n\t" \
  "s_add_u32 s89, s87, 5263360\n\t" \
  "buffer_store_dwordx4 v[12:15], v202, s[48:51], s89 offen sc1\n\t" \
  "s_add_u32 s89, s87, 5283840\n\t" \
  "buffer_store_dwordx4 v[16:19], v202, s[48:51], s89 offen sc1\n\t" \
  "s_add_u32 s89, s87, 5304320\n\t" \
  "buffer_store_dwordx4 v[20:23], v202, s[48:51], s89 offen sc1\n\t" \
  "s_add_u32 s89, s87, 5324800\n\t" \
  "buffer_store_dwordx4 v[24:27], v202, s[48:51], s89 offen sc1\n\t" \
  "s_add_u32 s89, s87, 5345280\n\t" \
  "buffer_store_dwordx4 v[28:31], v202, s[48:51], s89 offen sc1\n\t" \
  "s_add_u32 s89, s87, 5365760\n\t" \
  "buffer_store_dwordx4 v[32:35], v202, s[48:51], s89 offen sc1\n\t" \
  "s_add_u32 s89, s87, 5386240\n\t" \
  "buffer_store_dwordx4 v[36:39], v202, s[48:51], s89 offen sc1\n\t" \
  "s_add_u32 s89, s87, 5406720\n\t" \
  "s_waitcnt lgkmcnt(0)\n\t" \
  "buffer_store_dwordx4 v[40:43], v202, s[48:51], s89 offen sc1\n\t" \
  "s_add_u32 s89, s87, 5427200\n\t" \
  "buffer_store_dwordx4 v[44:47], v202, s[48:51], s89 offen sc1\n\t" \
  "s_add_u32 s89, s87, 5447680\n\t" \
  "buffer_store_dwordx4 v[48:51], v202, s[48:51], s89 offen sc1\n\t" \
  "s_add_u32 s89, s87, 5468160\n\t" \
  "buffer_store_dwordx4 v[52:55], v202, s[48:51], s89 offen sc1\n\t" \
  "s_add_u32 s89, s87, 5488640\n\t" \
  "buffer_store_dwordx4 v[56:59], v202, s[48:51], s89 offen sc1\n\t" \
  "s_add_u32 s89, s87, 5509120\n\t" \
  "s_cmp_lt_u32 s85, 2\n\t" \
  "s_cselect_b32 s89, s89, 0x7ff00000\n\t" \
  "buffer_store_dwordx4 v[60:63], v202, s[48:51], s89 offen sc1\n\t" \
  "ds_read_b64_tr_b16 a[0:1], v224 offset:0\n\t" \
  "ds_read_b64_tr_b16 a[2:3], v224 offset:4096\n\t" \
  "ds_read_b64_tr_b16 a[4:5], v225 offset:0\n\t" \
  "ds_read_b64_tr_b16 a[6:7], v225 offset:4096\n\t" \
  "ds_read_b128 a[8:11], v228 offset:0\n\t" \
  "ds_read_b64_tr_b16 a[12:13], v224 offset:8192\n\t" \
  "ds_read_b64_tr_b16 a[14:15], v224 offset:12288\n\t" \
  "ds_read_b64_tr_b16 a[16:17], v225 offset:8192\n\t" \
  "ds_read_b64_tr_b16 a[18:19], v225 offset:12288\n\t" \
  "ds_read_b128 a[20:23], v228 offset:128\n\t" \
  "ds_read_b64_tr_b16 a[24:25], v224 offset:16384\n\t" \
  "ds_read_b64_tr_b16 a[26:27], v224 offset:20480\n\t" \
  "ds_read_b64_tr_b16 a[28:29], v225 offset:16384\n\t" \
  "ds_read_b64_tr_b16 a[30:31], v225 offset:20480\n\t" \
  "ds_read_b128 a[32:35], v228 offset:256\n\t" \
  "ds_read_b64_tr_b16 a[36:37], v224 offset:24576\n\t" \
  "ds_read_b64_tr_b16 a[38:39], v224 offset:28672\n\t" \
  "ds_read_b64_tr_b16 a[40:41], v225 offset:24576\n\t" \
  "ds_read_b64_tr_b16 a[42:43], v225 offset:28672\n\t" \
  "ds_read_b128 a[44:47], v228 offset:384\n\t" \
  "ds_read_b64_tr_b16 a[48:49], v224 offset:32768\n\t" \
  "ds_read_b64_tr_b16 a[50:51], v224 offset:36864\n\t" \
  "ds_read_b64_tr_b16 a[52:53], v225 offset:32768\n\t" \
  "ds_read_b64_tr_b16 a[54:55], v225 offset:36864\n\t" \
  "ds_read_b128 a[56:59], v228 offset:512\n\t" \
  "ds_read_b64_tr_b16 a[60:61], v224 offset:40960\n\t" \
  "ds_read_b64_tr_b16 a[62:63], v224 offset:45056\n\t" \
  "ds_read_b64_tr_b16 a[64:65], v225 offset:40960\n\t" \
  "ds_read_b64_tr_b16 a[66:67], v225 offset:45056\n\t" \
  "ds_read_b128 a[68:71], v228 offset:640\n\t" \
  "ds_read_b64_tr_b16 a[72:73], v224 offset:49152\n\t" \
  "ds_read_b64_tr_b16 a[74:75], v224 offset:53248\n\t" \
  "ds_read_b64_tr_b16 a[76:77], v225 offset:49152\n\t" \
  "ds_read_b64_tr_b16 a[78:79], v225 offset:53248\n\t" \
  "ds_read_b128 a[80:83], v228 offset:768\n\t" \
  "ds_read_b64_tr_b16 a[84:85], v224 offset:57344\n\t" \
  "ds_read_b64_tr_b16 a[86:87], v224 offset:61440\n\t" \
  "ds_read_b64_tr_b16 a[88:89], v225 offset:57344\n\t" \
  "ds_read_b64_tr_b16 a[90:91], v225 offset:61440\n\t" \
  "ds_read_b128 a[92:95], v228 offset:896\n\t" \
  "s_waitcnt lgkmcnt(12)\n\t" \
  "v_mfma_f32_16x16x32_bf16 v[72:75], a[0:3], a[8:11], 0\n\t" \
  "v_mfma_f32_16x16x32_bf16 v[76:79], a[4:7], a[8:11], 0\n\t" \
  "v_mfma_f32_16x16x32_bf16 v[72:75], a[12:15], a[20:23], v[72:75]\n\t" \
  "v_mfma_f32_16x16x32_bf16 v[76:79], a[16:19], a[20:23], v[76:79]\n\t" \
  "v_mfma_f32_16x16x32_bf16 v[72:75], a[24:27], a[32:35], v[72:75]\n\t" \
  "v_mfma_f32_16x16x32_bf16 v[76:79], a[28:31], a[32:35], v[76:79]\n\t" \
  "v_mfma_f32_16x16x32_bf16 v[72:75], a[36:39], a[44:47], v[72:75]\n\t" \
  "v_mfma_f32_16x16x32_bf16 v[76:79], a[40:43], a[44:47], v[76:79]\n\t" \
  "v_mfma_f32_16x16x32_bf16 v[72:75], a[48:51], a[56:59], v[72:75]\n\t" \
  "v_mfma_f32_16x16x32_bf16 v[76:79], a[52:55], a[56:59], v[76:79]\n\t" \
  "s_waitcnt lgkmcnt(2)\n\t" \
  "v_mfma_f32_16x16x32_bf16 v[72:75], a[60:63], a[68:71], v[72:75]\n\t" \
  "v_mfma_f32_16x16x32_bf16 v[76:79], a[64:67], a[68:71], v[76:79]\n\t" \
  "v_mfma_f32_16x16x32_bf16 v[72:75], a[72:75], a[80:83], v[72:75]\n\t" \
  "v_mfma_f32_16x16x32_bf16 v[76:79], a[76:79], a[80:83], v[76:79]\n\t" \
  "s_waitcnt lgkmcnt(0)\n\t" \
  "v_mfma_f32_16x16x32_bf16 v[72:75], a[84:87], a[92:95], v[72:75]\n\t" \
  "v_mfma_f32_16x16x32_bf16 v[76:79], a[88:91], a[92:95], v[76:79]\n\t" \
  "ds_read_b64_tr_b16 a[0:1], v226 offset:0\n\t" \
  "ds_read_b64_tr_b16 a[2:3], v226 offset:4096\n\t" \
  "ds_read_b64_tr_b16 a[4:5], v227 offset:0\n\t" \
  "ds_read_b64_tr_b16 a[6:7], v227 offset:4096\n\t" \
  "ds_read_b128 a[8:11], v228 offset:1024\n\t" \
  "ds_read_b64_tr_b16 a[12:13], v226 offset:8192\n\t" \
  "ds_read_b64_tr_b16 a[14:15], v226 offset:12288\n\t" \
  "ds_read_b64_tr_b16 a[16:17], v227 offset:8192\n\t" \
  "ds_read_b64_tr_b16 a[18:19], v227 offset:12288\n\t" \
  "ds_read_b128 a[20:23], v228 offset:1152\n\t" \
  "s_waitcnt lgkmcnt(5)\n\t" \
  "v_mfma_f32_16x16x32_bf16 v[72:75], a[0:3], a[8:11], v[72:75]\n\t" \
  "v_mfma_f32_16x16x32_bf16 v[76:79], a[4:7], a[8:11], v[76:79]\n\t" \
  "s_waitcnt lgkmcnt(0)\n\t" \
  "v_mfma_f32_16x16x32_bf16 v[72:75], a[12:15], a[20:23], v[72:75]\n\t" \
  "v_mfma_f32_16x16x32_bf16 v[76:79], a[16:19], a[20:23], v[76:79]\n\t" \
  "s_mul_i32 s68, s86, 128\n\t" \
  "s_add_u32 s68, s68, 42336256\n\t" \
  "s_mov_b64 s[72:73], %[ws]\n\t" \
  "s_add_u32 s68, s68, s72\n\t" \
  "s_addc_u32 s69, s73, 0\n\t" \
  "s_waitcnt vmcnt(0) lgkmcnt(0)\n\t" \
  "s_barrier\n\t" \
  "s_cmp_lg_u32 %[w], 0\n\t" \
  "s_cbranch_scc1 .Lfd%=\n\t" \
  "s_mov_b64 exec, 0xffffffff\n\t" \
  "v_lshlrev_b32 v200, 2, v203\n\t" \
  "v_mov_b32 v201, %[tag]\n\t" \
  "global_store_dword v200, v201, s[68:69] sc1\n\t" \
  "s_mov_b64 exec, -1\n\t" \
  ".Lfd%=:\n\t" \
  "s_add_u32 m0, s80, 0\n\t" \
  "s_mov_b32 s74, 1105920\n\t" \
  "buffer_load_dwordx4 v216, s[56:59], s74 offen offset:0 lds\n\t" \
  "buffer_load_dwordx4 v217, s[60:63], s74 offen offset:1024 lds\n\t" \
  "buffer_load_dwordx4 v218, s[56:59], s74 offen offset:2048 lds\n\t" \
  "buffer_load_dwordx4 v219, s[60:63], s74 offen offset:3072 lds\n\t" \
  "s_add_u32 m0, s80, 16384\n\t" \
  "s_mov_b32 s74, 1327104\n\t" \
  "buffer_load_dwordx4 v216, s[56:59], s74 offen offset:0 lds\n\t" \
  "buffer_load_dwordx4 v217, s[60:63], s74 offen offset:1024 lds\n\t" \
  "buffer_load_dwordx4 v218, s[56:59], s74 offen offset:2048 lds\n\t" \
  "buffer_load_dwordx4 v219, s[60:63], s74 offen offset:3072 lds\n\t" \
  "s_add_u32 m0, s80, 32768\n\t" \
  "s_mov_b32 s74, 1548288\n\t" \
  "buffer_load_dwordx4 v216, s[56:59], s74 offen offset:0 lds\n\t" \
  "buffer_load_dwordx4 v217, s[60:63], s74 offen offset:1024 lds\n\t" \
  "buffer_load_dwordx4 v218, s[56:59], s74 offen offset:2048 lds\n\t" \
  "buffer_load_dwordx4 v219, s[60:63], s74 offen offset:3072 lds\n\t" \
  "s_add_u32 m0, s80, 49152\n\t" \
  "s_mov_b32 s74, 1769472\n\t" \
  "buffer_load_dwordx4 v216, s[56:59], s74 offen offset:0 lds\n\t" \
  "buffer_load_dwordx4 v217, s[60:63], s74 offen offset:1024 lds\n\t" \
  "buffer_load_dwordx4 v218, s[56:59], s74 offen offset:2048 lds\n\t" \
  "buffer_load_dwordx4 v219, s[60:63], s74 offen offset:3072 lds\n\t" \
  "s_add_u32 m0, s80, 131072\n\t" \
  "s_mov_b32 s74, 1990656\n\t" \
  "buffer_load_dwordx4 v216, s[56:59], s74 offen offset:0 lds\n\t" \
  "buffer_load_dwordx4 v217, s[60:63], s74 offen offset:1024 lds\n\t" \
  "buffer_load_dwordx4 v218, s[56:59], s74 offen offset:2048 lds\n\t" \
  "buffer_load_dwordx4 v219, s[60:63], s74 offen offset:3072 lds\n\t" \
  "s_waitcnt vmcnt(0)\n\t" \
  "s_barrier\n\t" \
  "ds_read_b64_tr_b16 a[0:1], v224 offset:0\n\t" \
  "ds_read_b64_tr_b16 a[2:3], v224 offset:4096\n\t" \
  "ds_read_b64_tr_b16 a[4:5], v225 offset:0\n\t" \
  "ds_read_b64_tr_b16 a[6:7], v225 offset:4096\n\t" \
  "ds_read_b128 a[8:11], v228 offset:1280\n\t" \
  "ds_read_b64_tr_b16 a[12:13], v224 offset:8192\n\t" \
  "ds_read_b64_tr_b16 a[14:15], v224 offset:12288\n\t" \
  "ds_read_b64_tr_b16 a[16:17], v225 offset:8192\n\t" \
  "ds_read_b64_tr_b16 a[18:19], v225 offset:12288\n\t" \
  "ds_read_b128 a[20:23], v228 offset:1408\n\t" \
  "ds_read_b64_tr_b16 a[24:25], v224 offset:16384\n\t" \
  "ds_read_b64_tr_b16 a[26:27], v224 offset:20480\n\t" \
  "ds_read_b64_tr_b16 a[28:29], v225 offset:16384\n\t" \
  "ds_read_b64_tr_b16 a[30:31], v225 offset:20480\n\t" \
  "ds_read_b128 a[32:35], v228 offset:1536\n\t" \
  "ds_read_b64_tr_b16 a[36:37], v224 offset:24576\n\t" \
  "ds_read_b64_tr_b16 a[38:39], v224 offset:28672\n\t" \
  "ds_read_b64_tr_b16 a[40:41], v225 offset:24576\n\t" \
  "ds_read_b64_tr_b16 a[42:43], v225 offset:28672\n\t" \
  "ds_read_b128 a[44:47], v228 offset:1664\n\t" \
  "ds_read_b64_tr_b16 a[48:49], v224 offset:32768\n\t" \
  "ds_read_b64_tr_b16 a[50:51], v224 offset:36864\n\t" \
  "ds_read_b64_tr_b16 a[52:53], v225 offset:32768\n\t" \
  "ds_read_b64_tr_b16 a[54:55], v225 offset:36864\n\t" \
  "ds_read_b128 a[56:59], v228 offset:1792\n\t" \
  "ds_read_b64_tr_b16 a[60:61], v224 offset:40960\n\t" \
  "ds_read_b64_tr_b16 a[62:63], v224 offset:45056\n\t" \
  "ds_read_b64_tr_b16 a[64:65], v225 offset:40960\n\t" \
  "ds_read_b64_tr_b16 a[66:67], v225 offset:45056\n\t" \
  "ds_read_b128 a[68:71], v228 offset:1920\n\t" \
  "ds_read_b64_tr_b16 a[72:73], v224 offset:49152\n\t" \
  "ds_read_b64_tr_b16 a[74:75], v224 offset:53248\n\t" \
  "ds_read_b64_tr_b16 a[76:77], v225 offset:49152\n\t" \
  "ds_read_b64_tr_b16 a[78:79], v225 offset:53248\n\t" \
  "ds_read_b128 a[80:83], v228 offset:2048\n\t" \
  "ds_read_b64_tr_b16 a[84:85], v224 offset:57344\n\t" \
  "ds_read_b64_tr_b16 a[86:87], v224 offset:61440\n\t" \
  "ds_read_b64_tr_b16 a[88:89], v225 offset:57344\n\t" \
  "ds_read_b64_tr_b16 a[90:91], v225 offset:61440\n\t" \
  "ds_read_b128 a[92:95], v228 offset:2176\n\t" \
  "s_waitcnt lgkmcnt(12)\n\t" \
  "v_mfma_f32_16x16x32_bf16 v[72:75], a[0:3], a[8:11], v[72:75]\n\t" \
  "v_mfma_f32_16x16x32_bf16 v[76:79], a[4:7], a[8:11], v[76:79]\n\t" \
  "v_mfma_f32_16x16x32_bf16 v[72:75], a[12:15], a[20:23], v[72:75]\n\t" \
  "v_mfma_f32_16x16x32_bf16 v[76:79], a[16:19], a[20:23], v[76:79]\n\t" \
  "v_mfma_f32_16x16x32_bf16 v[72:75], a[24:27], a[32:35], v[72:75]\n\t" \
  "v_mfma_f32_16x16x32_bf16 v[76:79], a[28:31], a[32:35], v[76:79]\n\t" \
  "v_mfma_f32_16x16x32_bf16 v[72:75], a[36:39], a[44:47], v[72:75]\n\t" \
  "v_mfma_f32_16x16x32_bf16 v[76:79], a[40:43], a[44:47], v[76:79]\n\t" \
  "v_mfma_f32_16x16x32_bf16 v[72:75], a[48:51], a[56:59], v[72:75]\n\t" \
  "v_mfma_f32_16x16x32_bf16 v[76:79], a[52:55], a[56:59], v[76:79]\n\t" \
  "s_waitcnt lgkmcnt(2)\n\t" \
  "v_mfma_f32_16x16x32_bf16 v[72:75], a[60:63], a[68:71], v[72:75]\n\t" \
  "v_mfma_f32_16x16x32_bf16 v[76:79], a[64:67], a[68:71], v[76:79]\n\t" \
  "v_mfma_f32_16x16x32_bf16 v[72:75], a[72:75], a[80:83], v[72:75]\n\t" \
  "v_mfma_f32_16x16x32_bf16 v[76:79], a[76:79], a[80:83], v[76:79]\n\t" \
  "s_waitcnt lgkmcnt(0)\n\t" \
  "v_mfma_f32_16x16x32_bf16 v[72:75], a[84:87], a[92:95], v[72:75]\n\t" \
  "v_mfma_f32_16x16x32_bf16 v[76:79], a[88:91], a[92:95], v[76:79]\n\t" \
  "ds_read_b64_tr_b16 a[0:1], v226 offset:0\n\t" \
  "ds_read_b64_tr_b16 a[2:3], v226 offset:4096\n\t" \
  "ds_read_b64_tr_b16 a[4:5], v227 offset:0\n\t" \
  "ds_read_b64_tr_b16 a[6:7], v227 offset:4096\n\t" \
  "ds_read_b128 a[8:11], v228 offset:2304\n\t" \
  "ds_read_b64_tr_b16 a[12:13], v226 offset:8192\n\t" \
  "ds_read_b64_tr_b16 a[14:15], v226 offset:12288\n\t" \
  "ds_read_b64_tr_b16 a[16:17], v227 offset:8192\n\t" \
  "ds_read_b64_tr_b16 a[18:19], v227 offset:12288\n\t" \
  "ds_read_b128 a[20:23], v228 offset:2432\n\t" \
  "s_waitcnt lgkmcnt(5)\n\t" \
  "v_mfma_f32_16x16x32_bf16 v[72:75], a[0:3], a[8:11], v[72:75]\n\t" \
  "v_mfma_f32_16x16x32_bf16 v[76:79], a[4:7], a[8:11], v[76:79]\n\t" \
  "s_waitcnt lgkmcnt(0)\n\t" \
  "v_mfma_f32_16x16x32_bf16 v[72:75], a[12:15], a[20:23], v[72:75]\n\t" \
  "v_mfma_f32_16x16x32_bf16 v[76:79], a[16:19], a[20:23], v[76:79]\n\t" \
  "s_cmp_lt_u32 %[w], 2\n\t" \
  "s_mov_b32 s84, 0xbf317218\n\t" \
  "s_cselect_b32 s84, 0xbfb8aa3b, s84\n\t" \
  "v_and_b32 v232, 1, v203\n\t" \
  "v_cmp_eq_u32 vcc, 1, v232\n\t" \
  "v_mov_b32 v233, s84\n\t" \
  "s_nop 0\n\t" \
  "v_cndmask_b32 v233, 1.0, v233, vcc\n\t" \
  "v_lshrrev_b32 v234, 4, v203\n\t" \
  "v_lshlrev_b32 v234, 4, v234\n\t" \
  "v_lshl_add_u32 v234, v232, 9, v234\n\t" \
  "s_lshl_b32 s84, %[w], 7\n\t" \
  "s_add_u32 s84, s84, 150528\n\t" \
  "v_add_u32 v234, s84, v234\n\t" \
  "v_add_u32 v235, 150528, v202\n\t" \
  "v_mul_f32 v72, v233, v72\n\t" \
  "v_mul_f32 v73, v233, v73\n\t" \
  "v_mul_f32 v74, v233, v74\n\t" \
  "v_mul_f32 v75, v233, v75\n\t" \
  "v_mul_f32 v76, v233, v76\n\t" \
  "v_mul_f32 v77, v233, v77\n\t" \
  "v_mul_f32 v78, v233, v78\n\t" \
  "v_mul_f32 v79, v233, v79\n\t" \
  "ds_write_b128 v234, v[72:75] offset:0\n\t" \
  "ds_write_b128 v234, v[76:79] offset:64\n\t" \
  "s_waitcnt lgkmcnt(0)\n\t" \
  "s_barrier\n\t" \
  "ds_read_b128 v[72:75], v235\n\t" \
  "s_mov_b32 s40, 42139648\n\t" \
  "s_mov_b64 s[72:73], %[ws]\n\t" \
  "s_add_u32 s40, s40, s72\n\t" \
  "s_addc_u32 s41, s73, 0\n\t" \
  "s_and_b32 s41, s41, 0xffff\n\t" \
  "s_mov_b32 s42, 0x6c00\n\t" \
  "s_mov_b32 s43, 0x20000\n\t" \
  "s_lshl_b32 s84, s86, 10\n\t" \
  "s_cmp_eq_u32 %[w], 0\n\t" \
  "s_cselect_b32 s84, s84, 0x7ff00000\n\t" \
  "s_waitcnt lgkmcnt(0)\n\t" \
  "buffer_store_dwordx4 v[72:75], v202, s[40:43], s84 offen sc1\n\t" \
  "s_mul_i32 s68, s86, 128\n\t" \
  "s_add_u32 s68, s68, 42344448\n\t" \
  "s_mov_b64 s[72:73], %[ws]\n\t" \
  "s_add_u32 s68, s68, s72\n\t" \
  "s_addc_u32 s69, s73, 0\n\t" \
  "s_waitcnt vmcnt(0) lgkmcnt(0)\n\t" \
  "s_barrier\n\t" \
  "s_cmp_lg_u32 %[w], 0\n\t" \
  "s_cbranch_scc1 .Lfu%=\n\t" \
  "s_mov_b64 exec, 0xffffffff\n\t" \
  "v_lshlrev_b32 v200, 2, v203\n\t" \
  "v_mov_b32 v201, %[tag]\n\t" \
  "global_store_dword v200, v201, s[68:69] sc1\n\t" \
  "s_mov_b64 exec, -1\n\t" \
  ".Lfu%=:\n\t" \
  "s_mov_b32 s68, 42336256\n\t" \
  "s_mov_b64 s[72:73], %[ws]\n\t" \
  "s_add_u32 s68, s68, s72\n\t" \
  "s_addc_u32 s69, s73, 0\n\t" \
  "v_min_u32 v200, 39, v203\n\t" \
  "v_lshlrev_b32 v200, 7, v200\n\t" \
  "s_mov_b32 s84, 0\n\t" \
  ".Lpi%=:\n\t" \
  "global_load_dword v232, v200, s[68:69] sc1\n\t" \
  "s_waitcnt vmcnt(0)\n\t" \
  "v_cmp_ne_u32 vcc, %[tag], v232\n\t" \
  "s_cbranch_vccz .Lpid%=\n\t" \
  "s_sleep 1\n\t" \
  "s_add_u32 s84, s84, 1\n\t" \
  "s_cmp_lt_u32 s84, 30000\n\t" \
  "s_cbranch_scc1 .Lpi%=\n\t" \
  "v_mov_b32 v232, 0\n\t" \
  "v_mov_b32 v233, 1\n\t" \
  "global_store_dword v232, v233, s[70:71] sc1\n\t" \
  "s_waitcnt vmcnt(0)\n\t" \
  ".Lpid%=:\n\t" \
  "s_mov_b32 s88, 0\n\t" \
  "buffer_load_dwordx4 a[208:211], v202, s[36:39], s88 offen offset:0 sc1\n\t" \
  "buffer_load_dwordx4 a[212:215], v202, s[36:39], s88 offen offset:1024 sc1\n\t" \
  "buffer_load_dwordx4 a[216:219], v202, s[36:39], s88 offen offset:2048 sc1\n\t" \
  "buffer_load_dwordx4 a[220:223], v202, s[36:39], s88 offen offset:3072 sc1\n\t" \
  "s_mov_b32 s88, 4096\n\t" \
  "buffer_load_dwordx4 a[128:131], v202, s[36:39], s88 offen offset:0 sc1\n\t" \
  "buffer_load_dwordx4 a[132:135], v202, s[36:39], s88 offen offset:1024 sc1\n\t" \
  "buffer_load_dwordx4 a[136:139], v202, s[36:39], s88 offen offset:2048 sc1\n\t" \
  "buffer_load_dwordx4 a[140:143], v202, s[36:39], s88 offen offset:3072 sc1\n\t" \
  "s_mov_b32 s88, 8192\n\t" \
  "buffer_load_dwordx4 a[144:147], v202, s[36:39], s88 offen offset:0 sc1\n\t" \
  "buffer_load_dwordx4 a[148:151], v202, s[36:39], s88 offen offset:1024 sc1\n\t" \
  "buffer_load_dwordx4 a[152:155], v202, s[36:39], s88 offen offset:2048 sc1\n\t" \
  "buffer_load_dwordx4 a[156:159], v202, s[36:39], s88 offen offset:3072 sc1\n\t" \
  "s_mov_b32 s88, 12288\n\t" \
  "buffer_load_dwordx4 a[160:163], v202, s[36:39], s88 offen offset:0 sc1\n\t" \
  "buffer_load_dwordx4 a[164:167], v202, s[36:39], s88 offen offset:1024 sc1\n\t" \
  "buffer_load_dwordx4 a[168:171], v202, s[36:39], s88 offen offset:2048 sc1\n\t" \
  "buffer_load_dwordx4 a[172:175], v202, s[36:39], s88 offen offset:3072 sc1\n\t" \
  "s_mov_b32 s88, 16384\n\t" \
  "buffer_load_dwordx4 a[176:179], v202, s[36:39], s88 offen offset:0 sc1\n\t" \
  "buffer_load_dwordx4 a[180:183], v202, s[36:39], s88 offen offset:1024 sc1\n\t" \
  "buffer_load_dwordx4 a[184:187], v202, s[36:39], s88 offen offset:2048 sc1\n\t" \
  "buffer_load_dwordx4 a[188:191], v202, s[36:39], s88 offen offset:3072 sc1\n\t" \
  "s_mov_b32 s88, 20480\n\t" \
  "buffer_load_dwordx4 a[192:195], v202, s[36:39], s88 offen offset:0 sc1\n\t" \
  "buffer_load_dwordx4 a[196:199], v202, s[36:39], s88 offen offset:1024 sc1\n\t" \
  "buffer_load_dwordx4 a[200:203], v202, s[36:39], s88 offen offset:2048 sc1\n\t" \
  "buffer_load_dwordx4 a[204:207], v202, s[36:39], s88 offen offset:3072 sc1\n\t" \
  "s_mov_b32 s88, 24576\n\t" \
  "buffer_load_dwordx4 a[224:227], v202, s[36:39], s88 offen offset:0 sc1\n\t" \
  "buffer_load_dwordx4 a[228:231], v202, s[36:39], s88 offen offset:1024 sc1\n\t" \
  "buffer_load_dwordx4 a[232:235], v202, s[36:39], s88 offen offset:2048 sc1\n\t" \
  "buffer_load_dwordx4 a[236:239], v202, s[36:39], s88 offen offset:3072 sc1\n\t" \
  "s_mov_b32 s88, 28672\n\t" \
  "buffer_load_dwordx4 a[240:243], v202, s[36:39], s88 offen offset:0 sc1\n\t" \
  "buffer_load_dwordx4 a[244:247], v202, s[36:39], s88 offen offset:1024 sc1\n\t" \
  "buffer_load_dwordx4 a[248:251], v202, s[36:39], s88 offen offset:2048 sc1\n\t" \
  "buffer_load_dwordx4 a[252:255], v202, s[36:39], s88 offen offset:3072 sc1\n\t" \
  "s_mov_b32 s88, 32768\n\t" \
  "buffer_load_dwordx4 a[64:67], v202, s[36:39], s88 offen offset:0 sc1\n\t" \
  "buffer_load_dwordx4 a[68:71], v202, s[36:39], s88 offen offset:1024 sc1\n\t" \
  "buffer_load_dwordx4 a[72:75], v202, s[36:39], s88 offen offset:2048 sc1\n\t" \
  "buffer_load_dwordx4 a[76:79], v202, s[36:39], s88 offen offset:3072 sc1\n\t" \
  "s_mov_b32 s88, 36864\n\t" \
  "buffer_load_dwordx4 a[80:83], v202, s[36:39], s88 offen offset:0 sc1\n\t" \
  "buffer_load_dwordx4 a[84:87], v202, s[36:39], s88 offen offset:1024 sc1\n\t" \
  "buffer_load_dwordx4 a[88:91], v202, s[36:39], s88 offen offset:2048 sc1\n\t" \
  "buffer_load_dwordx4 a[92:95], v202, s[36:39], s88 offen offset:3072 sc1\n\t" \
  "s_waitcnt vmcnt(20)\n\t" \
  "s_mov_b32 s88, 40960\n\t" \
  "buffer_load_dwordx4 a[96:99], v202, s[36:39], s88 offen offset:0 sc1\n\t" \
  "buffer_load_dwordx4 a[100:103], v202, s[36:39], s88 offen offset:1024 sc1\n\t" \
  "buffer_load_dwordx4 a[0:3], v202, s[36:39], s88 offen offset:2048 sc1\n\t" \
  "buffer_load_dwordx4 a[4:7], v202, s[36:39], s88 offen offset:3072 sc1\n\t" \
  "s_mov_b32 s88, 45056\n\t" \
  "buffer_load_dwordx4 a[8:11], v202, s[36:39], s88 offen offset:0 sc1\n\t" \
  "buffer_load_dwordx4 a[12:15], v202, s[36:39], s88 offen offset:1024 sc1\n\t" \
  "buffer_load_dwordx4 a[16:19], v202, s[36:39], s88 offen offset:2048 sc1\n\t" \
  "buffer_load_dwordx4 a[20:23], v202, s[36:39], s88 offen offset:3072 sc1\n\t" \
  "s_mov_b32 s88, 49152\n\t" \
  "buffer_load_dwordx4 a[24:27], v202, s[36:39], s88 offen offset:0 sc1\n\t" \
  "buffer_load_dwordx4 a[28:31], v202, s[36:39], s88 offen offset:1024 sc1\n\t" \
  "buffer_load_dwordx4 a[32:35], v202, s[36:39], s88 offen offset:2048 sc1\n\t" \
  "buffer_load_dwordx4 a[36:39], v202, s[36:39], s88 offen offset:3072 sc1\n\t" \
  "s_mov_b32 s88, 53248\n\t" \
  "buffer_load_dwordx4 a[40:43], v202, s[36:39], s88 offen offset:0 sc1\n\t" \
  "buffer_load_dwordx4 a[44:47], v202, s[36:39], s88 offen offset:1024 sc1\n\t" \
  "buffer_load_dwordx4 a[48:51], v202, s[36:39], s88 offen offset:2048 sc1\n\t" \
  "buffer_load_dwordx4 a[52:55], v202, s[36:39], s88 offen offset:3072 sc1\n\t" \
  "s_mov_b32 s88, 57344\n\t" \
  "buffer_load_dwordx4 a[56:59], v202, s[36:39], s88 offen offset:0 sc1\n\t" \
  "buffer_load_dwordx4 a[60:63], v202, s[36:39], s88 offen offset:1024 sc1\n\t" \
  "buffer_load_dwordx4 a[104:107], v202, s[36:39], s88 offen offset:2048 sc1\n\t" \
  "buffer_load_dwordx4 a[108:111], v202, s[36:39], s88 offen offset:3072 sc1\n\t" \
  "s_mov_b32 s88, 61440\n\t" \
  "buffer_load_dwordx4 a[112:115], v202, s[36:39], s88 offen offset:0 sc1\n\t" \
  "buffer_load_dwordx4 a[116:119], v202, s[36:39], s88 offen offset:1024 sc1\n\t" \
  "buffer_load_dwordx4 a[120:123], v202, s[36:39], s88 offen offset:2048 sc1\n\t" \
  "buffer_load_dwordx4 a[124:127], v202, s[36:39], s88 offen offset:3072 sc1\n\t" \
  "s_waitcnt vmcnt(0)\n\t" \
  "s_branch .Ldjoin%=\n\t" \
  ".Labad%=:\n\t" \
  "buffer_store_dwordx4 v[136:139], v202, s[48:51], 0 offen offset:0 sc1\n\t" \
  "buffer_store_dwordx4 v[140:143], v202, s[48:51], s85 offen offset:0 sc1\n\t" \
  "buffer_store_dwordx4 v[144:147], v202, s[48:51], s86 offen offset:0 sc1\n\t" \
  "buffer_store_dwordx4 v[148:151], v202, s[48:51], s87 offen offset:0 sc1\n\t" \
  "buffer_store_dwordx4 v[152:155], v202, s[48:51], 0 offen offset:1024 sc1\n\t" \
  "buffer_store_dwordx4 v[156:159], v202, s[48:51], s85 offen offset:1024 sc1\n\t" \
  "buffer_store_dwordx4 v[160:163], v202, s[48:51], s86 offen offset:1024 sc1\n\t" \
  "buffer_store_dwordx4 v[164:167], v202, s[48:51], s87 offen offset:1024 sc1\n\t" \
  "buffer_store_dwordx4 v[168:171], v202, s[48:51], 0 offen offset:2048 sc1\n\t" \
  "buffer_store_dwordx4 v[172:175], v202, s[48:51], s85 offen offset:2048 sc1\n\t" \
  "buffer_store_dwordx4 v[176:179], v202, s[48:51], s86 offen offset:2048 sc1\n\t" \
  "buffer_store_dwordx4 v[180:183], v202, s[48:51], s87 offen offset:2048 sc1\n\t" \
  "buffer_store_dwordx4 v[184:187], v202, s[48:51], 0 offen offset:3072 sc1\n\t" \
  "buffer_store_dwordx4 v[188:191], v202, s[48:51], s85 offen offset:3072 sc1\n\t" \
  "buffer_store_dwordx4 v[192:195], v202, s[48:51], s86 offen offset:3072 sc1\n\t" \
  "buffer_store_dwordx4 v[196:199], v202, s[48:51], s87 offen offset:3072 sc1\n\t" \
  "s_waitcnt vmcnt(0)\n\t" \
  "s_mov_b32 %[bad], 1\n\t" \
  "s_branch .Lend%=\n\t" \
  ".Lbcold%=:\n\t" \
  "s_and_b32 s86, %[mt], 1\n\t" \
  "s_mul_i32 s36, %[nt], 655360\n\t" \
  "s_mul_i32 s72, s86, 327680\n\t" \
  "s_add_u32 s36, s36, s72\n\t" \
  "s_mul_i32 s72, %[w], 81920\n\t" \
  "s_add_u32 s36, s36, s72\n\t" \
  "s_add_u32 s36, s36, 29409280\n\t" \
  "s_mov_b64 s[72:73], %[ws]\n\t" \
  "s_add_u32 s36, s36, s72\n\t" \
  "s_addc_u32 s37, s73, 0\n\t" \
  "s_and_b32 s37, s37, 0xffff\n\t" \
  "s_mov_b32 s38, 0x14000\n\t" \
  "s_mov_b32 s39, 0x20000\n\t" \
  "s_mov_b32 s48, 5554176\n\t" \
  "s_mov_b64 s[72:73], %[ws]\n\t" \
  "s_add_u32 s48, s48, s72\n\t" \
  "s_addc_u32 s49, s73, 0\n\t" \
  "s_and_b32 s49, s49, 0xffff\n\t" \
  "s_mov_b32 s50, 0x4000\n\t" \
  "s_mov_b32 s51, 0x20000\n\t" \
  "s_waitcnt vmcnt(0)\n\t" \
  "s_lshl_b32 s86, %[w], 12\n\t" \
  "v_add_u32 v254, s86, v202\n\t" \
  "v_add_u32 v254, 0x8000, v254\n\t" \
  "s_mov_b32 s88, 0\n\t" \
  "buffer_load_dwordx4 a[192:195], v202, s[36:39], s88 offen offset:0 sc1\n\t" \
  "buffer_load_dwordx4 a[196:199], v202, s[36:39], s88 offen offset:1024 sc1\n\t" \
  "buffer_load_dwordx4 a[200:203], v202, s[36:39], s88 offen offset:2048 sc1\n\t" \
  "buffer_load_dwordx4 a[204:207], v202, s[36:39], s88 offen offset:3072 sc1\n\t" \
  "s_mov_b32 s88, 4096\n\t" \
  "buffer_load_dwordx4 a[208:211], v202, s[36:39], s88 offen offset:0 sc1\n\t" \
  "buffer_load_dwordx4 a[212:215], v202, s[36:39], s88 offen offset:1024 sc1\n\t" \
  "buffer_load_dwordx4 a[216:219], v202, s[36:39], s88 offen offset:2048 sc1\n\t" \
  "buffer_load_dwordx4 a[220:223], v202, s[36:39], s88 offen offset:3072 sc1\n\t" \
  "s_mov_b32 s88, 8192\n\t" \
  "buffer_load_dwordx4 a[224:227], v202, s[36:39], s88 offen offset:0 sc1\n\t" \
  "buffer_load_dwordx4 a[228:231], v202, s[36:39], s88 offen offset:1024 sc1\n\t" \
  "buffer_load_dwordx4 a[232:235], v202, s[36:39], s88 offen offset:2048 sc1\n\t" \
  "buffer_load_dwordx4 a[236:239], v202, s[36:39], s88 offen offset:3072 sc1\n\t" \
  "s_mov_b32 s88, 12288\n\t" \
  "buffer_load_dwordx4 a[240:243], v202, s[36:39], s88 offen offset:0 sc1\n\t" \
  "buffer_load_dwordx4 a[244:247], v202, s[36:39], s88 offen offset:1024 sc1\n\t" \
  "buffer_load_dwordx4 a[248:251], v202, s[36:39], s88 offen offset:2048 sc1\n\t" \
  "buffer_load_dwordx4 a[252:255], v202, s[36:39], s88 offen offset:3072 sc1\n\t" \
  "s_branch .Lbjoin%=\n\t" \
  ".Lend%=:\n\t" \
  ".Lblock_end:\n\t" \
  "s_nop 1\n\t"
#define MAIN_CLOB "v8", "v9", "v10", "v11", "v12", "v13", "v14", "v15", "v16", "v17", "v18", "v19", "v20", "v21", "v22", "v23", "v24", "v25", "v26", "v27", "v28", "v29", "v30", "v31", "v32", "v33", "v34", "v35", "v36", "v37", "v38", "v39", "v40", "v41", "v42", "v43", "v44", "v45", "v46", "v47", "v48", "v49", "v50", "v51", "v52", "v53", "v54", "v55", "v56", "v57", "v58", "v59", "v60", "v61", "v62", "v63", "v64", "v65", "v66", "v67", "v68", "v69", "v70", "v71", "v72", "v73", "v74", "v75", "v76", "v77", "v78", "v79", "v80", "v81", "v82", "v83", "v84", "v85", "v86", "v87", "v88", "v89", "v90", "v91", "v92", "v93", "v94", "v95", "v96", "v97", "v98", "v99", "v100", "v101", "v102", "v103", "v104", "v105", "v106", "v107", "v108", "v109", "v110", "v111", "v112", "v113", "v114", "v115", "v116", "v117", "v118", "v119", "v120", "v121", "v122", "v123", "v124", "v125", "v126", "v127", "v128", "v129", "v130", "v131", "v132", "v133", "v134", "v135", "v136", "v137", "v138", "v139", "v140", "v141", "v142", "v143", "v144", "v145", "v146", "v147", "v148", "v149", "v150", "v151", "v152", "v153", "v154", "v155", "v156", "v157", "v158", "v159", "v160", "v161", "v162", "v163", "v164", "v165", "v166", "v167", "v168", "v169", "v170", "v171", "v172", "v173", "v174", "v175", "v176", "v177", "v178", "v179", "v180", "v181", "v182", "v183", "v184", "v185", "v186", "v187", "v188", "v189", "v190", "v191", "v192", "v193", "v194", "v195", "v196", "v197", "v198", "v199", "v200", "v201", "v202", "v203", "v204", "v205", "v206", "v207", "v208", "v209", "v210", "v211", "v212", "v213", "v214", "v215", "v216", "v217", "v218", "v219", "v220", "v221", "v222", "v223", "v224", "v225", "v226", "v227", "v228", "v229", "v230", "v231", "v232", "v233", "v234", "v235", "v236", "v237", "v238", "v239", "v240", "v241", "v242", "v243", "v244", "v245", "v246", "v247", "v248", "v249", "v250", "v251", "v252", "v253", "v254", "v255", "a0", "a1", "a2", "a3", "a4", "a5", "a6", "a7", "a8", "a9", "a10", "a11", "a12", "a13", "a14", "a15", "a16", "a17", "a18", "a19", "a20", "a21", "a22", "a23", "a24", "a25", "a26", "a27", "a28", "a29", "a30", "a31", "a32", "a33", "a34", "a35", "a36", "a37", "a38", "a39", "a40", "a41", "a42", "a43", "a44", "a45", "a46", "a47", "a48", "a49", "a50", "a51", "a52", "a53", "a54", "a55", "a56", "a57", "a58", "a59", "a60", "a61", "a62", "a63", "a64", "a65", "a66", "a67", "a68", "a69", "a70", "a71", "a72", "a73", "a74", "a75", "a76", "a77", "a78", "a79", "a80", "a81", "a82", "a83", "a84", "a85", "a86", "a87", "a88", "a89", "a90", "a91", "a92", "a93", "a94", "a95", "a96", "a97", "a98", "a99", "a100", "a101", "a102", "a103", "a104", "a105", "a106", "a107", "a108", "a109", "a110", "a111", "a112", "a113", "a114", "a115", "a116", "a117", "a118", "a119", "a120", "a121", "a122", "a123", "a124", "a125", "a126", "a127", "a128", "a129", "a130", "a131", "a132", "a133", "a134", "a135", "a136", "a137", "a138", "a139", "a140", "a141", "a142", "a143", "a144", "a145", "a146", "a147", "a148", "a149", "a150", "a151", "a152", "a153", "a154", "a155", "a156", "a157", "a158", "a159", "a160", "a161", "a162", "a163", "a164", "a165", "a166", "a167", "a168", "a169", "a170", "a171", "a172", "a173", "a174", "a175", "a176", "a177", "a178", "a179", "a180", "a181", "a182", "a183", "a184", "a185", "a186", "a187", "a188", "a189", "a190", "a191", "a192", "a193", "a194", "a195", "a196", "a197", "a198", "a199", "a200", "a201", "a202", "a203", "a204", "a205", "a206", "a207", "a208", "a209", "a210", "a211", "a212", "a213", "a214", "a215", "a216", "a217", "a218", "a219", "a220", "a221", "a222", "a223", "a224", "a225", "a226", "a227", "a228", "a229", "a230", "a231", "a232", "a233", "a234", "a235", "a236", "a237", "a238", "a239", "a240", "a241", "a242", "a243", "a244", "a245", "a246", "a247", "a248", "a249", "a250", "a251", "a252", "a253", "a254", "a255", "vcc", "scc", "memory", "s36", "s37", "s38", "s39", "s40", "s41", "s42", "s43", "s44", "s45", "s46", "s47", "s48", "s49", "s50", "s51", "s52", "s53", "s54", "s55", "s56", "s57", "s58", "s59", "s60", "s61", "s62", "s63", "s64", "s65", "s66", "s67", "s68", "s69", "s70", "s71", "s72", "s73", "s74", "s75", "s76", "s77", "s78", "s79", "s80", "s81", "s82", "s83", "s84", "s85", "s86", "s87", "s88", "s89", "s90", "s91", "s92"
__global__ __launch_bounds__(256, 1) void block_kernel(const u16* __restrict__ x, const u16* __restrict__ kc, const u16* __restrict__ vc, const u16* __restrict__ wq,
    const u16* __restrict__ wo, const u16* __restrict__ wg, const u16* __restrict__ wu, const u16* __restrict__ wd, const u16* __restrict__ cnd, const u16* __restrict__ wmod,
    char* __restrict__ ws, u16* __restrict__ out) {
  extern __shared__ __attribute__((aligned(16))) char smem_[];
  LAS char* const lds = (LAS char*)smem_;
  const int bid = blockIdx.x, xcd = bid & 7, jj = bid >> 3;
  const int w = __builtin_amdgcn_readfirstlane(threadIdx.x >> 6);
  const rsrc_t rw = __builtin_amdgcn_make_buffer_rsrc(ws, 0, WS_END, 0x00020000);
  char* const tmo = ws + WS_TMO;
  const int head = (xcd & 3) * 8 + (jj >> 2), qblk = (xcd >> 2) * 4 + (jj & 3);
  const int mt = (xcd >> 1) * 8 + (jj >> 3) * 2 + (xcd & 1), nt = jj & 7;
  const int fh = xcd & 1, pc = (xcd >> 1) * 2 + (jj & 1), fi = jj >> 1;
  const int ft = fi < 14 - fh ? fh * 14 + fi : 27 + pc * 5 + (fh ? fi - 11 : fi - 14);
  u32 tag;
  {
    const int tid = threadIdx.x, lane = tid & 63;
    const int b4 = min(bid, 239), cg = b4 >> 2, kq = b4 & 3;
    const int row = bid * 8 + (lane & 7), ch = (lane >> 3) + (w & 1) * 40;
    u32 ep, cval;
    asm volatile("global_load_dword %0, %1, %2 sc1" : "=v"(ep) : "v"(0), "s"(ws + WS_EPOCH) : "memory");
    asm volatile("global_load_ushort %0, %1, %2" : "=v"(cval) : "v"(min(tid, 159) * 2), "s"(cnd + kq * 160) : "memory");
    const bool crit = bid < 80;
    typedef short v4s __attribute__((ext_vector_type(4)));
    auto x_dma = [&]() {
#pragma unroll
      for (int i = 0; i < 10; ++i) glds16(x + bid * 8 * DM + (i * 64 + lane) * 8, L_XR + w * 10240 + i * 1024); };
    if (crit) {
#pragma unroll
      for (int i = 0; i < 5; ++i) { const int pc8 = w * 5 + i;
        glds16(wmod + (size_t)(kq * 160 + pc8 * 8 + ((lane >> 2) & 7)) * 3840 + cg * 64 + (lane >> 5) * 32 + (((lane & 3) ^ ((pc8 & 1) * 2)) * 8), L_GV + pc8 * 1024); }
    }
    if (w < 2) x_dma();
    asm volatile("s_waitcnt vmcnt(15)\n\ts_cmp_eq_u32 %2, 3\n\ts_cbranch_scc1 1f\n\ts_waitcnt vmcnt(10)\n\ts_cmp_eq_u32 %2, 2\n\ts_cbranch_scc1 1f\n\ts_waitcnt vmcnt(5)\n\ts_cmp_eq_u32 %2, 1\n\ts_cbranch_scc1 1f\n\ts_waitcnt vmcnt(0)\n1:"
                 : "+v"(ep), "+v"(cval) : "s"(__builtin_amdgcn_readfirstlane((crit ? 1 : 0) + (w < 2 ? 2 : 0))) : "scc", "memory");
    if (!crit && w >= 2) {
      __builtin_amdgcn_s_sleep(10);
      u64 pc; int nb; asm volatile("s_getpc_b64 %0\n.Lblock_pc:\n\ts_mov_b32 %1, .Lblock_end-.Lblock_pc" : "=s"(pc), "=s"(nb));
      touch((const char*)pc + min((((jj - 10) * 2 + w - 2) * 13 + min(lane, 12)) * 128, nb & ~3));
#pragma unroll
      for (int i = 0; i < 2; ++i) {
        const int ln = min((i * 44 + (jj - 10) * 2 + w - 2) * 64 + lane, 10239);
        touch((const char*)wq + (size_t)(ln >> 4) * 8192 + (xcd & 3) * 2048 + (ln & 15) * 128);
      }
#pragma unroll
      for (int i = 0; i < 10; ++i)
        glds16(wmod + (size_t)(kq * 160 + i * 16 + (lane >> 2)) * 3840 + cg * 64 + (w - 2) * 32 + (((lane & 3) ^ ((lane >> 4) & 2)) * 8), L_GV + (w - 2) * 10240 + i * 1024);
    }
    tag = __builtin_amdgcn_readfirstlane(ep) + 1u;
    *(LAS u16*)(lds + L_SC + min(tid, 159) * 2) = (u16)(pk_bf16(silu_f(bf_lo(cval)), 0.f));
    if (crit) {
      if (w < 2) asm volatile("s_waitcnt vmcnt(10)" ::: "memory"); else drain();
      lds_barrier();

      const int g = lane >> 4;
      LAS char* ta = lds + L_GV + g * 1024 + (w >> 1) * 512 + ((lane >> 2) & 3) * 64 + (((w ^ g) & 1) * 32) + (lane & 3) * 8;
      f32x4 acc = {0.f, 0.f, 0.f, 0.f};
#pragma unroll
      for (int s = 0; s < 5; ++s) {
        const u32x2 lo = __builtin_bit_cast(u32x2, __builtin_amdgcn_ds_read_tr16_b64_v4i16((LAS v4s*)(ta + s * 4096))), hi = __builtin_bit_cast(u32x2, __builtin_amdgcn_ds_read_tr16_b64_v4i16((LAS v4s*)(ta + s * 4096 + 256)));
        acc = mfma16(u32x4{lo[0], lo[1], hi[0], hi[1]}, *(LAS u32x4*)(lds + L_SC + s * 64 + g * 16), acc);
      }
      const int e = lane & 3; const float m01 = (e & 1) ? acc[1] : acc[0], m23 = (e & 1) ? acc[3] : acc[2], mine = (e & 2) ? m23 : m01;
      if ((lane & 12) == 0) __hip_atomic_store((GAS u64*)(ws + WS_GR) + kq * 1280 + cg * 64 + w * 16 + g * 4 + e, ((u64)tag << 32) | __float_as_uint(mine), RLX_AGENT);
      if (w < 2) asm volatile("s_waitcnt vmcnt(1)" ::: "memory"); else __builtin_amdgcn_s_sleep(10);
    } else if (w < 2) drain(); else __builtin_amdgcn_s_sleep(16);
    float f[5][8], mu = 0.f; int ho = 0;
    if (w < 2) {
      float s = 0.f, fo[5][8];
#pragma unroll
      for (int i = 0; i < 5; ++i) {
        const u32x4 xv = *(LAS u32x4*)(lds + L_XR + w * 10240 + (lane & 7) * 1280 + (ch + 8 * i) * 16), xo = *(LAS u32x4*)(lds + L_XR + w * 10240 + (lane & 7) * 1280 + ((lane >> 3) + (w ^ 1) * 40 + 8 * i) * 16);
#pragma unroll
        for (int j = 0; j < 4; ++j) { f[i][2 * j] = bf_lo(xv[j]); f[i][2 * j + 1] = bf_hi(xv[j]); fo[i][2 * j] = bf_lo(xo[j]); fo[i][2 * j + 1] = bf_hi(xo[j]);
          s += (f[i][2 * j] + fo[i][2 * j]) + (f[i][2 * j + 1] + fo[i][2 * j + 1]); } }
      const float mean = sum32(sum16(sum8(s))) * (1.f / DM);
      float q = 0.f;
#pragma unroll
      for (int i = 0; i < 5; ++i)
#pragma unroll
        for (int j = 0; j < 8; ++j) { f[i][j] -= mean; fo[i][j] -= mean; q += f[i][j] * f[i][j] + fo[i][j] * fo[i][j]; }
      const float rstd = __builtin_amdgcn_rsqf(sum32(sum16(sum8(q))) * (1.f / DM) + 1e-5f);
#pragma unroll
      for (int i = 0; i < 5; ++i)
#pragma unroll
        for (int j = 0; j < 8; ++j) f[i][j] *= rstd;
      mu = mean;
      if (!crit) __builtin_amdgcn_s_sleep(4);
      ho = WS_H + (((row >> 6) * 20 + (ch >> 2)) * 4 + ((row >> 4) & 3)) * 1024 + ((ch & 3) * 16 + (row & 15)) * 16;
    } else {
#pragma unroll
      for (int i = 0; i < 5; ++i)
#pragma unroll
        for (int j = 0; j < 8; ++j) f[i][j] = 0.f;
    }

    auto sweep = [&](auto N, const int (&pr)[3], float (&v)[3][2]) {
      u32x4 gv[3][4];
      for (u32 spin = 0;; ++spin) {
        bool ok = true;
        asm volatile("" ::: "memory");
#pragma unroll
        for (int i = 0; i < N; ++i)
#pragma unroll
          for (int qq = 0; qq < 4; ++qq) gv[i][qq] = ld16(rw, WS_GR + qq * 10240 + pr[i] * 16);
#pragma unroll
        for (int i = 0; i < N; ++i)
#pragma unroll
          for (int qq = 0; qq < 4; ++qq) ok &= gv[i][qq][1] == tag && gv[i][qq][3] == tag;
        if (__all(ok)) break;
        if (spin > SPINCAP) { __hip_atomic_store((GAS u32*)tmo, 1u, RLX_AGENT); break; }
        __builtin_amdgcn_s_sleep(4);
      }
#pragma unroll
      for (int i = 0; i < N; ++i)
#pragma unroll
        for (int e = 0; e < 2; ++e) v[i][e] = rbf((__uint_as_float(gv[i][0][2 * e]) + __uint_as_float(gv[i][1][2 * e])) + (__uint_as_float(gv[i][2][2 * e]) + __uint_as_float(gv[i][3][2 * e])));
    };
    {
      const int pr[3] = {tid, tid + 256, w < 2 ? tid : tid + 384}; float v[3][2];
      sweep(std::integral_constant<int, 3>{}, pr, v);
#pragma unroll
      for (int i = 0; i < 3; ++i) { const bool sc = pr[i] >= 320;
        *(LAS f32x2*)(lds + L_TS + pr[i] * 8) = f32x2{(sc ? rbf(1.f + v[i][0]) : v[i][0]) * QSCALE, (sc ? rbf(1.f + v[i][1]) : v[i][1]) * QSCALE}; }
    }
    lds_barrier();
    if (w < 2) {
#pragma unroll
      for (int i = 0; i < 5; ++i) { const int c = ch + 8 * i; f32x4 hv[2];
#pragma unroll
        for (int a = 0; a < 2; ++a) { const f32x4 sh = *(LAS f32x4*)(lds + L_TS + (c * 8 + 4 * a) * 4), sc = *(LAS f32x4*)(lds + L_TS + (DM + c * 8 + 4 * a) * 4);
#pragma unroll
          for (int e = 0; e < 4; ++e) hv[a][e] = f[i][4 * a + e] * sc[e] + sh[e]; }
        st16(rw, ho + i * 8192, pack8(hv[0], hv[1])); }
      if (tid < 32) st4(rw, WS_MU + bid * 128 + lane * 4, __float_as_uint(mu));
      drain();
    } else {
      if (!crit) {
        const int g = lane >> 4;
        LAS char* ta = lds + L_GV + (w - 2) * 10240 + g * 512 + ((lane >> 2) & 3) * 64 + (lane & 3) * 8;
        f32x4 acc[2] = {};
#pragma unroll
        for (int s = 0; s < 5; ++s) { const u32x4 sc = *(LAS u32x4*)(lds + L_SC + s * 64 + g * 16);
#pragma unroll
          for (int c = 0; c < 2; ++c) { LAS char* t = ta + s * 2048 + ((c ^ g) & 1) * 32;
            const u32x2 lo = __builtin_bit_cast(u32x2, __builtin_amdgcn_ds_read_tr16_b64_v4i16((LAS v4s*)t)), hi = __builtin_bit_cast(u32x2, __builtin_amdgcn_ds_read_tr16_b64_v4i16((LAS v4s*)(t + 256)));
            acc[c] = mfma16(u32x4{lo[0], lo[1], hi[0], hi[1]}, sc, acc[c]); } }
        const f32x4 mine = (lane & 1) ? acc[1] : acc[0];
        if ((lane & 14) == 0) st16(rw, WS_MODF + (kq * 2560 + cg * 64 - 1280 + (w - 2) * 32 + (lane & 1) * 16 + g * 4) * 4, __builtin_bit_cast(u32x4, mine));
      }
      asm volatile(EARLY_ASM :: [wq] "s"(wq), [head] "s"(head), [w] "s"((w - 2) * 2) : EARLY_CLOB);
    }
    lds_barrier();
    if (tid < 32) __hip_atomic_store((GAS u32*)(ws + WS_FH + bid * 128) + tid, tag, RLX_AGENT);
  }
  {
    int entry = 0, bad;
#pragma unroll 1
    for (;;) {
      asm volatile(MAIN_ASM : [bad] "=&s"(bad)
        : [ws] "s"(ws), [wq] "s"(wq), [kc] "s"(kc), [vc] "s"(vc), [wo] "s"(wo), [wd] "s"(wd), [wg] "s"(wg), [wu] "s"(wu), [out] "s"(out), [x] "s"(x),
          [w] "s"(w), [tag] "s"(tag), [bid] "s"(bid), [head] "s"(head), [qblk] "s"(qblk), [mt] "s"(mt), [nt] "s"(nt), [pc] "s"(pc), [ft] "s"(ft), [entry] "s"(entry) : MAIN_CLOB);
      if (!bad) break;
      attn_safe(kc + ((size_t)head * SK + (SK - WIN)) * DH, vc + ((size_t)head * SK + (SK - WIN)) * DH, __builtin_amdgcn_make_buffer_rsrc(ws, 0, WS_END, 0x00020000),
                WS_OF + ((qblk * 4 + w) * 128 + head * 4) * 4096, lane_id());
      entry = 1;
    }
    if (bid == 0 && w == 0 && lane_id() == 0) __hip_atomic_store((GAS u32*)(ws + WS_EPOCH), tag, RLX_AGENT);
  }
}
extern "C" void kernel_launch(void* const* d_in, const int* in_sizes, int n_in, void* d_out, int out_size, void* d_ws, size_t ws_size, hipStream_t stream) {
  (void)hipFuncSetAttribute((const void*)block_kernel, hipFuncAttributeMaxDynamicSharedMemorySize, L_END);
  block_kernel<<<256, 256, L_END, stream>>>((const u16*)d_in[0], (const u16*)d_in[1], (const u16*)d_in[2], (const u16*)d_in[3], (const u16*)d_in[4],
      (const u16*)d_in[6], (const u16*)d_in[7], (const u16*)d_in[8], (const u16*)d_in[9], (const u16*)d_in[10], (char*)d_ws, (u16*)d_out);
}
```
